```python
import torch

def _softmax(x, axis=-1):
    return torch.softmax(x, dim=axis)
def _gelu(x):
    return torch.nn.functional.gelu(x, approximate="tanh")
def _silu(x):
    return torch.nn.functional.silu(x)
def _sigmoid(x):
    return torch.sigmoid(x)
def _layernorm(x, axis=-1, eps=1e-5):
    m = x.mean(dim=axis, keepdim=True)
    v = x.var(dim=axis, keepdim=True, unbiased=False)
    return (x - m) / torch.sqrt(v + eps)
def _scatter_add(tgt, idx, src, axis=0):
    return tgt.clone().index_add_(axis, idx, src)
def _segment_sum(src, seg, n):
    out = torch.zeros((n,) + src.shape[1:], dtype=src.dtype)
    return out.index_add_(0, seg, src)
def _pad(x, pw):
    flat = tuple(v for pair in reversed(pw) for v in pair)
    return torch.nn.functional.pad(x, flat)

kernel_name = 'gated_mix__ssm_conv_s4__swiglu_ffn__prompt'

def setup_inputs():

    _w = (lambda a: (lambda t: t.to(torch.bfloat16) if t.is_floating_point() else t)(torch.as_tensor(a)))
    def _A_setup():
        B, L, D, N = 1, 2048, 448, 256
        A_re = -(lambda _x: torch.exp(torch.as_tensor(_x)))((lambda *s: torch.randn(*s, dtype=torch.bfloat16) * 3.2)(D, N))
        A_im = torch.pi * (lambda *s: torch.randn(*s, dtype=torch.bfloat16) * 0.05)(D, N)
        B_re = (lambda *s: torch.randn(*s, dtype=torch.bfloat16) * 0.05)(D, N)
        B_im = (lambda *s: torch.randn(*s, dtype=torch.bfloat16) * 0.05)(D, N)
        C_re = (lambda *s: torch.randn(*s, dtype=torch.bfloat16) * 0.1)(D, N)
        C_im = (lambda *s: torch.randn(*s, dtype=torch.bfloat16) * 0.1)(D, N)
        log_dt = (lambda *s: torch.randn(*s, dtype=torch.bfloat16) * 0.05)(D) * 0.5 - 4.0
        Dskip = (lambda *s: torch.randn(*s, dtype=torch.bfloat16) * 0.05)(D)
        return {
            "u": _w((lambda *s: torch.randn(*s, dtype=torch.bfloat16) * 0.05)(B, L, D)),
            "A_re": _w(A_re),
            "A_im": _w(A_im),
            "B_re": _w(B_re),
            "B_im": _w(B_im),
            "C_re": _w(C_re),
            "C_im": _w(C_im),
            "log_dt": _w(log_dt),
            "Dskip": _w(Dskip),
        }
    _A_in = _A_setup()

    def _B_setup():
        B, S, D, F = 1, 2048, 448, 1792
        return {
            "x": _w((lambda *s: torch.randn(*s, dtype=torch.bfloat16) * 0.05)(B, S, D)),
            "w_gate": _w((lambda *s: torch.randn(*s, dtype=torch.bfloat16) * 0.05)(D, F)),
            "w_up": _w((lambda *s: torch.randn(*s, dtype=torch.bfloat16) * 0.05)(D, F)),
            "w_down": _w((lambda *s: torch.randn(*s, dtype=torch.bfloat16) * 0.05)(F, D)),
        }
    _B_in = _B_setup()

    return {
        "A_u": _A_in["u"],
        "A_A_re": _A_in["A_re"],
        "A_A_im": _A_in["A_im"],
        "A_B_re": _A_in["B_re"],
        "A_B_im": _A_in["B_im"],
        "A_C_re": _A_in["C_re"],
        "A_C_im": _A_in["C_im"],
        "A_log_dt": _A_in["log_dt"],
        "A_Dskip": _A_in["Dskip"],
        "B_x": _B_in["x"],
        "B_w_gate": _B_in["w_gate"],
        "B_w_up": _B_in["w_up"],
        "B_w_down": _B_in["w_down"],
        "gate_w": _w((lambda *s: torch.randn(*s, dtype=torch.bfloat16) * 0.05)(448) * 16.0),
    }

def reference(A_u, A_A_re, A_A_im, A_B_re, A_B_im, A_C_re, A_C_im, A_log_dt, A_Dskip, B_x, B_w_gate, B_w_up, B_w_down, gate_w):
    def _A_ref(u, A_re, A_im, B_re, B_im, C_re, C_im, log_dt, Dskip):
        Bsz, L, D = u.shape
        dt = torch.exp(log_dt)[:, None]
        A = A_re + 1j * A_im
        Bc = B_re + 1j * B_im
        Cc = C_re + 1j * C_im
        logdA = dt * A
        dB = dt * Bc
        l = (lambda x, dt: x.to(dt))(torch.arange(L), A_re.dtype)
        P = torch.exp(logdA[:, :, None] * l[None, None, :])
        K = torch.einsum("dn,dnl,dn->dl", Cc, P, dB).real
        L2 = 2 * L
        Kp = _pad(K, ((0, 0), (0, L)))
        up = (lambda x, *d: x.permute(*d))(u, 0, 2, 1)
        up = _pad(up, ((0, 0), (0, 0), (0, L)))
        Kf = (lambda x, axes=(-2,-1): torch.fft.rfft2(x.float(), dim=axes))(Kp[:, None, :], axes=(-2, -1))
        Uf = (lambda x, axes=(-2,-1): torch.fft.rfft2(x.float(), dim=axes))(up[:, :, None, :], axes=(-2, -1))
        Yf = Uf * Kf[None, :, :, :]
        y = (lambda x, s, axes=(-2,-1): torch.fft.irfft2(x, s=s, dim=axes).to(torch.bfloat16))(Yf, (1, L2), axes=(-2, -1))[:, :, 0, :L]
        y = (lambda x, *d: x.permute(*d))(y, 0, 2, 1) + u * Dskip[None, None, :]
        return y

    def _B_ref(x, w_gate, w_up, w_down):
        g = _silu(x @ w_gate)
        u = x @ w_up
        return (g * u) @ w_down

    x = A_u
    g = _sigmoid((x * gate_w).sum(-1, keepdims=True))
    a = _A_ref(x, A_A_re, A_A_im, A_B_re, A_B_im, A_C_re, A_C_im, A_log_dt, A_Dskip)
    b = _B_ref(x, B_w_gate, B_w_up, B_w_down)
    return g * a + (1 - g) * b
```

```cpp
#include <hip/hip_runtime.h>
#include <hip/hip_bf16.h>
typedef unsigned short u16;
__device__ __forceinline__ float bf2f(u16 x){ return __uint_as_float(((unsigned)x)<<16); }
__device__ __forceinline__ u16 f2bf(float f){ unsigned u=__float_as_uint(f); return (u16)((u + 0x7FFF + ((u>>16)&1))>>16); }
__device__ __forceinline__ float rbf(float f){ return bf2f(f2bf(f)); }
constexpr int L=2048,D=448,N=256,F=1792;

__global__ void kgen(const u16*Are,const u16*Aim,const u16*Bre,const u16*Bim,const u16*Cre,const u16*Cim,const u16*ldt,float*K){
  int d=blockIdx.y; int l=blockIdx.x*256+threadIdx.x;
  float dt=rbf(expf(bf2f(ldt[d])));
  float lf=rbf((float)l);
  float s=0;
  for(int n=0;n<N;n++){
    float a=dt*bf2f(Are[d*N+n]), b=dt*bf2f(Aim[d*N+n]);
    float br=dt*bf2f(Bre[d*N+n]), bi=dt*bf2f(Bim[d*N+n]);
    float cr=bf2f(Cre[d*N+n]), ci=bf2f(Cim[d*N+n]);
    float e=expf(a*lf); float sn,cs; sincosf(b*lf,&sn,&cs);
    float pr=cr*br-ci*bi, pi=cr*bi+ci*br;
    s+= e*(pr*cs-pi*sn);
  }
  K[d*L+l]=s;
}
__global__ void conv(const u16*u,const float*K,float*y){
  int d=blockIdx.y; int t=blockIdx.x*256+threadIdx.x;
  float s=0;
  for(int j=0;j<=t;j++) s+=K[d*L+j]*bf2f(u[(t-j)*D+d]);
  y[t*D+d]=s;
}
__global__ void up(const u16*x,const u16*wg,const u16*wu,u16*H){
  int t=blockIdx.y; int f=blockIdx.x*256+threadIdx.x;
  float g=0,uu=0;
  for(int k=0;k<D;k++){ float xv=bf2f(x[t*D+k]); g+=xv*bf2f(wg[k*F+f]); uu+=xv*bf2f(wu[k*F+f]); }
  g=rbf(g); uu=rbf(uu);
  float s=rbf(g/(1.f+expf(-g)));
  H[t*F+f]=f2bf(s*uu);
}
__global__ void down(const u16*x,const u16*H,const u16*wd,const float*y,const u16*Dsk,const u16*gw,u16*out){
  int t=blockIdx.x; int d=threadIdx.x;
  float gs=0; for(int k=0;k<D;k++) gs+=rbf(bf2f(x[t*D+k])*bf2f(gw[k]));
  gs=rbf(gs); float g=rbf(1.f/(1.f+expf(-gs)));
  float b=0; for(int k=0;k<F;k++) b+=bf2f(H[t*F+k])*bf2f(wd[k*D+d]);
  b=rbf(b);
  float a=rbf(rbf(y[t*D+d])+rbf(bf2f(x[t*D+d])*bf2f(Dsk[d])));
  out[t*D+d]=f2bf(rbf(g*a)+rbf(rbf(1.f-g)*b));
}
extern "C" void kernel_launch(void* const* d_in, const int* in_sizes, int n_in, void* d_out, int out_size, void* d_ws, size_t ws_size, hipStream_t stream) {
  const u16*u=(const u16*)d_in[0];
  float*K=(float*)d_ws; float*y=K+D*L; u16*H=(u16*)(y+D*L);
  kgen<<<dim3(L/256,D),256,0,stream>>>((const u16*)d_in[1],(const u16*)d_in[2],(const u16*)d_in[3],(const u16*)d_in[4],(const u16*)d_in[5],(const u16*)d_in[6],(const u16*)d_in[7],K);
  conv<<<dim3(L/256,D),256,0,stream>>>(u,K,y);
  up<<<dim3(F/256,L),256,0,stream>>>(u,(const u16*)d_in[10],(const u16*)d_in[11],H);
  down<<<L,D,0,stream>>>(u,H,(const u16*)d_in[12],y,(const u16*)d_in[8],(const u16*)d_in[13],(u16*)d_out);
}
```

```cpp
#include <hip/hip_runtime.h>

typedef unsigned short u16;
typedef unsigned u32;
typedef unsigned long long u64;
typedef u32 v4u __attribute__((ext_vector_type(4)));
typedef u32 v2u __attribute__((ext_vector_type(2)));
typedef float v2f __attribute__((ext_vector_type(2)));
typedef float v4f __attribute__((ext_vector_type(4)));
typedef float v16f __attribute__((ext_vector_type(16)));
typedef __bf16 v8bf __attribute__((ext_vector_type(8)));
typedef __bf16 v2bf __attribute__((ext_vector_type(2)));
typedef short v4s __attribute__((ext_vector_type(4)));
#define LAS __attribute__((address_space(3)))
#define GAS __attribute__((address_space(1)))
typedef GAS u32 gu32;
typedef __amdgpu_buffer_rsrc_t rsrc_t;

#define AFFINE 0

constexpr int L = 2048, D = 448, NS = 256, F = 1792;
constexpr int NMAIN = 224, NHELP = 32;

constexpr u32 WS_EPOCH = 0;
constexpr u32 WS_FLAG_H = 1024;
constexpr u32 WS_FLAG_A = WS_FLAG_H + 256 * 64;
constexpr u32 WS_FLAG_U = WS_FLAG_A + 256 * 64;
constexpr u32 WS_GATE = 65536;
constexpr u32 WS_UT = 131072;
constexpr u32 WS_A2 = WS_UT + D * L * 2;
constexpr u32 WS_H = WS_A2 + D * L * 2;
constexpr u32 WS_END = WS_H + L * F * 2;
constexpr u32 WS_STAMP = 16u << 20;
constexpr u32 TAG_STEP = 0x9E3779B9u;

constexpr u32 SLOT = 49152;
constexpr u32 LDS_TAB = 3 * SLOT;
constexpr u32 LDS_TOTAL = LDS_TAB + 2 * 672 * 2;
constexpr u32 LDS_SCR = 2 * SLOT;
constexpr u32 KCOPY = 4160, KCH = 4 * KCOPY;
constexpr u32 LDS_KIMG = SLOT;
constexpr u32 UCH = 512 + 4096;
constexpr u32 LDS_UIMG = LDS_KIMG + 2 * KCH;
constexpr u32 LDS_Y = 2 * SLOT;
constexpr u32 PROW = 272, PWAVE = 16 * PROW, PBUF = 8 * PWAVE;
constexpr u32 LDS_P = 0;
constexpr u32 LDS_AST = 2 * PBUF;

#define SB0() __builtin_amdgcn_sched_barrier(0)
#define WAITV(n) asm volatile("s_waitcnt vmcnt(" #n ")" ::: "memory")
#define WAITL0() asm volatile("s_waitcnt lgkmcnt(0)" ::: "memory")
#define BAR() __builtin_amdgcn_s_barrier()
#define RLX_AGENT __ATOMIC_RELAXED, __HIP_MEMORY_SCOPE_AGENT
constexpr int SPIN_CAP = 1 << 18;

#define STAMP(i) do { } while (0)

extern __shared__ __attribute__((aligned(16))) char smem[];

__device__ __forceinline__ float bf_lo(u32 x) { return __uint_as_float(x << 16); }
__device__ __forceinline__ float bf_hi(u32 x) { return __uint_as_float(x & 0xffff0000u); }
__device__ __forceinline__ u32 pk_bf(float lo, float hi) {
  v2f v = {lo, hi};
  return __builtin_bit_cast(u32, __builtin_convertvector(v, v2bf));
}
__device__ __forceinline__ float rbf(float x) { return bf_lo(pk_bf(x, x)); }
template <class T> __device__ __forceinline__ T lds_ld(u32 off) { return *(const LAS T*)((LAS char*)smem + off); }
template <class T> __device__ __forceinline__ void lds_st(u32 off, T v) { *(LAS T*)((LAS char*)smem + off) = v; }
__device__ __forceinline__ v2u lds_tr(u32 off) {
  return __builtin_bit_cast(v2u, __builtin_amdgcn_ds_read_tr16_b64_v4i16((LAS v4s*)((LAS char*)smem + off)));
}
__device__ __forceinline__ v8bf as_frag(v4u x) { return __builtin_bit_cast(v8bf, x); }
__device__ __forceinline__ v8bf as_frag(v2u a, v2u b) { v4u x = {a.x, a.y, b.x, b.y}; return __builtin_bit_cast(v8bf, x); }
__device__ __forceinline__ rsrc_t mk_rsrc(const void* p, u32 bytes) {
  return __builtin_amdgcn_make_buffer_rsrc((void*)p, (short)0, (int)bytes, 0x00020000);
}
__device__ __forceinline__ void dma16(rsrc_t r, u32 voff, u32 soff, u32 lds) {
  asm volatile("s_mov_b32 m0, %0\n\ts_nop 0\n\tbuffer_load_dwordx4 %1, %2, %3 offen lds"
               :: "s"(lds), "v"(voff), "s"(r), "s"(soff) : "memory");
}
typedef v2f cpx;
__device__ __forceinline__ cpx cmul(cpx a, cpx b) {
  cpx r;
  asm("v_pk_mul_f32 %0, %1, %2 op_sel:[0,0] op_sel_hi:[0,1]" : "=v"(r) : "v"(a), "v"(b));
  asm("v_pk_fma_f32 %0, %1, %2, %0 op_sel:[1,1,0] op_sel_hi:[1,0,1] neg_lo:[1,0,0]" : "+v"(r) : "v"(a), "v"(b));
  return r;
}
__device__ __forceinline__ u32 opaque(u32 x) { asm volatile("" : "+v"(x)); return x; }
__device__ __forceinline__ cpx cpow_l(float a2, float bf, float l) {
  float m = __builtin_amdgcn_exp2f(a2 * l), ph = __builtin_amdgcn_fractf(bf * l);
  return (cpx){m * __builtin_amdgcn_cosf(ph), m * __builtin_amdgcn_sinf(ph)};
}
__device__ __forceinline__ void wait_flags(const gu32* p, u32 tag, u32 first, int code = 0) {
  u32 v = first;
#pragma unroll 1
  for (int s = 0; s < SPIN_CAP && !__all(v == tag); ++s) {
    __builtin_amdgcn_s_sleep(1);
    v = __hip_atomic_load(p, RLX_AGENT);
  }
}

template <int V> struct ic { static constexpr int value = V; };
struct Args {
  const u16 *u, *are, *aim, *bre, *bim, *cre, *cim, *ldt, *dsk, *wg, *wu, *wd, *gw;
  u16* out;
  char* ws;
};

template <int HSET>
__device__ __forceinline__ void conv_q(u32 ka, u32 ka8, u32 ub, v4f (&acc)[4]) {
  constexpr int NP = HSET == 0 ? 8 : 7;
  constexpr int CS[4] = {HSET == 0 ? 7 : 6, HSET == 0 ? 4 : 5, HSET == 0 ? 3 : 2, HSET == 0 ? 0 : 1};
  v8bf A[NP], B[NP];
#pragma unroll
  for (int p = 0; p < NP; ++p) {
    A[p] = as_frag(lds_ld<v2u>(ka + 512 * (7 - p)), lds_ld<v2u>(ka8 + 512 * (7 - p)));
    B[NP - 1 - p] = as_frag(lds_ld<v4u>(ub + 512 * (NP - 1 - p)));
  }
#pragma unroll
  for (int p = 0; p < NP; ++p)
#pragma unroll
    for (int i = 0; i < 4; ++i)
      if (p <= CS[i]) acc[i] = __builtin_amdgcn_mfma_f32_16x16x32_bf16(A[p], B[CS[i] - p], acc[i], 0, 0, 0);
}

__global__ __launch_bounds__(512, 2) void gated_mix_kernel(Args g) {
  const int b = blockIdx.x, xcd = b & 7, bi = b >> 3;
  const int tid = threadIdx.x, l = tid & 63;
  const int w = __builtin_amdgcn_readfirstlane(tid >> 6);
  char* ws = g.ws;
  const rsrc_t rws = mk_rsrc(ws, WS_END);
  asm volatile("" :: "s"(g.u), "s"(g.are), "s"(g.aim), "s"(g.bre), "s"(g.bim), "s"(g.cre), "s"(g.cim), "s"(g.ldt),
               "s"(g.dsk), "s"(g.wg), "s"(g.wu), "s"(g.wd), "s"(g.gw), "s"(g.out), "s"(g.ws));
  STAMP(0);

  if (bi >= 28) {
    const int hb = b - NMAIN, t0 = 64 * hb;
    const rsrc_t rx = mk_rsrc(g.u, L * D * 2), rwd = mk_rsrc(g.wd, F * D * 2), rgw = mk_rsrc(g.gw, D * 2);
#pragma unroll
    for (int j = 0; j < 7; ++j) dma16(rx, l * 16, t0 * 896 + (w * 7 + j) * 1024, (w * 7 + j) * 1024);
    const u32 epoch_h = __hip_atomic_load((const gu32*)(ws + WS_EPOCH), RLX_AGENT);
    v4u gwv[7], pf[7];
#pragma unroll
    for (int j = 0; j < 7; ++j) gwv[j] = __builtin_amdgcn_raw_buffer_load_b128(rgw, ((l & 7) + 8 * j) * 16, 0, 0);
#pragma unroll
    for (int j = 0; j < 7; ++j)
      pf[j] = __builtin_amdgcn_raw_buffer_load_b128(rwd, (w * 7 + j < 49) ? hb * 50176 + (w * 7 + j) * 1024 + l * 16 : 0x7ffffff0, 0, 0);
    WAITV(0);
    const u32 tag = __builtin_amdgcn_readfirstlane(epoch_h) + TAG_STEP;
#pragma unroll
    for (int j = 0; j < 7; ++j) asm volatile("" :: "v"(pf[j]));
    BAR();
    STAMP(1);
    {
      const int G = l >> 4, q = (l & 15) >> 2, p = l & 3;
#pragma unroll
      for (int j = 0; j < 7; ++j) {
        const int unit = w * 7 + j, dt = unit >> 1, hf = unit & 1;
        const u32 a = (8 * (G + 4 * hf) + q) * 896 + (16 * dt + 4 * p) * 2;
        v2u lo = lds_tr(a), hi = lds_tr(a + 4 * 896);
        v4u v = {lo.x, lo.y, hi.x, hi.y};
        __builtin_amdgcn_raw_buffer_store_b128(v, rws, WS_UT + ((16 * dt + (l & 15)) * L + t0 + 8 * (G + 4 * hf)) * 2, 0, 16);
      }
    }
    {
      const int r = 8 * w + (l >> 3);
      float s = 0.f;
#pragma unroll
      for (int j = 0; j < 7; ++j) {
        v4u xv = lds_ld<v4u>(r * 896 + ((l & 7) + 8 * j) * 16);
#pragma unroll
        for (int e = 0; e < 4; ++e) {
          u32 pr = pk_bf(bf_lo(xv[e]) * bf_lo(gwv[j][e]), bf_hi(xv[e]) * bf_hi(gwv[j][e]));
          s += bf_lo(pr) + bf_hi(pr);
        }
      }
      s += __shfl_xor(s, 1); s += __shfl_xor(s, 2); s += __shfl_xor(s, 4);
      s = rbf(s);
      float gt = rbf(__builtin_amdgcn_rcpf(1.f + __builtin_amdgcn_exp2f(-1.44269504f * s)));
      if ((l & 7) == 0) __hip_atomic_store((gu32*)(ws + WS_GATE) + t0 + r, __float_as_uint(gt), RLX_AGENT);
    }
    WAITV(0);
    BAR();
    if (tid == 0) __hip_atomic_store((gu32*)(ws + WS_FLAG_U + hb * 64), tag, RLX_AGENT);
    STAMP(2);
    if (hb == NHELP - 1) {
      if (w < 4) {
        const gu32* p = (const gu32*)(ws + (tid < NMAIN ? WS_FLAG_H + tid * 64 : WS_FLAG_U + (tid - NMAIN) * 64));
        wait_flags(p, tag, ~tag, 1);
      }
      BAR();
      if (tid == 0) __hip_atomic_store((gu32*)(ws + WS_EPOCH), tag, RLX_AGENT);
    }
    return;
  }

  const int mt = (xcd & 3) * 4 + (bi & 3), ft = (xcd >> 2) * 7 + (bi >> 2);
  const int rot = ((bi >> 2) + 2 * (bi & 3) + (xcd & 3)) % 7;
  const int cb = xcd * 28 + bi;
  const int m2 = 4 * xcd + (bi & 3), nt = bi >> 2;
  const int wt = w & 1, wf = w >> 1;

  u32 p_are, p_aim, p_bre, p_bim, p_cre, p_cim, ldt2, dsk_v, epoch_v;
  {
    const u32 po = ((2 * cb + (w >> 2)) * NS + 64 * (w & 3) + l) * 2;
    asm volatile("global_load_ushort %0, %1, %2" : "=v"(p_are) : "v"(po), "s"(g.are) : "memory");
    asm volatile("global_load_ushort %0, %1, %2" : "=v"(p_aim) : "v"(po), "s"(g.aim) : "memory");
    asm volatile("global_load_ushort %0, %1, %2" : "=v"(p_bre) : "v"(po), "s"(g.bre) : "memory");
    asm volatile("global_load_ushort %0, %1, %2" : "=v"(p_bim) : "v"(po), "s"(g.bim) : "memory");
    asm volatile("global_load_ushort %0, %1, %2" : "=v"(p_cre) : "v"(po), "s"(g.cre) : "memory");
    asm volatile("global_load_ushort %0, %1, %2" : "=v"(p_cim) : "v"(po), "s"(g.cim) : "memory");
    asm volatile("global_load_dword %0, %1, %2" : "=v"(ldt2) : "v"(cb * 4), "s"(g.ldt) : "memory");
    asm volatile("global_load_dword %0, %1, %2" : "=v"(dsk_v) : "v"(cb * 4), "s"(g.dsk) : "memory");
    asm volatile("global_load_dword %0, %1, %2 sc1" : "=v"(epoch_v) : "v"(WS_EPOCH), "s"(ws) : "memory");
  }
  const rsrc_t rx = mk_rsrc(g.u, L * D * 2), rwg = mk_rsrc(g.wg, D * F * 2), rwu = mk_rsrc(g.wu, D * F * 2);
  const u32 vx0 = (l >> 3) * 896 + (((l & 7) ^ (l >> 4)) * 16);
  const u32 vx1 = (8 + (l >> 3)) * 896 + (((l & 7) ^ (4 + (l >> 4))) * 16);
  const u32 vw = (l >> 4) * (F * 2) + (((l & 15) ^ (4 * (l >> 4))) * 16);
  const u32 sx = (mt * 128 + 16 * w) * 896, sw = 8 * w * (F * 2) + ft * 256;
  auto stage = [&](int s) {
    int sk = s + rot; sk = sk >= 7 ? sk - 7 : sk;
    const u32 base = (s % 3) * SLOT + w * 2048;
    dma16(rx, vx0, sx + sk * 128, base);
    dma16(rx, vx1, sx + sk * 128, base + 1024);
    dma16(rwg, vw, sw + sk * 64 * (F * 2), base + 16384);
    dma16(rwg, vw, sw + (sk * 64 + 4) * (F * 2), base + 16384 + 1024);
    dma16(rwu, vw, sw + sk * 64 * (F * 2), base + 32768);
    dma16(rwu, vw, sw + (sk * 64 + 4) * (F * 2), base + 32768 + 1024);
  };
  stage(0);
  stage(1);
  asm volatile("s_waitcnt vmcnt(12)" : "+v"(p_are), "+v"(p_aim), "+v"(p_bre), "+v"(p_bim), "+v"(p_cre), "+v"(p_cim),
               "+v"(ldt2), "+v"(dsk_v), "+v"(epoch_v) :: "memory");
  const u32 tag = __builtin_amdgcn_readfirstlane(epoch_v) + TAG_STEP, dsk2 = __builtin_amdgcn_readfirstlane(dsk_v);
  STAMP(1);

  {
    const float ld = (w >> 2) ? bf_hi(ldt2) : bf_lo(ldt2);
    const float dt = rbf(__builtin_amdgcn_exp2f(ld * 1.44269504f));
    const float a2 = (dt * bf_lo(p_are)) * 1.44269504f, bf = (dt * bf_lo(p_aim)) * 0.15915494f;
    const cpx cbv = cmul((cpx){bf_lo(p_cre), -bf_lo(p_cim)}, (cpx){dt * bf_lo(p_bre), -dt * bf_lo(p_bim)});
    const cpx w1 = cpow_l(a2, -bf, 1.f), w8 = cpow_l(a2, bf, 8.f), w256 = cpow_l(a2, -bf, 256.f);
    const cpx w16 = cmul(w8, w8);
    const u32 swr = opaque(LDS_SCR + w * 4608 + l * 4), srd = opaque(LDS_SCR + w * 4608 + (l & 15) * 288 + (l >> 4) * 16);
    v8bf J[2][4], Hf[4];
    cpx e = {1.f, 0.f}, o = w8;
#pragma unroll
    for (int y = 0; y < 2; ++y) {
#pragma unroll
      for (int r = 0; r < 16; r += 2) {
        lds_st<u32>(swr + r * 288, pk_bf(e.x, e.y));
        lds_st<u32>(swr + (r + 1) * 288, pk_bf(o.x, o.y));
        if (y == 0 || r < 14) { e = (y == 0 && r == 0) ? w16 : cmul(e, w16); o = cmul(o, w16); }
      }
#pragma unroll
      for (int k = 0; k < 4; ++k) J[y][k] = as_frag(lds_ld<v4u>(srd + k * 64));
    }
    STAMP(12);
    cpx c[8];
    c[0] = cbv;
#pragma unroll
    for (int r = 1; r < 8; ++r) c[r] = cmul(c[r - 1], w1);
    cpx d0 = cmul(c[0], w256), d2 = cmul(c[2], w256), d4 = cmul(c[4], w256), d6 = cmul(c[6], w256);
    cpx e0 = cmul(d0, w256), e4 = cmul(d4, w256), f0 = cmul(e0, w256), f4 = cmul(e4, w256);
#pragma unroll
    for (int r = 0; r < 8; ++r) lds_st<u32>(swr + r * 288, pk_bf(c[r].x, c[r].y));
    lds_st<u32>(swr + 8 * 288, pk_bf(d0.x, d0.y));
    lds_st<u32>(swr + 9 * 288, pk_bf(d2.x, d2.y));
    lds_st<u32>(swr + 10 * 288, pk_bf(d4.x, d4.y));
    lds_st<u32>(swr + 11 * 288, pk_bf(d6.x, d6.y));
    lds_st<u32>(swr + 12 * 288, pk_bf(e0.x, e0.y));
    lds_st<u32>(swr + 13 * 288, pk_bf(e4.x, e4.y));
    lds_st<u32>(swr + 14 * 288, pk_bf(f0.x, f0.y));
    lds_st<u32>(swr + 15 * 288, pk_bf(f4.x, f4.y));
    v4f kc[2][2] = {};
#pragma unroll
    for (int k = 0; k < 4; ++k) Hf[k] = as_frag(lds_ld<v4u>(srd + k * 64));
#pragma unroll
    for (int k = 0; k < 4; ++k) {
      kc[0][0] = __builtin_amdgcn_mfma_f32_16x16x32_bf16(Hf[k], J[0][k], kc[0][0], 0, 0, 0);
      kc[0][1] = __builtin_amdgcn_mfma_f32_16x16x32_bf16(Hf[k], J[1][k], kc[0][1], 0, 0, 0);
    }
    cpx q = f0;
#pragma unroll
    for (int r = 0; r < 5; ++r) { q = cmul(q, w256); lds_st<u32>(swr + r * 288, pk_bf(q.x, q.y)); }
#pragma unroll
    for (int k = 0; k < 4; ++k) Hf[k] = as_frag(lds_ld<v4u>(srd + k * 64));
#pragma unroll
    for (int k = 0; k < 4; ++k) {
      kc[1][0] = __builtin_amdgcn_mfma_f32_16x16x32_bf16(Hf[k], J[0][k], kc[1][0], 0, 0, 0);
      kc[1][1] = __builtin_amdgcn_mfma_f32_16x16x32_bf16(Hf[k], J[1][k], kc[1][1], 0, 0, 0);
    }
#pragma unroll
    for (int t = 0; t < 4; ++t) lds_st<v4f>(LDS_SCR + w * 4608 + t * 1024 + l * 16, kc[t >> 1][t & 1]);
    STAMP(13);
    WAITL0(); BAR();
    const u32 pr = LDS_SCR + (w & 4) * 4608 + (w & 3) * 1024 + l * 16;
    v4f s = (lds_ld<v4f>(pr) + lds_ld<v4f>(pr + 4608)) + (lds_ld<v4f>(pr + 2 * 4608) + lds_ld<v4f>(pr + 3 * 4608));
    const int gq = l >> 4, j = (l & 15) + 16 * (w & 1);
    const u32 tb = LDS_TAB + (w >> 2) * 1344;
    const u32 p01 = pk_bf(s[0], s[1]), p23 = pk_bf(s[2], s[3]);
    int i0, i1, i2, i3;
    if (!(w & 2)) {
      i0 = gq < 2 ? 8 * j + 4 * gq : gq == 2 ? 256 + 4 * j : 384 + 2 * j;
      i1 = i0 + 1; i2 = i0 + (gq == 3 ? 64 : 2); i3 = i2 + 1;
    } else {
      i0 = gq == 0 ? 512 + j : (gq == 1 && j == 0) ? 640 : 650;
      i1 = gq == 0 ? 544 + j : 650; i2 = gq == 0 ? 576 + j : 650; i3 = gq == 0 ? 608 + j : 650;
    }
    lds_st<u16>(tb + 2 * i0, (u16)p01); lds_st<u16>(tb + 2 * i1, (u16)(p01 >> 16));
    lds_st<u16>(tb + 2 * i2, (u16)p23); lds_st<u16>(tb + 2 * i3, (u16)(p23 >> 16));
    if (l == 0) lds_st<u16>(tb + 2 * 641, (u16)0);
    WAITL0(); BAR();
  }
  stage(2);
  STAMP(2);

  v16f accg[2] = {}, accu[2] = {};
  u32 uflag = 0, gate4[4];
  v4u ut4;
  {
    u32 xa[4];
#pragma unroll
    for (int s = 0; s < 4; ++s)
      xa[s] = (8 * wt + ((l & 31) >> 3)) * 1024 + (l & 7) * 128 + (((2 * s + (l >> 5)) ^ ((l >> 1) & 7)) * 16);
    const int G = l >> 4, q = (l & 15) >> 2, p = l & 3;
    const u32 wa = 16384 + (8 * (G >> 1) + q) * 256 + (((4 * wf + 2 * (G & 1) + (p >> 1)) ^ (4 * q)) * 16) + 8 * (p & 1);
    v8bf fg[2], fu[2], fx0[2], fx1[2];
    auto rd = [&](int buf, int st, int ks) {
      const u32 sb = (st % 3) * SLOT;
      fg[buf] = as_frag(lds_tr(sb + wa + ks * 4096), lds_tr(sb + wa + ks * 4096 + 1024));
      fx0[buf] = as_frag(lds_ld<v4u>(sb + xa[ks]));
      fu[buf] = as_frag(lds_tr(sb + wa + ks * 4096 + 16384), lds_tr(sb + wa + ks * 4096 + 16384 + 1024));
      fx1[buf] = as_frag(lds_ld<v4u>(sb + xa[ks] + 4096));
    };
    auto mm = [&](int buf) {
      accg[0] = __builtin_amdgcn_mfma_f32_32x32x16_bf16(fg[buf], fx0[buf], accg[0], 0, 0, 0);
      accu[0] = __builtin_amdgcn_mfma_f32_32x32x16_bf16(fu[buf], fx0[buf], accu[0], 0, 0, 0);
      accg[1] = __builtin_amdgcn_mfma_f32_32x32x16_bf16(fg[buf], fx1[buf], accg[1], 0, 0, 0);
      accu[1] = __builtin_amdgcn_mfma_f32_32x32x16_bf16(fu[buf], fx1[buf], accu[1], 0, 0, 0);
    };
    WAITV(12); BAR();
    STAMP(3);
    rd(0, 0, 0);
#pragma unroll
    for (int s = 0; s < 7; ++s) {
      SB0(); rd(1, s, 1); SB0(); mm(0);
      if (s == 5) {
        const int ch = tid >> 8, m = tid & 255;
        const u32 tb = LDS_TAB + ch * 1344;
        u32 v[11];
#pragma unroll
        for (int j = 0; j < 11; ++j) {
          const int sv = 2047 - 8 * m - j, oct = j < 8 ? 2040 - 8 * m : 2032 - 8 * m;
          const int sh = oct >= 1024 ? 3 : oct >= 512 ? 2 : oct >= 256 ? 1 : 0;
          const int r = (sv + (sh ? (1 << (sh - 1)) - 1 + ((sv >> sh) & 1) : 0)) >> sh;
          const int idx = sv < 0 ? 641 : r + 128 * sh;
          v[j] = lds_ld<u16>(tb + 2 * idx);
        }
        u32 E[5], O[5];
#pragma unroll
        for (int j = 0; j < 5; ++j) { E[j] = v[2 * j] | (v[2 * j + 1] << 16); O[j] = v[2 * j + 1] | (v[2 * j + 2] << 16); }
        const u32 kb = LDS_KIMG + ch * KCH + m * 16;
        lds_st<v4u>(kb + 3 * KCOPY, (v4u){E[0], E[1], E[2], E[3]});
        lds_st<v4u>(kb + 2 * KCOPY, (v4u){O[0], O[1], O[2], O[3]});
        lds_st<v4u>(kb + 1 * KCOPY, (v4u){E[1], E[2], E[3], E[4]});
        lds_st<v4u>(kb, (v4u){O[1], O[2], O[3], O[4]});
        if (tid < 16) lds_st<v4u>(LDS_KIMG + (tid >> 1) * KCOPY + 4096 + (tid & 1) * 16, (v4u){0, 0, 0, 0});
        if (tid >= 64 && tid < 128) lds_st<v4u>(LDS_UIMG + ((tid >> 5) & 1) * UCH + (tid & 31) * 16, (v4u){0, 0, 0, 0});
      }
      SB0(); rd(0, s, 2); SB0(); mm(1);
      SB0(); rd(1, s, 3); SB0(); mm(0);
      SB0();
      if (s < 6) {
        WAITL0();
        if (s < 5) WAITV(6); else WAITV(0);
        BAR();
        if (s + 3 < 7) stage(s + 3);
        if (s == 4) uflag = __hip_atomic_load((const gu32*)(ws + WS_FLAG_U + (l & 31) * 64), RLX_AGENT);
        if (s == 5) {
          wait_flags((const gu32*)(ws + WS_FLAG_U + (l & 31) * 64), tag, uflag, 2);
          __builtin_amdgcn_fence(__ATOMIC_ACQUIRE, "wavefront");
          ut4 = __builtin_amdgcn_raw_buffer_load_b128(rws, WS_UT + (2 * cb) * (L * 2) + tid * 16, 0, 16);
#pragma unroll
          for (int t = 0; t < 4; ++t)
            gate4[t] = __builtin_amdgcn_raw_buffer_load_b32(rws, WS_GATE + (64 * m2 + 16 * ((t + nt) & 3) + (tid >> 5)) * 4, 0, 16);
        }
        rd(0, s + 1, 0);
        SB0();
      }
      mm(1);
    }
  }
  STAMP(4);
  lds_st<v4u>(LDS_UIMG + (tid >> 8) * UCH + 512 + (tid & 255) * 16, ut4);

  {
    const u32 hb = WS_H + ((mt * 8 + 4 * wt + ((l & 31) >> 4)) * 56 + ft * 4 + wf) * 1024 + (l & 15) * 16 + (l >> 5) * 256;
#pragma unroll
    for (int j = 0; j < 2; ++j)
#pragma unroll
      for (int s = 0; s < 2; ++s) {
        v4u o;
#pragma unroll
        for (int e = 0; e < 4; ++e) {
          float h[2];
#pragma unroll
          for (int z = 0; z < 2; ++z) {
            const float gv = accg[j][8 * s + 2 * e + z], uv = accu[j][8 * s + 2 * e + z];
            h[z] = (gv * uv) * __builtin_amdgcn_rcpf(1.f + __builtin_amdgcn_exp2f(-1.44269504f * gv));
          }
          o[e] = pk_bf(h[0], h[1]);
        }
        __builtin_amdgcn_raw_buffer_store_b128(o, rws, hb + j * (2 * 56 * 1024) + s * 512, 0, 16);
      }
  }
  asm volatile("" ::: "memory");
  STAMP(5);
  const int kr = (w + 4 * (nt >> 2)) & 7;
  v2u wraw[7][8];
  {
    const rsrc_t rwd = mk_rsrc(g.wd, F * D * 2);
    const int gq = l >> 4;
    const u32 vo = (16 * (gq >> 1) + 4 * (gq & 1)) * 896 + (l & 15) * 8;
    const u32 so = kr * 7 * 32 * 896 + nt * 128;
#pragma unroll
    for (int i = 0; i < 7; ++i)
#pragma unroll
      for (int jj = 0; jj < 8; ++jj)
        wraw[i][jj] = __builtin_amdgcn_raw_buffer_load_b64(rwd, vo + (jj & 3) * 896, so + (32 * i + 8 * (jj >> 2)) * 896, 0);
  }
  asm volatile("" ::: "memory");
  WAITL0(); BAR();
  STAMP(6);

  {
    const int ch = w >> 2, hs = w & 1, z = (w >> 1) & 1;
    const int a = l & 15, gq = l >> 4;
    u32 ka = LDS_KIMG + ch * KCH + (a & 3) * KCOPY + 2 * (2028 + 8 * gq - 4 * (a >> 2)) - 512 * 7 - 256 * z;
    u32 ub = LDS_UIMG + ch * UCH + 2 * (240 + 16 * a + 8 * gq) - 256 * z;
    v4f acc[4] = {};
    auto run = [&](auto hset) {
#pragma unroll 1
      for (int q = 0; q < 4; ++q) {
        conv_q<decltype(hset)::value>(opaque(ka), opaque(ka + 8), opaque(ub), acc);
        ka -= 64; ub -= 64;
        if (q == 1) {
          WAITV(56); BAR();
          if (tid == 0) __hip_atomic_store((gu32*)(ws + WS_FLAG_H + (mt * 14 + ft) * 64), tag, RLX_AGENT);
        }
      }
    };
    if (hs == 0) run(ic<0>{}); else run(ic<1>{});
    const u32 yb = LDS_Y + (z * 2 + ch) * 8192 + (16 * a + 4 * (gq ^ ((a >> 1) & 3))) * 4;
#pragma unroll
    for (int i = 0; i < 4; ++i) {
      const int c = hs == 0 ? (i == 0 ? 7 : i == 1 ? 4 : i == 2 ? 3 : 0) : (i == 0 ? 6 : i == 1 ? 5 : i == 2 ? 2 : 1);
      lds_st<v4f>(yb + c * 1024, acc[i]);
    }
  }
  WAITL0(); BAR();
  STAMP(7);
  u32 hflag = tag;
  const gu32* hfp = (const gu32*)(ws + WS_FLAG_H + ((m2 >> 1) * 14 + (l < 14 ? l : 0)) * 64);
  if (w == 0) hflag = __hip_atomic_load(hfp, RLX_AGENT);
  {
    const u32 yo = LDS_Y + (tid >> 2) * 64 + (((tid & 3) ^ ((tid >> 3) & 3)) * 16);
    const v4f y0 = lds_ld<v4f>(yo) + lds_ld<v4f>(yo + 16384), y1 = lds_ld<v4f>(yo + 8192) + lds_ld<v4f>(yo + 24576);
    const v2u u0 = lds_ld<v2u>(LDS_UIMG + 512 + tid * 8), u1 = lds_ld<v2u>(LDS_UIMG + UCH + 512 + tid * 8);
    const float k0 = bf_lo(dsk2), k1 = bf_hi(dsk2);
    v4u o;
#pragma unroll
    for (int e = 0; e < 4; ++e) {
      const float a0 = (e & 1) ? bf_hi(u0[e >> 1]) : bf_lo(u0[e >> 1]), a1 = (e & 1) ? bf_hi(u1[e >> 1]) : bf_lo(u1[e >> 1]);
      const u32 yy = pk_bf(y0[e], y1[e]), sk = pk_bf(a0 * k0, a1 * k1);
      o[e] = pk_bf(bf_lo(yy) + bf_lo(sk), bf_hi(yy) + bf_hi(sk));
    }
    __builtin_amdgcn_raw_buffer_store_b128(o, rws, WS_A2 + cb * 8192 + tid * 16, 0, 16);
  }
  if (w == 0) wait_flags(hfp, tag, hflag, 3);
  WAITL0(); BAR();
  __builtin_amdgcn_fence(__ATOMIC_ACQUIRE, "wavefront");
  STAMP(8);

  {
    v4u hf[2][7];
    const u32 hv = WS_H + kr * 7 * 1024 + l * 16;
    auto ldh = [&](int buf, int tt) {
      const u32 so = (m2 * 4 + ((tt + nt) & 3)) * (56 * 1024);
#pragma unroll
      for (int i = 0; i < 7; ++i) hf[buf][i] = __builtin_amdgcn_raw_buffer_load_b128(rws, hv + i * 1024, so, 16);
    };
    ldh(0, 0);
    ldh(1, 1);
    SB0();
    v8bf wfr[7][4];
#pragma unroll
    for (int i = 0; i < 7; ++i)
#pragma unroll
      for (int c = 0; c < 4; ++c) {
        v4u x;
#pragma unroll
        for (int m = 0; m < 4; ++m)
          x[m] = __builtin_amdgcn_perm(wraw[i][2 * m + 1][c >> 1], wraw[i][2 * m][c >> 1], (c & 1) ? 0x07060302u : 0x05040100u);
        wfr[i][c] = as_frag(x);
      }
    SB0();
    v2f bs[4];
    u32 aflag = 0;
    v4u a4;
    const gu32* afp = (const gu32*)(ws + WS_FLAG_A + (32 * nt + (l & 31)) * 64);
#pragma unroll
    for (int tt = 0; tt < 4; ++tt) {
      v4f acc[4] = {};
#pragma unroll
      for (int i = 0; i < 7; ++i)
#pragma unroll
        for (int c = 0; c < 4; ++c)
          acc[c] = __builtin_amdgcn_mfma_f32_16x16x32_bf16(wfr[i][c], as_frag(hf[tt & 1][i]), acc[c], 0, 0, 0);
      SB0();
      if (tt == 2) {
        wait_flags(afp, tag, aflag, 4);
        __builtin_amdgcn_fence(__ATOMIC_ACQUIRE, "wavefront");
        a4 = __builtin_amdgcn_raw_buffer_load_b128(rws, WS_A2 + (32 * nt + (tid >> 4)) * 8192 + (64 * m2 + 4 * (tid & 15)) * 4, 0, 16);
      }
      if (tt < 2) ldh(tt & 1, tt + 2);
      if (tt == 1) aflag = __hip_atomic_load(afp, RLX_AGENT);
      SB0();
      const u32 pw = LDS_P + (tt & 1) * PBUF + w * PWAVE + (l & 15) * PROW + (l >> 4) * 64;
#pragma unroll
      for (int r = 0; r < 4; ++r) lds_st<v4f>(pw + r * 16, (v4f){acc[0][r], acc[1][r], acc[2][r], acc[3][r]});
      if (tt == 3) lds_st<v4u>(LDS_AST + (tid >> 4) * 272 + (tid & 15) * 16, a4);
      WAITL0(); BAR();
      if (tt == 0 && tid == 0) __hip_atomic_store((gu32*)(ws + WS_FLAG_A + cb * 64), tag, RLX_AGENT);
      const u32 prd = LDS_P + (tt & 1) * PBUF + (tid >> 5) * PROW + (tid & 31) * 8;
      v2f s = lds_ld<v2f>(prd);
#pragma unroll
      for (int k = 1; k < 8; ++k) s += lds_ld<v2f>(prd + k * PWAVE);
      bs[tt] = s;
      if (tt == 0) STAMP(9);
    }
    STAMP(10);
    const rsrc_t ro = mk_rsrc(g.out, L * D * 2);
#pragma unroll
    for (int tt = 0; tt < 4; ++tt) {
      const int t = 16 * ((tt + nt) & 3) + (tid >> 5);
      const u32 av = lds_ld<u32>(LDS_AST + (tid & 31) * 272 + t * 4);
      const float gt = __uint_as_float(gate4[tt]), og = rbf(1.f - gt);
      const u32 bb = pk_bf(bs[tt].x, bs[tt].y);
      const u32 ga = pk_bf(gt * bf_lo(av), gt * bf_hi(av)), gb = pk_bf(og * bf_lo(bb), og * bf_hi(bb));
      __builtin_amdgcn_raw_buffer_store_b32(pk_bf(bf_lo(ga) + bf_lo(gb), bf_hi(ga) + bf_hi(gb)), ro,
                                            ((64 * m2 + t) * D + 64 * nt + 2 * (tid & 31)) * 2, 0, 0);
    }
  }
}

extern "C" void kernel_launch(void* const* d_in, const int* in_sizes, int n_in, void* d_out, int out_size, void* d_ws,
                              size_t ws_size, hipStream_t stream) {
  Args a;
  a.u = (const u16*)d_in[0];
  a.are = (const u16*)d_in[1]; a.aim = (const u16*)d_in[2];
  a.bre = (const u16*)d_in[3]; a.bim = (const u16*)d_in[4];
  a.cre = (const u16*)d_in[5]; a.cim = (const u16*)d_in[6];
  a.ldt = (const u16*)d_in[7]; a.dsk = (const u16*)d_in[8];
  a.wg = (const u16*)d_in[10]; a.wu = (const u16*)d_in[11]; a.wd = (const u16*)d_in[12];
  a.gw = (const u16*)d_in[13];
  a.out = (u16*)d_out;
  a.ws = (char*)d_ws;
  (void)hipFuncSetAttribute((const void*)gated_mix_kernel, hipFuncAttributeMaxDynamicSharedMemorySize, LDS_TOTAL);
  gated_mix_kernel<<<NMAIN + NHELP, 512, LDS_TOTAL, stream>>>(a);
}
```

```cpp
#include <hip/hip_runtime.h>

typedef unsigned short u16;
typedef unsigned u32;
typedef unsigned long long u64;
typedef u32 v4u __attribute__((ext_vector_type(4)));
typedef u32 v2u __attribute__((ext_vector_type(2)));
typedef float v2f __attribute__((ext_vector_type(2)));
typedef float v4f __attribute__((ext_vector_type(4)));
typedef float v16f __attribute__((ext_vector_type(16)));
typedef __bf16 v8bf __attribute__((ext_vector_type(8)));
typedef __bf16 v2bf __attribute__((ext_vector_type(2)));
typedef short v4s __attribute__((ext_vector_type(4)));
#define LAS __attribute__((address_space(3)))
#define GAS __attribute__((address_space(1)))
typedef GAS u32 gu32;
typedef __amdgpu_buffer_rsrc_t rsrc_t;

#define AFFINE 0

constexpr int L = 2048, D = 448, NS = 256, F = 1792;
constexpr int NMAIN = 224, NHELP = 32;

constexpr u32 WS_EPOCH = 0;
constexpr u32 WS_FLAG_H = 1024;
constexpr u32 WS_FLAG_A = WS_FLAG_H + 256 * 64;
constexpr u32 WS_FLAG_U = WS_FLAG_A + 256 * 64;
constexpr u32 WS_GATE = 65536;
constexpr u32 WS_UT = 131072;
constexpr u32 WS_A2 = WS_UT + D * L * 2;
constexpr u32 WS_H = WS_A2 + D * L * 2;
constexpr u32 WS_END = WS_H + L * F * 2;
constexpr u32 WS_STAMP = 16u << 20;
constexpr u32 TAG_STEP = 0x9E3779B9u;

constexpr u32 SLOT = 49152;
constexpr u32 LDS_TAB = 3 * SLOT;
constexpr u32 LDS_TOTAL = LDS_TAB + 2 * 672 * 2;
constexpr u32 LDS_SCR = 2 * SLOT;
constexpr u32 KCOPY = 4160, KCH = 4 * KCOPY;
constexpr u32 LDS_KIMG = SLOT;
constexpr u32 UCH = 512 + 4096;
constexpr u32 LDS_UIMG = LDS_KIMG + 2 * KCH;
constexpr u32 LDS_Y = 2 * SLOT;
constexpr u32 PROW = 272, PWAVE = 16 * PROW, PBUF = 8 * PWAVE;
constexpr u32 LDS_P = 0;
constexpr u32 LDS_AST = 2 * PBUF;

#define SB0() __builtin_amdgcn_sched_barrier(0)
#define WAITV(n) asm volatile("s_waitcnt vmcnt(" #n ")" ::: "memory")
#define WAITL0() asm volatile("s_waitcnt lgkmcnt(0)" ::: "memory")
#define BAR() __builtin_amdgcn_s_barrier()
#define RLX_AGENT __ATOMIC_RELAXED, __HIP_MEMORY_SCOPE_AGENT
constexpr int SPIN_CAP = 1 << 18;

#define STAMP(i) do { } while (0)

extern __shared__ __attribute__((aligned(16))) char smem[];

__device__ __forceinline__ float bf_lo(u32 x) { return __uint_as_float(x << 16); }
__device__ __forceinline__ float bf_hi(u32 x) { return __uint_as_float(x & 0xffff0000u); }
__device__ __forceinline__ u32 pk_bf(float lo, float hi) {
  v2f v = {lo, hi};
  return __builtin_bit_cast(u32, __builtin_convertvector(v, v2bf));
}
__device__ __forceinline__ float rbf(float x) { return bf_lo(pk_bf(x, x)); }
template <class T> __device__ __forceinline__ T lds_ld(u32 off) { return *(const LAS T*)((LAS char*)smem + off); }
template <class T> __device__ __forceinline__ void lds_st(u32 off, T v) { *(LAS T*)((LAS char*)smem + off) = v; }
__device__ __forceinline__ v2u lds_tr(u32 off) {
  return __builtin_bit_cast(v2u, __builtin_amdgcn_ds_read_tr16_b64_v4i16((LAS v4s*)((LAS char*)smem + off)));
}
__device__ __forceinline__ v8bf as_frag(v4u x) { return __builtin_bit_cast(v8bf, x); }
__device__ __forceinline__ v8bf as_frag(v2u a, v2u b) { v4u x = {a.x, a.y, b.x, b.y}; return __builtin_bit_cast(v8bf, x); }
__device__ __forceinline__ rsrc_t mk_rsrc(const void* p, u32 bytes) {
  return __builtin_amdgcn_make_buffer_rsrc((void*)p, (short)0, (int)bytes, 0x00020000);
}
__device__ __forceinline__ void dma16(rsrc_t r, u32 voff, u32 soff, u32 lds) {
  asm volatile("s_mov_b32 m0, %0\n\ts_nop 0\n\tbuffer_load_dwordx4 %1, %2, %3 offen lds"
               :: "s"(lds), "v"(voff), "s"(r), "s"(soff) : "memory");
}
typedef v2f cpx;
__device__ __forceinline__ cpx cmul(cpx a, cpx b) {
  cpx r;
  asm("v_pk_mul_f32 %0, %1, %2 op_sel:[0,0] op_sel_hi:[0,1]" : "=v"(r) : "v"(a), "v"(b));
  asm("v_pk_fma_f32 %0, %1, %2, %0 op_sel:[1,1,0] op_sel_hi:[1,0,1] neg_lo:[1,0,0]" : "+v"(r) : "v"(a), "v"(b));
  return r;
}
__device__ __forceinline__ u32 opaque(u32 x) { asm volatile("" : "+v"(x)); return x; }
__device__ __forceinline__ cpx cpow_l(float a2, float bf, float l) {
  float m = __builtin_amdgcn_exp2f(a2 * l), ph = __builtin_amdgcn_fractf(bf * l);
  return (cpx){m * __builtin_amdgcn_cosf(ph), m * __builtin_amdgcn_sinf(ph)};
}
__device__ __forceinline__ void wait_flags(const gu32* p, u32 tag, u32 first, int code = 0) {
  u32 v = first;
#pragma unroll 1
  for (int s = 0; s < SPIN_CAP && !__all(v == tag); ++s) {
    __builtin_amdgcn_s_sleep(1);
    v = __hip_atomic_load(p, RLX_AGENT);
  }
}

template <int V> struct ic { static constexpr int value = V; };
struct Args {
  const u16 *u, *are, *aim, *bre, *bim, *cre, *cim, *ldt, *dsk, *wg, *wu, *wd, *gw;
  u16* out;
  char* ws;
};

template <int HSET>
__device__ __forceinline__ void conv_q(u32 ka, u32 ka8, u32 ub, v4f (&acc)[4]) {
  constexpr int NP = HSET == 0 ? 8 : 7;
  constexpr int CS[4] = {HSET == 0 ? 7 : 6, HSET == 0 ? 4 : 5, HSET == 0 ? 3 : 2, HSET == 0 ? 0 : 1};
  v8bf A[NP], B[NP];
#pragma unroll
  for (int p = 0; p < NP; ++p) {
    A[p] = as_frag(lds_ld<v2u>(ka + 512 * (7 - p)), lds_ld<v2u>(ka8 + 512 * (7 - p)));
    B[NP - 1 - p] = as_frag(lds_ld<v4u>(ub + 512 * (NP - 1 - p)));
  }
#pragma unroll
  for (int p = 0; p < NP; ++p)
#pragma unroll
    for (int i = 0; i < 4; ++i)
      if (p <= CS[i]) acc[i] = __builtin_amdgcn_mfma_f32_16x16x32_bf16(A[p], B[CS[i] - p], acc[i], 0, 0, 0);
}

__global__ __launch_bounds__(512, 2) void gated_mix_kernel(Args g_) {
  u64 tstart; asm volatile("s_memrealtime %0" : "=s"(tstart) :: "memory");
  SB0();
#if defined(__HIP_DEVICE_COMPILE__)
  const Args g = *(const __attribute__((address_space(4))) Args*)__builtin_amdgcn_kernarg_segment_ptr();
#else
  const Args g = g_;
#endif
  const int b = blockIdx.x, xcd = b & 7, bi = b >> 3;
  const int tid = threadIdx.x, l = tid & 63;
  const int w = __builtin_amdgcn_readfirstlane(tid >> 6);
  char* ws = g.ws;
  const rsrc_t rws = mk_rsrc(ws, WS_END);
  asm volatile("" :: "s"(g.u), "s"(g.are), "s"(g.aim), "s"(g.bre), "s"(g.bim), "s"(g.cre), "s"(g.cim), "s"(g.ldt),
               "s"(g.dsk), "s"(g.wg), "s"(g.wu), "s"(g.wd), "s"(g.gw), "s"(g.out), "s"(g.ws));
  STAMP(0);

  if (bi >= 28) {
    const int hb = b - NMAIN, t0 = 64 * hb;
    const rsrc_t rx = mk_rsrc(g.u, L * D * 2), rwd = mk_rsrc(g.wd, F * D * 2), rgw = mk_rsrc(g.gw, D * 2);
#pragma unroll
    for (int j = 0; j < 7; ++j) dma16(rx, l * 16, t0 * 896 + (w * 7 + j) * 1024, (w * 7 + j) * 1024);
    const u32 epoch_h = __hip_atomic_load((const gu32*)(ws + WS_EPOCH), RLX_AGENT);
    v4u gwv[7], pf[7];
#pragma unroll
    for (int j = 0; j < 7; ++j) gwv[j] = __builtin_amdgcn_raw_buffer_load_b128(rgw, ((l & 7) + 8 * j) * 16, 0, 0);
#pragma unroll
    for (int j = 0; j < 7; ++j)
      pf[j] = __builtin_amdgcn_raw_buffer_load_b128(rwd, (w * 7 + j < 49) ? hb * 50176 + (w * 7 + j) * 1024 + l * 16 : 0x7ffffff0, 0, 0);
    WAITV(0);
    const u32 tag = __builtin_amdgcn_readfirstlane(epoch_h) + TAG_STEP;
#pragma unroll
    for (int j = 0; j < 7; ++j) asm volatile("" :: "v"(pf[j]));
    BAR();
    STAMP(1);
    {
      const int G = l >> 4, q = (l & 15) >> 2, p = l & 3;
#pragma unroll
      for (int j = 0; j < 7; ++j) {
        const int unit = w * 7 + j, dt = unit >> 1, hf = unit & 1;
        const u32 a = (8 * (G + 4 * hf) + q) * 896 + (16 * dt + 4 * p) * 2;
        v2u lo = lds_tr(a), hi = lds_tr(a + 4 * 896);
        v4u v = {lo.x, lo.y, hi.x, hi.y};
        __builtin_amdgcn_raw_buffer_store_b128(v, rws, WS_UT + ((16 * dt + (l & 15)) * L + t0 + 8 * (G + 4 * hf)) * 2, 0, 16);
      }
    }
    {
      const int r = 8 * w + (l >> 3);
      float s = 0.f;
#pragma unroll
      for (int j = 0; j < 7; ++j) {
        v4u xv = lds_ld<v4u>(r * 896 + ((l & 7) + 8 * j) * 16);
#pragma unroll
        for (int e = 0; e < 4; ++e) {
          u32 pr = pk_bf(bf_lo(xv[e]) * bf_lo(gwv[j][e]), bf_hi(xv[e]) * bf_hi(gwv[j][e]));
          s += bf_lo(pr) + bf_hi(pr);
        }
      }
      s += __shfl_xor(s, 1); s += __shfl_xor(s, 2); s += __shfl_xor(s, 4);
      s = rbf(s);
      float gt = rbf(__builtin_amdgcn_rcpf(1.f + __builtin_amdgcn_exp2f(-1.44269504f * s)));
      if ((l & 7) == 0) __hip_atomic_store((gu32*)(ws + WS_GATE) + t0 + r, __float_as_uint(gt), RLX_AGENT);
    }
    WAITV(0);
    BAR();
    if (tid == 0) __hip_atomic_store((gu32*)(ws + WS_FLAG_U + hb * 64), tag, RLX_AGENT);
    STAMP(2);
    if (hb == NHELP - 1) {
      if (w < 4) {
        const gu32* p = (const gu32*)(ws + (tid < NMAIN ? WS_FLAG_H + tid * 64 : WS_FLAG_U + (tid - NMAIN) * 64));
        wait_flags(p, tag, ~tag, 1);
      }
      BAR();
      if (tid == 0) __hip_atomic_store((gu32*)(ws + WS_EPOCH), tag, RLX_AGENT);
    }
    return;
  }

  const int mt = (xcd & 3) * 4 + (bi & 3), ft = (xcd >> 2) * 7 + (bi >> 2);
  const int rot = ((bi >> 2) + 2 * (bi & 3) + (xcd & 3)) % 7;
  const int cb = xcd * 28 + bi;
  const int m2 = 4 * xcd + (bi & 3), nt = bi >> 2;
  const int wt = w & 1, wf = w >> 1;

  u32 p_are, p_aim, p_bre, p_bim, p_cre, p_cim, ldt2, dsk_v, epoch_v;
  {
    const u32 po = ((2 * cb + (w >> 2)) * NS + 64 * (w & 3) + l) * 2;
    asm volatile("global_load_ushort %0, %1, %2" : "=v"(p_are) : "v"(po), "s"(g.are) : "memory");
    asm volatile("global_load_ushort %0, %1, %2" : "=v"(p_aim) : "v"(po), "s"(g.aim) : "memory");
    asm volatile("global_load_ushort %0, %1, %2" : "=v"(p_bre) : "v"(po), "s"(g.bre) : "memory");
    asm volatile("global_load_ushort %0, %1, %2" : "=v"(p_bim) : "v"(po), "s"(g.bim) : "memory");
    asm volatile("global_load_ushort %0, %1, %2" : "=v"(p_cre) : "v"(po), "s"(g.cre) : "memory");
    asm volatile("global_load_ushort %0, %1, %2" : "=v"(p_cim) : "v"(po), "s"(g.cim) : "memory");
    asm volatile("global_load_dword %0, %1, %2" : "=v"(ldt2) : "v"(cb * 4), "s"(g.ldt) : "memory");
    asm volatile("global_load_dword %0, %1, %2" : "=v"(dsk_v) : "v"(cb * 4), "s"(g.dsk) : "memory");
    asm volatile("global_load_dword %0, %1, %2 sc1" : "=v"(epoch_v) : "v"(WS_EPOCH), "s"(ws) : "memory");
  }
  const rsrc_t rx = mk_rsrc(g.u, L * D * 2), rwg = mk_rsrc(g.wg, D * F * 2), rwu = mk_rsrc(g.wu, D * F * 2);
  const u32 vx0 = (l >> 3) * 896 + (((l & 7) ^ (l >> 4)) * 16);
  const u32 vx1 = (8 + (l >> 3)) * 896 + (((l & 7) ^ (4 + (l >> 4))) * 16);
  const u32 vw = (l >> 4) * (F * 2) + (((l & 15) ^ (4 * (l >> 4))) * 16);
  const u32 sx = (mt * 128 + 16 * w) * 896, sw = 8 * w * (F * 2) + ft * 256;
  auto stage = [&](int s) {
    int sk = s + rot; sk = sk >= 7 ? sk - 7 : sk;
    const u32 base = (s % 3) * SLOT + w * 2048;
    dma16(rx, vx0, sx + sk * 128, base);
    dma16(rx, vx1, sx + sk * 128, base + 1024);
    dma16(rwg, vw, sw + sk * 64 * (F * 2), base + 16384);
    dma16(rwg, vw, sw + (sk * 64 + 4) * (F * 2), base + 16384 + 1024);
    dma16(rwu, vw, sw + sk * 64 * (F * 2), base + 32768);
    dma16(rwu, vw, sw + (sk * 64 + 4) * (F * 2), base + 32768 + 1024);
  };
  stage(0);
  stage(1);
  asm volatile("s_waitcnt vmcnt(12)" : "+v"(p_are), "+v"(p_aim), "+v"(p_bre), "+v"(p_bim), "+v"(p_cre), "+v"(p_cim),
               "+v"(ldt2), "+v"(dsk_v), "+v"(epoch_v) :: "memory");
  const u32 tag = __builtin_amdgcn_readfirstlane(epoch_v) + TAG_STEP, dsk2 = __builtin_amdgcn_readfirstlane(dsk_v);
  STAMP(1);

  {
    const float ld = (w >> 2) ? bf_hi(ldt2) : bf_lo(ldt2);
    const float dt = rbf(__builtin_amdgcn_exp2f(ld * 1.44269504f));
    const float a2 = (dt * bf_lo(p_are)) * 1.44269504f, bf = (dt * bf_lo(p_aim)) * 0.15915494f;
    const cpx cbv = cmul((cpx){bf_lo(p_cre), -bf_lo(p_cim)}, (cpx){dt * bf_lo(p_bre), -dt * bf_lo(p_bim)});
    const cpx w1 = cpow_l(a2, -bf, 1.f), w8 = cpow_l(a2, bf, 8.f), w256 = cpow_l(a2, -bf, 256.f);
    const cpx w16 = cmul(w8, w8);
    const u32 swr = opaque(LDS_SCR + w * 4608 + l * 4), srd = opaque(LDS_SCR + w * 4608 + (l & 15) * 288 + (l >> 4) * 16);
    v8bf J[2][4], Hf[4];
    cpx e = {1.f, 0.f}, o = w8;
#pragma unroll
    for (int y = 0; y < 2; ++y) {
#pragma unroll
      for (int r = 0; r < 16; r += 2) {
        lds_st<u32>(swr + r * 288, pk_bf(e.x, e.y));
        lds_st<u32>(swr + (r + 1) * 288, pk_bf(o.x, o.y));
        if (y == 0 || r < 14) { e = (y == 0 && r == 0) ? w16 : cmul(e, w16); o = cmul(o, w16); }
      }
#pragma unroll
      for (int k = 0; k < 4; ++k) J[y][k] = as_frag(lds_ld<v4u>(srd + k * 64));
    }
    STAMP(12);
    cpx c[8];
    c[0] = cbv;
#pragma unroll
    for (int r = 1; r < 8; ++r) c[r] = cmul(c[r - 1], w1);
    cpx d0 = cmul(c[0], w256), d2 = cmul(c[2], w256), d4 = cmul(c[4], w256), d6 = cmul(c[6], w256);
    cpx e0 = cmul(d0, w256), e4 = cmul(d4, w256), f0 = cmul(e0, w256), f4 = cmul(e4, w256);
#pragma unroll
    for (int r = 0; r < 8; ++r) lds_st<u32>(swr + r * 288, pk_bf(c[r].x, c[r].y));
    lds_st<u32>(swr + 8 * 288, pk_bf(d0.x, d0.y));
    lds_st<u32>(swr + 9 * 288, pk_bf(d2.x, d2.y));
    lds_st<u32>(swr + 10 * 288, pk_bf(d4.x, d4.y));
    lds_st<u32>(swr + 11 * 288, pk_bf(d6.x, d6.y));
    lds_st<u32>(swr + 12 * 288, pk_bf(e0.x, e0.y));
    lds_st<u32>(swr + 13 * 288, pk_bf(e4.x, e4.y));
    lds_st<u32>(swr + 14 * 288, pk_bf(f0.x, f0.y));
    lds_st<u32>(swr + 15 * 288, pk_bf(f4.x, f4.y));
    v4f kc[2][2] = {};
#pragma unroll
    for (int k = 0; k < 4; ++k) Hf[k] = as_frag(lds_ld<v4u>(srd + k * 64));
#pragma unroll
    for (int k = 0; k < 4; ++k) {
      kc[0][0] = __builtin_amdgcn_mfma_f32_16x16x32_bf16(Hf[k], J[0][k], kc[0][0], 0, 0, 0);
      kc[0][1] = __builtin_amdgcn_mfma_f32_16x16x32_bf16(Hf[k], J[1][k], kc[0][1], 0, 0, 0);
    }
    cpx q = f0;
#pragma unroll
    for (int r = 0; r < 5; ++r) { q = cmul(q, w256); lds_st<u32>(swr + r * 288, pk_bf(q.x, q.y)); }
#pragma unroll
    for (int k = 0; k < 4; ++k) Hf[k] = as_frag(lds_ld<v4u>(srd + k * 64));
#pragma unroll
    for (int k = 0; k < 4; ++k) {
      kc[1][0] = __builtin_amdgcn_mfma_f32_16x16x32_bf16(Hf[k], J[0][k], kc[1][0], 0, 0, 0);
      kc[1][1] = __builtin_amdgcn_mfma_f32_16x16x32_bf16(Hf[k], J[1][k], kc[1][1], 0, 0, 0);
    }
#pragma unroll
    for (int t = 0; t < 4; ++t) lds_st<v4f>(LDS_SCR + w * 4608 + t * 1024 + l * 16, kc[t >> 1][t & 1]);
    STAMP(13);
    WAITL0(); BAR();
    const u32 pr = LDS_SCR + (w & 4) * 4608 + (w & 3) * 1024 + l * 16;
    v4f s = (lds_ld<v4f>(pr) + lds_ld<v4f>(pr + 4608)) + (lds_ld<v4f>(pr + 2 * 4608) + lds_ld<v4f>(pr + 3 * 4608));
    const int gq = l >> 4, j = (l & 15) + 16 * (w & 1);
    const u32 tb = LDS_TAB + (w >> 2) * 1344;
    const u32 p01 = pk_bf(s[0], s[1]), p23 = pk_bf(s[2], s[3]);
    int i0, i1, i2, i3;
    if (!(w & 2)) {
      i0 = gq < 2 ? 8 * j + 4 * gq : gq == 2 ? 256 + 4 * j : 384 + 2 * j;
      i1 = i0 + 1; i2 = i0 + (gq == 3 ? 64 : 2); i3 = i2 + 1;
    } else {
      i0 = gq == 0 ? 512 + j : (gq == 1 && j == 0) ? 640 : 650;
      i1 = gq == 0 ? 544 + j : 650; i2 = gq == 0 ? 576 + j : 650; i3 = gq == 0 ? 608 + j : 650;
    }
    lds_st<u16>(tb + 2 * i0, (u16)p01); lds_st<u16>(tb + 2 * i1, (u16)(p01 >> 16));
    lds_st<u16>(tb + 2 * i2, (u16)p23); lds_st<u16>(tb + 2 * i3, (u16)(p23 >> 16));
    if (l == 0) lds_st<u16>(tb + 2 * 641, (u16)0);
    WAITL0(); BAR();
  }
  stage(2);
  STAMP(2);

  v16f accg[2] = {}, accu[2] = {};
  u32 uflag = 0, gate4[4];
  v4u ut4;
  {
    u32 xa[4];
#pragma unroll
    for (int s = 0; s < 4; ++s)
      xa[s] = (8 * wt + ((l & 31) >> 3)) * 1024 + (l & 7) * 128 + (((2 * s + (l >> 5)) ^ ((l >> 1) & 7)) * 16);
    const int G = l >> 4, q = (l & 15) >> 2, p = l & 3;
    const u32 wa = 16384 + (8 * (G >> 1) + q) * 256 + (((4 * wf + 2 * (G & 1) + (p >> 1)) ^ (4 * q)) * 16) + 8 * (p & 1);
    v8bf fg[2], fu[2], fx0[2], fx1[2];
    auto rd = [&](int buf, int st, int ks) {
      const u32 sb = (st % 3) * SLOT;
      fg[buf] = as_frag(lds_tr(sb + wa + ks * 4096), lds_tr(sb + wa + ks * 4096 + 1024));
      fx0[buf] = as_frag(lds_ld<v4u>(sb + xa[ks]));
      fu[buf] = as_frag(lds_tr(sb + wa + ks * 4096 + 16384), lds_tr(sb + wa + ks * 4096 + 16384 + 1024));
      fx1[buf] = as_frag(lds_ld<v4u>(sb + xa[ks] + 4096));
    };
    auto mm = [&](int buf) {
      accg[0] = __builtin_amdgcn_mfma_f32_32x32x16_bf16(fg[buf], fx0[buf], accg[0], 0, 0, 0);
      accu[0] = __builtin_amdgcn_mfma_f32_32x32x16_bf16(fu[buf], fx0[buf], accu[0], 0, 0, 0);
      accg[1] = __builtin_amdgcn_mfma_f32_32x32x16_bf16(fg[buf], fx1[buf], accg[1], 0, 0, 0);
      accu[1] = __builtin_amdgcn_mfma_f32_32x32x16_bf16(fu[buf], fx1[buf], accu[1], 0, 0, 0);
    };
    WAITV(12); BAR();
    STAMP(3);
    rd(0, 0, 0);
#pragma unroll
    for (int s = 0; s < 7; ++s) {
      SB0(); rd(1, s, 1); SB0(); mm(0);
      if (s == 5) {
        const int ch = tid >> 8, m = tid & 255;
        const u32 tb = LDS_TAB + ch * 1344;
        u32 v[11];
#pragma unroll
        for (int j = 0; j < 11; ++j) {
          const int sv = 2047 - 8 * m - j, oct = j < 8 ? 2040 - 8 * m : 2032 - 8 * m;
          const int sh = oct >= 1024 ? 3 : oct >= 512 ? 2 : oct >= 256 ? 1 : 0;
          const int r = (sv + (sh ? (1 << (sh - 1)) - 1 + ((sv >> sh) & 1) : 0)) >> sh;
          const int idx = sv < 0 ? 641 : r + 128 * sh;
          v[j] = lds_ld<u16>(tb + 2 * idx);
        }
        u32 E[5], O[5];
#pragma unroll
        for (int j = 0; j < 5; ++j) { E[j] = v[2 * j] | (v[2 * j + 1] << 16); O[j] = v[2 * j + 1] | (v[2 * j + 2] << 16); }
        const u32 kb = LDS_KIMG + ch * KCH + m * 16;
        lds_st<v4u>(kb + 3 * KCOPY, (v4u){E[0], E[1], E[2], E[3]});
        lds_st<v4u>(kb + 2 * KCOPY, (v4u){O[0], O[1], O[2], O[3]});
        lds_st<v4u>(kb + 1 * KCOPY, (v4u){E[1], E[2], E[3], E[4]});
        lds_st<v4u>(kb, (v4u){O[1], O[2], O[3], O[4]});
        if (tid < 16) lds_st<v4u>(LDS_KIMG + (tid >> 1) * KCOPY + 4096 + (tid & 1) * 16, (v4u){0, 0, 0, 0});
        if (tid >= 64 && tid < 128) lds_st<v4u>(LDS_UIMG + ((tid >> 5) & 1) * UCH + (tid & 31) * 16, (v4u){0, 0, 0, 0});
      }
      SB0(); rd(0, s, 2); SB0(); mm(1);
      SB0(); rd(1, s, 3); SB0(); mm(0);
      SB0();
      if (s < 6) {
        WAITL0();
        if (s < 5) WAITV(6); else WAITV(0);
        BAR();
        if (s + 3 < 7) stage(s + 3);
        if (s == 4) uflag = __hip_atomic_load((const gu32*)(ws + WS_FLAG_U + (l & 31) * 64), RLX_AGENT);
        if (s == 5) {
          wait_flags((const gu32*)(ws + WS_FLAG_U + (l & 31) * 64), tag, uflag, 2);
          __builtin_amdgcn_fence(__ATOMIC_ACQUIRE, "wavefront");
          ut4 = __builtin_amdgcn_raw_buffer_load_b128(rws, WS_UT + (2 * cb) * (L * 2) + tid * 16, 0, 16);
#pragma unroll
          for (int t = 0; t < 4; ++t)
            gate4[t] = __builtin_amdgcn_raw_buffer_load_b32(rws, WS_GATE + (64 * m2 + 16 * ((t + nt) & 3) + (tid >> 5)) * 4, 0, 16);
        }
        rd(0, s + 1, 0);
        SB0();
      }
      mm(1);
    }
  }
  STAMP(4);
  lds_st<v4u>(LDS_UIMG + (tid >> 8) * UCH + 512 + (tid & 255) * 16, ut4);

  {
    const u32 hb = WS_H + ((mt * 8 + 4 * wt + ((l & 31) >> 4)) * 56 + ft * 4 + wf) * 1024 + (l & 15) * 16 + (l >> 5) * 256;
#pragma unroll
    for (int j = 0; j < 2; ++j)
#pragma unroll
      for (int s = 0; s < 2; ++s) {
        v4u o;
#pragma unroll
        for (int e = 0; e < 4; ++e) {
          float h[2];
#pragma unroll
          for (int z = 0; z < 2; ++z) {
            const float gv = accg[j][8 * s + 2 * e + z], uv = accu[j][8 * s + 2 * e + z];
            h[z] = (gv * uv) * __builtin_amdgcn_rcpf(1.f + __builtin_amdgcn_exp2f(-1.44269504f * gv));
          }
          o[e] = pk_bf(h[0], h[1]);
        }
        __builtin_amdgcn_raw_buffer_store_b128(o, rws, hb + j * (2 * 56 * 1024) + s * 512, 0, 16);
      }
  }
  asm volatile("" ::: "memory");
  STAMP(5);
  const int kr = (w + 4 * (nt >> 2)) & 7;
  v2u wraw[7][8];
  {
    const rsrc_t rwd = mk_rsrc(g.wd, F * D * 2);
    const int gq = l >> 4;
    const u32 vo = (16 * (gq >> 1) + 4 * (gq & 1)) * 896 + (l & 15) * 8;
    const u32 so = kr * 7 * 32 * 896 + nt * 128;
#pragma unroll
    for (int i = 0; i < 7; ++i)
#pragma unroll
      for (int jj = 0; jj < 8; ++jj)
        wraw[i][jj] = __builtin_amdgcn_raw_buffer_load_b64(rwd, vo + (jj & 3) * 896, so + (32 * i + 8 * (jj >> 2)) * 896, 0);
  }
  asm volatile("" ::: "memory");
  WAITL0(); BAR();
  STAMP(6);

  {
    const int ch = w >> 2, hs = w & 1, z = (w >> 1) & 1;
    const int a = l & 15, gq = l >> 4;
    u32 ka = LDS_KIMG + ch * KCH + (a & 3) * KCOPY + 2 * (2028 + 8 * gq - 4 * (a >> 2)) - 512 * 7 - 256 * z;
    u32 ub = LDS_UIMG + ch * UCH + 2 * (240 + 16 * a + 8 * gq) - 256 * z;
    v4f acc[4] = {};
    auto run = [&](auto hset) {
#pragma unroll 1
      for (int q = 0; q < 4; ++q) {
        conv_q<decltype(hset)::value>(opaque(ka), opaque(ka + 8), opaque(ub), acc);
        ka -= 64; ub -= 64;
        if (q == 1) {
          WAITV(56); BAR();
          if (tid == 0) __hip_atomic_store((gu32*)(ws + WS_FLAG_H + (mt * 14 + ft) * 64), tag, RLX_AGENT);
        }
      }
    };
    if (hs == 0) run(ic<0>{}); else run(ic<1>{});
    const u32 yb = LDS_Y + (z * 2 + ch) * 8192 + (16 * a + 4 * (gq ^ ((a >> 1) & 3))) * 4;
#pragma unroll
    for (int i = 0; i < 4; ++i) {
      const int c = hs == 0 ? (i == 0 ? 7 : i == 1 ? 4 : i == 2 ? 3 : 0) : (i == 0 ? 6 : i == 1 ? 5 : i == 2 ? 2 : 1);
      lds_st<v4f>(yb + c * 1024, acc[i]);
    }
  }
  WAITL0(); BAR();
  STAMP(7);
  u32 hflag = tag;
  const gu32* hfp = (const gu32*)(ws + WS_FLAG_H + ((m2 >> 1) * 14 + (l < 14 ? l : 0)) * 64);
  if (w == 0) hflag = __hip_atomic_load(hfp, RLX_AGENT);
  {
    const u32 yo = LDS_Y + (tid >> 2) * 64 + (((tid & 3) ^ ((tid >> 3) & 3)) * 16);
    const v4f y0 = lds_ld<v4f>(yo) + lds_ld<v4f>(yo + 16384), y1 = lds_ld<v4f>(yo + 8192) + lds_ld<v4f>(yo + 24576);
    const v2u u0 = lds_ld<v2u>(LDS_UIMG + 512 + tid * 8), u1 = lds_ld<v2u>(LDS_UIMG + UCH + 512 + tid * 8);
    const float k0 = bf_lo(dsk2), k1 = bf_hi(dsk2);
    v4u o;
#pragma unroll
    for (int e = 0; e < 4; ++e) {
      const float a0 = (e & 1) ? bf_hi(u0[e >> 1]) : bf_lo(u0[e >> 1]), a1 = (e & 1) ? bf_hi(u1[e >> 1]) : bf_lo(u1[e >> 1]);
      const u32 yy = pk_bf(y0[e], y1[e]), sk = pk_bf(a0 * k0, a1 * k1);
      o[e] = pk_bf(bf_lo(yy) + bf_lo(sk), bf_hi(yy) + bf_hi(sk));
    }
    __builtin_amdgcn_raw_buffer_store_b128(o, rws, WS_A2 + cb * 8192 + tid * 16, 0, 16);
  }
  if (w == 0) wait_flags(hfp, tag, hflag, 3);
  WAITL0(); BAR();
  __builtin_amdgcn_fence(__ATOMIC_ACQUIRE, "wavefront");
  STAMP(8);

  {
    v4u hf[2][7];
    const u32 hv = WS_H + kr * 7 * 1024 + l * 16;
    auto ldh = [&](int buf, int tt) {
      const u32 so = (m2 * 4 + ((tt + nt) & 3)) * (56 * 1024);
#pragma unroll
      for (int i = 0; i < 7; ++i) hf[buf][i] = __builtin_amdgcn_raw_buffer_load_b128(rws, hv + i * 1024, so, 16);
    };
    ldh(0, 0);
    ldh(1, 1);
    SB0();
    v8bf wfr[7][4];
#pragma unroll
    for (int i = 0; i < 7; ++i)
#pragma unroll
      for (int c = 0; c < 4; ++c) {
        v4u x;
#pragma unroll
        for (int m = 0; m < 4; ++m)
          x[m] = __builtin_amdgcn_perm(wraw[i][2 * m + 1][c >> 1], wraw[i][2 * m][c >> 1], (c & 1) ? 0x07060302u : 0x05040100u);
        wfr[i][c] = as_frag(x);
      }
    SB0();
    v2f bs[4];
    u32 aflag = 0;
    v4u a4;
    const gu32* afp = (const gu32*)(ws + WS_FLAG_A + (32 * nt + (l & 31)) * 64);
#pragma unroll
    for (int tt = 0; tt < 4; ++tt) {
      v4f acc[4] = {};
#pragma unroll
      for (int i = 0; i < 7; ++i)
#pragma unroll
        for (int c = 0; c < 4; ++c)
          acc[c] = __builtin_amdgcn_mfma_f32_16x16x32_bf16(wfr[i][c], as_frag(hf[tt & 1][i]), acc[c], 0, 0, 0);
      SB0();
      if (tt == 2) {
        wait_flags(afp, tag, aflag, 4);
        __builtin_amdgcn_fence(__ATOMIC_ACQUIRE, "wavefront");
        a4 = __builtin_amdgcn_raw_buffer_load_b128(rws, WS_A2 + (32 * nt + (tid >> 4)) * 8192 + (64 * m2 + 4 * (tid & 15)) * 4, 0, 16);
      }
      if (tt < 2) ldh(tt & 1, tt + 2);
      if (tt == 1) aflag = __hip_atomic_load(afp, RLX_AGENT);
      SB0();
      const u32 pw = LDS_P + (tt & 1) * PBUF + w * PWAVE + (l & 15) * PROW + (l >> 4) * 64;
#pragma unroll
      for (int r = 0; r < 4; ++r) lds_st<v4f>(pw + r * 16, (v4f){acc[0][r], acc[1][r], acc[2][r], acc[3][r]});
      if (tt == 3) lds_st<v4u>(LDS_AST + (tid >> 4) * 272 + (tid & 15) * 16, a4);
      WAITL0(); BAR();
      if (tt == 0 && tid == 0) __hip_atomic_store((gu32*)(ws + WS_FLAG_A + cb * 64), tag, RLX_AGENT);
      const u32 prd = LDS_P + (tt & 1) * PBUF + (tid >> 5) * PROW + (tid & 31) * 8;
      v2f s = lds_ld<v2f>(prd);
#pragma unroll
      for (int k = 1; k < 8; ++k) s += lds_ld<v2f>(prd + k * PWAVE);
      bs[tt] = s;
      if (tt == 0) STAMP(9);
    }
    STAMP(10);
    const rsrc_t ro = mk_rsrc(g.out, L * D * 2);
#pragma unroll
    for (int tt = 0; tt < 4; ++tt) {
      const int t = 16 * ((tt + nt) & 3) + (tid >> 5);
      const u32 av = lds_ld<u32>(LDS_AST + (tid & 31) * 272 + t * 4);
      const float gt = __uint_as_float(gate4[tt]), og = rbf(1.f - gt);
      const u32 bb = pk_bf(bs[tt].x, bs[tt].y);
      const u32 ga = pk_bf(gt * bf_lo(av), gt * bf_hi(av)), gb = pk_bf(og * bf_lo(bb), og * bf_hi(bb));
      __builtin_amdgcn_raw_buffer_store_b32(pk_bf(bf_lo(ga) + bf_lo(gb), bf_hi(ga) + bf_hi(gb)), ro,
                                            ((64 * m2 + t) * D + 64 * nt + 2 * (tid & 31)) * 2, 0, 0);
    }
  }
  WAITV(0); BAR(); if (w == 0) { __builtin_amdgcn_fence(__ATOMIC_RELEASE, "agent"); WAITV(0); }
  while (__builtin_amdgcn_s_memrealtime() - tstart < 4000) __builtin_amdgcn_s_sleep(2);
}

extern "C" void kernel_launch(void* const* d_in, const int* in_sizes, int n_in, void* d_out, int out_size, void* d_ws,
                              size_t ws_size, hipStream_t stream) {
  Args a;
  a.u = (const u16*)d_in[0];
  a.are = (const u16*)d_in[1]; a.aim = (const u16*)d_in[2];
  a.bre = (const u16*)d_in[3]; a.bim = (const u16*)d_in[4];
  a.cre = (const u16*)d_in[5]; a.cim = (const u16*)d_in[6];
  a.ldt = (const u16*)d_in[7]; a.dsk = (const u16*)d_in[8];
  a.wg = (const u16*)d_in[10]; a.wu = (const u16*)d_in[11]; a.wd = (const u16*)d_in[12];
  a.gw = (const u16*)d_in[13];
  a.out = (u16*)d_out;
  a.ws = (char*)d_ws;
  (void)hipFuncSetAttribute((const void*)gated_mix_kernel, hipFuncAttributeMaxDynamicSharedMemorySize, LDS_TOTAL);
  gated_mix_kernel<<<NMAIN + NHELP, 512, LDS_TOTAL, stream>>>(a);
}
```

```cpp
#include <hip/hip_runtime.h>

typedef unsigned short u16;
typedef unsigned u32;
typedef unsigned long long u64;
typedef u32 v4u __attribute__((ext_vector_type(4)));
typedef u32 v2u __attribute__((ext_vector_type(2)));
typedef float v2f __attribute__((ext_vector_type(2)));
typedef float v4f __attribute__((ext_vector_type(4)));
typedef float v16f __attribute__((ext_vector_type(16)));
typedef __bf16 v8bf __attribute__((ext_vector_type(8)));
typedef __bf16 v2bf __attribute__((ext_vector_type(2)));
typedef short v4s __attribute__((ext_vector_type(4)));
#define LAS __attribute__((address_space(3)))
#define GAS __attribute__((address_space(1)))
typedef GAS u32 gu32;
typedef __amdgpu_buffer_rsrc_t rsrc_t;

#define AFFINE 0

constexpr int L = 2048, D = 448, NS = 256, F = 1792;
constexpr int NMAIN = 224, NHELP = 32;

constexpr u32 WS_EPOCH = 0;
constexpr u32 WS_FLAG_H = 1024;
constexpr u32 WS_FLAG_A = WS_FLAG_H + 256 * 64;
constexpr u32 WS_FLAG_U = WS_FLAG_A + 256 * 64;
constexpr u32 WS_GATE = 65536;
constexpr u32 WS_UT = 131072;
constexpr u32 WS_A2 = WS_UT + D * L * 2;
constexpr u32 WS_H = WS_A2 + D * L * 2;
constexpr u32 WS_END = WS_H + L * F * 2;
constexpr u32 WS_STAMP = 16u << 20;
constexpr u32 TAG_STEP = 0x9E3779B9u;

constexpr u32 SLOT = 49152;
constexpr u32 LDS_TAB = 3 * SLOT;
constexpr u32 LDS_TOTAL = LDS_TAB + 2 * 672 * 2;
constexpr u32 LDS_SCR = 2 * SLOT;
constexpr u32 KCOPY = 4160, KCH = 4 * KCOPY;
constexpr u32 LDS_KIMG = SLOT;
constexpr u32 UCH = 512 + 4096;
constexpr u32 LDS_UIMG = LDS_KIMG + 2 * KCH;
constexpr u32 LDS_Y = 2 * SLOT;
constexpr u32 PROW = 272, PWAVE = 16 * PROW, PBUF = 8 * PWAVE;
constexpr u32 LDS_P = 0;
constexpr u32 LDS_AST = 2 * PBUF;

#define SB0() __builtin_amdgcn_sched_barrier(0)
#define WAITV(n) asm volatile("s_waitcnt vmcnt(" #n ")" ::: "memory")
#define WAITL0() asm volatile("s_waitcnt lgkmcnt(0)" ::: "memory")
#define BAR() __builtin_amdgcn_s_barrier()
#define RLX_AGENT __ATOMIC_RELAXED, __HIP_MEMORY_SCOPE_AGENT
constexpr int SPIN_CAP = 1 << 18;

#define STAMP(i) do { } while (0)

extern __shared__ __attribute__((aligned(16))) char smem[];

__device__ __forceinline__ float bf_lo(u32 x) { return __uint_as_float(x << 16); }
__device__ __forceinline__ float bf_hi(u32 x) { return __uint_as_float(x & 0xffff0000u); }
__device__ __forceinline__ u32 pk_bf(float lo, float hi) {
  v2f v = {lo, hi};
  return __builtin_bit_cast(u32, __builtin_convertvector(v, v2bf));
}
__device__ __forceinline__ float rbf(float x) { return bf_lo(pk_bf(x, x)); }
template <class T> __device__ __forceinline__ T lds_ld(u32 off) { return *(const LAS T*)((LAS char*)smem + off); }
template <class T> __device__ __forceinline__ void lds_st(u32 off, T v) { *(LAS T*)((LAS char*)smem + off) = v; }
__device__ __forceinline__ v2u lds_tr(u32 off) {
  return __builtin_bit_cast(v2u, __builtin_amdgcn_ds_read_tr16_b64_v4i16((LAS v4s*)((LAS char*)smem + off)));
}
__device__ __forceinline__ v8bf as_frag(v4u x) { return __builtin_bit_cast(v8bf, x); }
__device__ __forceinline__ v8bf as_frag(v2u a, v2u b) { v4u x = {a.x, a.y, b.x, b.y}; return __builtin_bit_cast(v8bf, x); }
__device__ __forceinline__ rsrc_t mk_rsrc(const void* p, u32 bytes) {
  return __builtin_amdgcn_make_buffer_rsrc((void*)p, (short)0, (int)bytes, 0x00020000);
}
__device__ __forceinline__ void dma16(rsrc_t r, u32 voff, u32 soff, u32 lds) {
  asm volatile("s_mov_b32 m0, %0\n\ts_nop 0\n\tbuffer_load_dwordx4 %1, %2, %3 offen lds"
               :: "s"(lds), "v"(voff), "s"(r), "s"(soff) : "memory");
}
typedef v2f cpx;
__device__ __forceinline__ cpx cmul(cpx a, cpx b) {
  cpx r;
  asm("v_pk_mul_f32 %0, %1, %2 op_sel:[0,0] op_sel_hi:[0,1]" : "=v"(r) : "v"(a), "v"(b));
  asm("v_pk_fma_f32 %0, %1, %2, %0 op_sel:[1,1,0] op_sel_hi:[1,0,1] neg_lo:[1,0,0]" : "+v"(r) : "v"(a), "v"(b));
  return r;
}
__device__ __forceinline__ u32 opaque(u32 x) { asm volatile("" : "+v"(x)); return x; }
__device__ __forceinline__ cpx cpow_l(float a2, float bf, float l) {
  float m = __builtin_amdgcn_exp2f(a2 * l), ph = __builtin_amdgcn_fractf(bf * l);
  return (cpx){m * __builtin_amdgcn_cosf(ph), m * __builtin_amdgcn_sinf(ph)};
}
__device__ __forceinline__ void wait_flags(const gu32* p, u32 tag, u32 first, int code = 0) {
  u32 v = first;
#pragma unroll 1
  for (int s = 0; s < SPIN_CAP && !__all(v == tag); ++s) {
    __builtin_amdgcn_s_sleep(1);
    v = __hip_atomic_load(p, RLX_AGENT);
  }
}

template <int V> struct ic { static constexpr int value = V; };
struct Args {
  const u16 *u, *are, *aim, *bre, *bim, *cre, *cim, *ldt, *dsk, *wg, *wu, *wd, *gw;
  u16* out;
  char* ws;
};

template <int HSET>
__device__ __forceinline__ void conv_q(u32 ka, u32 ka8, u32 ub, v4f (&acc)[4]) {
  constexpr int NP = HSET == 0 ? 8 : 7;
  constexpr int CS[4] = {HSET == 0 ? 7 : 6, HSET == 0 ? 4 : 5, HSET == 0 ? 3 : 2, HSET == 0 ? 0 : 1};
  v8bf A[NP], B[NP];
#pragma unroll
  for (int p = 0; p < NP; ++p) {
    A[p] = as_frag(lds_ld<v2u>(ka + 512 * (7 - p)), lds_ld<v2u>(ka8 + 512 * (7 - p)));
    B[NP - 1 - p] = as_frag(lds_ld<v4u>(ub + 512 * (NP - 1 - p)));
  }
#pragma unroll
  for (int p = 0; p < NP; ++p)
#pragma unroll
    for (int i = 0; i < 4; ++i)
      if (p <= CS[i]) acc[i] = __builtin_amdgcn_mfma_f32_16x16x32_bf16(A[p], B[CS[i] - p], acc[i], 0, 0, 0);
}

__global__ __launch_bounds__(512, 2) void gated_mix_kernel(Args g_) {
  u64 tstart; asm volatile("s_memrealtime %0\n\ts_waitcnt lgkmcnt(0)" : "=s"(tstart) :: "memory");
  SB0();
#if defined(__HIP_DEVICE_COMPILE__)
  const Args g = *(const __attribute__((address_space(4))) Args*)__builtin_amdgcn_kernarg_segment_ptr();
#else
  const Args g = g_;
#endif
  const int b = blockIdx.x, xcd = b & 7, bi = b >> 3;
  const int tid = threadIdx.x, l = tid & 63;
  if (b == 0 && tid == 0) __hip_atomic_store((GAS u64*)(g.ws + 512), tstart, RLX_AGENT);
  const int w = __builtin_amdgcn_readfirstlane(tid >> 6);
  char* ws = g.ws;
  const rsrc_t rws = mk_rsrc(ws, WS_END);
  asm volatile("" :: "s"(g.u), "s"(g.are), "s"(g.aim), "s"(g.bre), "s"(g.bim), "s"(g.cre), "s"(g.cim), "s"(g.ldt),
               "s"(g.dsk), "s"(g.wg), "s"(g.wu), "s"(g.wd), "s"(g.gw), "s"(g.out), "s"(g.ws));
  STAMP(0);

  if (bi >= 28) {
    const int hb = b - NMAIN, t0 = 64 * hb;
    const rsrc_t rx = mk_rsrc(g.u, L * D * 2), rwd = mk_rsrc(g.wd, F * D * 2), rgw = mk_rsrc(g.gw, D * 2);
#pragma unroll
    for (int j = 0; j < 7; ++j) dma16(rx, l * 16, t0 * 896 + (w * 7 + j) * 1024, (w * 7 + j) * 1024);
    const u32 epoch_h = __hip_atomic_load((const gu32*)(ws + WS_EPOCH), RLX_AGENT);
    v4u gwv[7], pf[7];
#pragma unroll
    for (int j = 0; j < 7; ++j) gwv[j] = __builtin_amdgcn_raw_buffer_load_b128(rgw, ((l & 7) + 8 * j) * 16, 0, 0);
#pragma unroll
    for (int j = 0; j < 7; ++j)
      pf[j] = __builtin_amdgcn_raw_buffer_load_b128(rwd, (w * 7 + j < 49) ? hb * 50176 + (w * 7 + j) * 1024 + l * 16 : 0x7ffffff0, 0, 0);
    WAITV(0);
    const u32 tag = __builtin_amdgcn_readfirstlane(epoch_h) + TAG_STEP;
#pragma unroll
    for (int j = 0; j < 7; ++j) asm volatile("" :: "v"(pf[j]));
    BAR();
    STAMP(1);
    {
      const int G = l >> 4, q = (l & 15) >> 2, p = l & 3;
#pragma unroll
      for (int j = 0; j < 7; ++j) {
        const int unit = w * 7 + j, dt = unit >> 1, hf = unit & 1;
        const u32 a = (8 * (G + 4 * hf) + q) * 896 + (16 * dt + 4 * p) * 2;
        v2u lo = lds_tr(a), hi = lds_tr(a + 4 * 896);
        v4u v = {lo.x, lo.y, hi.x, hi.y};
        __builtin_amdgcn_raw_buffer_store_b128(v, rws, WS_UT + ((16 * dt + (l & 15)) * L + t0 + 8 * (G + 4 * hf)) * 2, 0, 16);
      }
    }
    {
      const int r = 8 * w + (l >> 3);
      float s = 0.f;
#pragma unroll
      for (int j = 0; j < 7; ++j) {
        v4u xv = lds_ld<v4u>(r * 896 + ((l & 7) + 8 * j) * 16);
#pragma unroll
        for (int e = 0; e < 4; ++e) {
          u32 pr = pk_bf(bf_lo(xv[e]) * bf_lo(gwv[j][e]), bf_hi(xv[e]) * bf_hi(gwv[j][e]));
          s += bf_lo(pr) + bf_hi(pr);
        }
      }
      s += __shfl_xor(s, 1); s += __shfl_xor(s, 2); s += __shfl_xor(s, 4);
      s = rbf(s);
      float gt = rbf(__builtin_amdgcn_rcpf(1.f + __builtin_amdgcn_exp2f(-1.44269504f * s)));
      if ((l & 7) == 0) __hip_atomic_store((gu32*)(ws + WS_GATE) + t0 + r, __float_as_uint(gt), RLX_AGENT);
    }
    WAITV(0);
    BAR();
    if (tid == 0) __hip_atomic_store((gu32*)(ws + WS_FLAG_U + hb * 64), tag, RLX_AGENT);
    STAMP(2);
    if (hb == NHELP - 1) {
      if (w < 4) {
        const gu32* p = (const gu32*)(ws + (tid < NMAIN ? WS_FLAG_H + tid * 64 : WS_FLAG_U + (tid - NMAIN) * 64));
        wait_flags(p, tag, ~tag, 1);
      }
      BAR();
      if (tid == 0) __hip_atomic_store((gu32*)(ws + WS_EPOCH), tag, RLX_AGENT);
    }
    return;
  }

  const int mt = (xcd & 3) * 4 + (bi & 3), ft = (xcd >> 2) * 7 + (bi >> 2);
  const int rot = ((bi >> 2) + 2 * (bi & 3) + (xcd & 3)) % 7;
  const int cb = xcd * 28 + bi;
  const int m2 = 4 * xcd + (bi & 3), nt = bi >> 2;
  const int wt = w & 1, wf = w >> 1;

  u32 p_are, p_aim, p_bre, p_bim, p_cre, p_cim, ldt2, dsk_v, epoch_v;
  {
    const u32 po = ((2 * cb + (w >> 2)) * NS + 64 * (w & 3) + l) * 2;
    asm volatile("global_load_ushort %0, %1, %2" : "=v"(p_are) : "v"(po), "s"(g.are) : "memory");
    asm volatile("global_load_ushort %0, %1, %2" : "=v"(p_aim) : "v"(po), "s"(g.aim) : "memory");
    asm volatile("global_load_ushort %0, %1, %2" : "=v"(p_bre) : "v"(po), "s"(g.bre) : "memory");
    asm volatile("global_load_ushort %0, %1, %2" : "=v"(p_bim) : "v"(po), "s"(g.bim) : "memory");
    asm volatile("global_load_ushort %0, %1, %2" : "=v"(p_cre) : "v"(po), "s"(g.cre) : "memory");
    asm volatile("global_load_ushort %0, %1, %2" : "=v"(p_cim) : "v"(po), "s"(g.cim) : "memory");
    asm volatile("global_load_dword %0, %1, %2" : "=v"(ldt2) : "v"(cb * 4), "s"(g.ldt) : "memory");
    asm volatile("global_load_dword %0, %1, %2" : "=v"(dsk_v) : "v"(cb * 4), "s"(g.dsk) : "memory");
    asm volatile("global_load_dword %0, %1, %2 sc1" : "=v"(epoch_v) : "v"(WS_EPOCH), "s"(ws) : "memory");
  }
  const rsrc_t rx = mk_rsrc(g.u, L * D * 2), rwg = mk_rsrc(g.wg, D * F * 2), rwu = mk_rsrc(g.wu, D * F * 2);
  const u32 vx0 = (l >> 3) * 896 + (((l & 7) ^ (l >> 4)) * 16);
  const u32 vx1 = (8 + (l >> 3)) * 896 + (((l & 7) ^ (4 + (l >> 4))) * 16);
  const u32 vw = (l >> 4) * (F * 2) + (((l & 15) ^ (4 * (l >> 4))) * 16);
  const u32 sx = (mt * 128 + 16 * w) * 896, sw = 8 * w * (F * 2) + ft * 256;
  auto stage = [&](int s) {
    int sk = s + rot; sk = sk >= 7 ? sk - 7 : sk;
    const u32 base = (s % 3) * SLOT + w * 2048;
    dma16(rx, vx0, sx + sk * 128, base);
    dma16(rx, vx1, sx + sk * 128, base + 1024);
    dma16(rwg, vw, sw + sk * 64 * (F * 2), base + 16384);
    dma16(rwg, vw, sw + (sk * 64 + 4) * (F * 2), base + 16384 + 1024);
    dma16(rwu, vw, sw + sk * 64 * (F * 2), base + 32768);
    dma16(rwu, vw, sw + (sk * 64 + 4) * (F * 2), base + 32768 + 1024);
  };
  stage(0);
  stage(1);
  asm volatile("s_waitcnt vmcnt(12)" : "+v"(p_are), "+v"(p_aim), "+v"(p_bre), "+v"(p_bim), "+v"(p_cre), "+v"(p_cim),
               "+v"(ldt2), "+v"(dsk_v), "+v"(epoch_v) :: "memory");
  const u32 tag = __builtin_amdgcn_readfirstlane(epoch_v) + TAG_STEP, dsk2 = __builtin_amdgcn_readfirstlane(dsk_v);
  STAMP(1);

  {
    const float ld = (w >> 2) ? bf_hi(ldt2) : bf_lo(ldt2);
    const float dt = rbf(__builtin_amdgcn_exp2f(ld * 1.44269504f));
    const float a2 = (dt * bf_lo(p_are)) * 1.44269504f, bf = (dt * bf_lo(p_aim)) * 0.15915494f;
    const cpx cbv = cmul((cpx){bf_lo(p_cre), -bf_lo(p_cim)}, (cpx){dt * bf_lo(p_bre), -dt * bf_lo(p_bim)});
    const cpx w1 = cpow_l(a2, -bf, 1.f), w8 = cpow_l(a2, bf, 8.f), w256 = cpow_l(a2, -bf, 256.f);
    const cpx w16 = cmul(w8, w8);
    const u32 swr = opaque(LDS_SCR + w * 4608 + l * 4), srd = opaque(LDS_SCR + w * 4608 + (l & 15) * 288 + (l >> 4) * 16);
    v8bf J[2][4], Hf[4];
    cpx e = {1.f, 0.f}, o = w8;
#pragma unroll
    for (int y = 0; y < 2; ++y) {
#pragma unroll
      for (int r = 0; r < 16; r += 2) {
        lds_st<u32>(swr + r * 288, pk_bf(e.x, e.y));
        lds_st<u32>(swr + (r + 1) * 288, pk_bf(o.x, o.y));
        if (y == 0 || r < 14) { e = (y == 0 && r == 0) ? w16 : cmul(e, w16); o = cmul(o, w16); }
      }
#pragma unroll
      for (int k = 0; k < 4; ++k) J[y][k] = as_frag(lds_ld<v4u>(srd + k * 64));
    }
    STAMP(12);
    cpx c[8];
    c[0] = cbv;
#pragma unroll
    for (int r = 1; r < 8; ++r) c[r] = cmul(c[r - 1], w1);
    cpx d0 = cmul(c[0], w256), d2 = cmul(c[2], w256), d4 = cmul(c[4], w256), d6 = cmul(c[6], w256);
    cpx e0 = cmul(d0, w256), e4 = cmul(d4, w256), f0 = cmul(e0, w256), f4 = cmul(e4, w256);
#pragma unroll
    for (int r = 0; r < 8; ++r) lds_st<u32>(swr + r * 288, pk_bf(c[r].x, c[r].y));
    lds_st<u32>(swr + 8 * 288, pk_bf(d0.x, d0.y));
    lds_st<u32>(swr + 9 * 288, pk_bf(d2.x, d2.y));
    lds_st<u32>(swr + 10 * 288, pk_bf(d4.x, d4.y));
    lds_st<u32>(swr + 11 * 288, pk_bf(d6.x, d6.y));
    lds_st<u32>(swr + 12 * 288, pk_bf(e0.x, e0.y));
    lds_st<u32>(swr + 13 * 288, pk_bf(e4.x, e4.y));
    lds_st<u32>(swr + 14 * 288, pk_bf(f0.x, f0.y));
    lds_st<u32>(swr + 15 * 288, pk_bf(f4.x, f4.y));
    v4f kc[2][2] = {};
#pragma unroll
    for (int k = 0; k < 4; ++k) Hf[k] = as_frag(lds_ld<v4u>(srd + k * 64));
#pragma unroll
    for (int k = 0; k < 4; ++k) {
      kc[0][0] = __builtin_amdgcn_mfma_f32_16x16x32_bf16(Hf[k], J[0][k], kc[0][0], 0, 0, 0);
      kc[0][1] = __builtin_amdgcn_mfma_f32_16x16x32_bf16(Hf[k], J[1][k], kc[0][1], 0, 0, 0);
    }
    cpx q = f0;
#pragma unroll
    for (int r = 0; r < 5; ++r) { q = cmul(q, w256); lds_st<u32>(swr + r * 288, pk_bf(q.x, q.y)); }
#pragma unroll
    for (int k = 0; k < 4; ++k) Hf[k] = as_frag(lds_ld<v4u>(srd + k * 64));
#pragma unroll
    for (int k = 0; k < 4; ++k) {
      kc[1][0] = __builtin_amdgcn_mfma_f32_16x16x32_bf16(Hf[k], J[0][k], kc[1][0], 0, 0, 0);
      kc[1][1] = __builtin_amdgcn_mfma_f32_16x16x32_bf16(Hf[k], J[1][k], kc[1][1], 0, 0, 0);
    }
#pragma unroll
    for (int t = 0; t < 4; ++t) lds_st<v4f>(LDS_SCR + w * 4608 + t * 1024 + l * 16, kc[t >> 1][t & 1]);
    STAMP(13);
    WAITL0(); BAR();
    const u32 pr = LDS_SCR + (w & 4) * 4608 + (w & 3) * 1024 + l * 16;
    v4f s = (lds_ld<v4f>(pr) + lds_ld<v4f>(pr + 4608)) + (lds_ld<v4f>(pr + 2 * 4608) + lds_ld<v4f>(pr + 3 * 4608));
    const int gq = l >> 4, j = (l & 15) + 16 * (w & 1);
    const u32 tb = LDS_TAB + (w >> 2) * 1344;
    const u32 p01 = pk_bf(s[0], s[1]), p23 = pk_bf(s[2], s[3]);
    int i0, i1, i2, i3;
    if (!(w & 2)) {
      i0 = gq < 2 ? 8 * j + 4 * gq : gq == 2 ? 256 + 4 * j : 384 + 2 * j;
      i1 = i0 + 1; i2 = i0 + (gq == 3 ? 64 : 2); i3 = i2 + 1;
    } else {
      i0 = gq == 0 ? 512 + j : (gq == 1 && j == 0) ? 640 : 650;
      i1 = gq == 0 ? 544 + j : 650; i2 = gq == 0 ? 576 + j : 650; i3 = gq == 0 ? 608 + j : 650;
    }
    lds_st<u16>(tb + 2 * i0, (u16)p01); lds_st<u16>(tb + 2 * i1, (u16)(p01 >> 16));
    lds_st<u16>(tb + 2 * i2, (u16)p23); lds_st<u16>(tb + 2 * i3, (u16)(p23 >> 16));
    if (l == 0) lds_st<u16>(tb + 2 * 641, (u16)0);
    WAITL0(); BAR();
  }
  stage(2);
  STAMP(2);

  v16f accg[2] = {}, accu[2] = {};
  u32 uflag = 0, gate4[4];
  v4u ut4;
  {
    u32 xa[4];
#pragma unroll
    for (int s = 0; s < 4; ++s)
      xa[s] = (8 * wt + ((l & 31) >> 3)) * 1024 + (l & 7) * 128 + (((2 * s + (l >> 5)) ^ ((l >> 1) & 7)) * 16);
    const int G = l >> 4, q = (l & 15) >> 2, p = l & 3;
    const u32 wa = 16384 + (8 * (G >> 1) + q) * 256 + (((4 * wf + 2 * (G & 1) + (p >> 1)) ^ (4 * q)) * 16) + 8 * (p & 1);
    v8bf fg[2], fu[2], fx0[2], fx1[2];
    auto rd = [&](int buf, int st, int ks) {
      const u32 sb = (st % 3) * SLOT;
      fg[buf] = as_frag(lds_tr(sb + wa + ks * 4096), lds_tr(sb + wa + ks * 4096 + 1024));
      fx0[buf] = as_frag(lds_ld<v4u>(sb + xa[ks]));
      fu[buf] = as_frag(lds_tr(sb + wa + ks * 4096 + 16384), lds_tr(sb + wa + ks * 4096 + 16384 + 1024));
      fx1[buf] = as_frag(lds_ld<v4u>(sb + xa[ks] + 4096));
    };
    auto mm = [&](int buf) {
      accg[0] = __builtin_amdgcn_mfma_f32_32x32x16_bf16(fg[buf], fx0[buf], accg[0], 0, 0, 0);
      accu[0] = __builtin_amdgcn_mfma_f32_32x32x16_bf16(fu[buf], fx0[buf], accu[0], 0, 0, 0);
      accg[1] = __builtin_amdgcn_mfma_f32_32x32x16_bf16(fg[buf], fx1[buf], accg[1], 0, 0, 0);
      accu[1] = __builtin_amdgcn_mfma_f32_32x32x16_bf16(fu[buf], fx1[buf], accu[1], 0, 0, 0);
    };
    WAITV(12); BAR();
    STAMP(3);
    rd(0, 0, 0);
#pragma unroll
    for (int s = 0; s < 7; ++s) {
      SB0(); rd(1, s, 1); SB0(); mm(0);
      if (s == 5) {
        const int ch = tid >> 8, m = tid & 255;
        const u32 tb = LDS_TAB + ch * 1344;
        u32 v[11];
#pragma unroll
        for (int j = 0; j < 11; ++j) {
          const int sv = 2047 - 8 * m - j, oct = j < 8 ? 2040 - 8 * m : 2032 - 8 * m;
          const int sh = oct >= 1024 ? 3 : oct >= 512 ? 2 : oct >= 256 ? 1 : 0;
          const int r = (sv + (sh ? (1 << (sh - 1)) - 1 + ((sv >> sh) & 1) : 0)) >> sh;
          const int idx = sv < 0 ? 641 : r + 128 * sh;
          v[j] = lds_ld<u16>(tb + 2 * idx);
        }
        u32 E[5], O[5];
#pragma unroll
        for (int j = 0; j < 5; ++j) { E[j] = v[2 * j] | (v[2 * j + 1] << 16); O[j] = v[2 * j + 1] | (v[2 * j + 2] << 16); }
        const u32 kb = LDS_KIMG + ch * KCH + m * 16;
        lds_st<v4u>(kb + 3 * KCOPY, (v4u){E[0], E[1], E[2], E[3]});
        lds_st<v4u>(kb + 2 * KCOPY, (v4u){O[0], O[1], O[2], O[3]});
        lds_st<v4u>(kb + 1 * KCOPY, (v4u){E[1], E[2], E[3], E[4]});
        lds_st<v4u>(kb, (v4u){O[1], O[2], O[3], O[4]});
        if (tid < 16) lds_st<v4u>(LDS_KIMG + (tid >> 1) * KCOPY + 4096 + (tid & 1) * 16, (v4u){0, 0, 0, 0});
        if (tid >= 64 && tid < 128) lds_st<v4u>(LDS_UIMG + ((tid >> 5) & 1) * UCH + (tid & 31) * 16, (v4u){0, 0, 0, 0});
      }
      SB0(); rd(0, s, 2); SB0(); mm(1);
      SB0(); rd(1, s, 3); SB0(); mm(0);
      SB0();
      if (s < 6) {
        WAITL0();
        if (s < 5) WAITV(6); else WAITV(0);
        BAR();
        if (s + 3 < 7) stage(s + 3);
        if (s == 4) uflag = __hip_atomic_load((const gu32*)(ws + WS_FLAG_U + (l & 31) * 64), RLX_AGENT);
        if (s == 5) {
          wait_flags((const gu32*)(ws + WS_FLAG_U + (l & 31) * 64), tag, uflag, 2);
          __builtin_amdgcn_fence(__ATOMIC_ACQUIRE, "wavefront");
          ut4 = __builtin_amdgcn_raw_buffer_load_b128(rws, WS_UT + (2 * cb) * (L * 2) + tid * 16, 0, 16);
#pragma unroll
          for (int t = 0; t < 4; ++t)
            gate4[t] = __builtin_amdgcn_raw_buffer_load_b32(rws, WS_GATE + (64 * m2 + 16 * ((t + nt) & 3) + (tid >> 5)) * 4, 0, 16);
        }
        rd(0, s + 1, 0);
        SB0();
      }
      mm(1);
    }
  }
  STAMP(4);
  lds_st<v4u>(LDS_UIMG + (tid >> 8) * UCH + 512 + (tid & 255) * 16, ut4);

  {
    const u32 hb = WS_H + ((mt * 8 + 4 * wt + ((l & 31) >> 4)) * 56 + ft * 4 + wf) * 1024 + (l & 15) * 16 + (l >> 5) * 256;
#pragma unroll
    for (int j = 0; j < 2; ++j)
#pragma unroll
      for (int s = 0; s < 2; ++s) {
        v4u o;
#pragma unroll
        for (int e = 0; e < 4; ++e) {
          float h[2];
#pragma unroll
          for (int z = 0; z < 2; ++z) {
            const float gv = accg[j][8 * s + 2 * e + z], uv = accu[j][8 * s + 2 * e + z];
            h[z] = (gv * uv) * __builtin_amdgcn_rcpf(1.f + __builtin_amdgcn_exp2f(-1.44269504f * gv));
          }
          o[e] = pk_bf(h[0], h[1]);
        }
        __builtin_amdgcn_raw_buffer_store_b128(o, rws, hb + j * (2 * 56 * 1024) + s * 512, 0, 16);
      }
  }
  asm volatile("" ::: "memory");
  STAMP(5);
  const int kr = (w + 4 * (nt >> 2)) & 7;
  v2u wraw[7][8];
  {
    const rsrc_t rwd = mk_rsrc(g.wd, F * D * 2);
    const int gq = l >> 4;
    const u32 vo = (16 * (gq >> 1) + 4 * (gq & 1)) * 896 + (l & 15) * 8;
    const u32 so = kr * 7 * 32 * 896 + nt * 128;
#pragma unroll
    for (int i = 0; i < 7; ++i)
#pragma unroll
      for (int jj = 0; jj < 8; ++jj)
        wraw[i][jj] = __builtin_amdgcn_raw_buffer_load_b64(rwd, vo + (jj & 3) * 896, so + (32 * i + 8 * (jj >> 2)) * 896, 0);
  }
  asm volatile("" ::: "memory");
  WAITL0(); BAR();
  STAMP(6);

  {
    const int ch = w >> 2, hs = w & 1, z = (w >> 1) & 1;
    const int a = l & 15, gq = l >> 4;
    u32 ka = LDS_KIMG + ch * KCH + (a & 3) * KCOPY + 2 * (2028 + 8 * gq - 4 * (a >> 2)) - 512 * 7 - 256 * z;
    u32 ub = LDS_UIMG + ch * UCH + 2 * (240 + 16 * a + 8 * gq) - 256 * z;
    v4f acc[4] = {};
    auto run = [&](auto hset) {
#pragma unroll 1
      for (int q = 0; q < 4; ++q) {
        conv_q<decltype(hset)::value>(opaque(ka), opaque(ka + 8), opaque(ub), acc);
        ka -= 64; ub -= 64;
        if (q == 1) {
          WAITV(56); BAR();
          if (tid == 0) __hip_atomic_store((gu32*)(ws + WS_FLAG_H + (mt * 14 + ft) * 64), tag, RLX_AGENT);
        }
      }
    };
    if (hs == 0) run(ic<0>{}); else run(ic<1>{});
    const u32 yb = LDS_Y + (z * 2 + ch) * 8192 + (16 * a + 4 * (gq ^ ((a >> 1) & 3))) * 4;
#pragma unroll
    for (int i = 0; i < 4; ++i) {
      const int c = hs == 0 ? (i == 0 ? 7 : i == 1 ? 4 : i == 2 ? 3 : 0) : (i == 0 ? 6 : i == 1 ? 5 : i == 2 ? 2 : 1);
      lds_st<v4f>(yb + c * 1024, acc[i]);
    }
  }
  WAITL0(); BAR();
  STAMP(7);
  u32 hflag = tag;
  const gu32* hfp = (const gu32*)(ws + WS_FLAG_H + ((m2 >> 1) * 14 + (l < 14 ? l : 0)) * 64);
  if (w == 0) hflag = __hip_atomic_load(hfp, RLX_AGENT);
  {
    const u32 yo = LDS_Y + (tid >> 2) * 64 + (((tid & 3) ^ ((tid >> 3) & 3)) * 16);
    const v4f y0 = lds_ld<v4f>(yo) + lds_ld<v4f>(yo + 16384), y1 = lds_ld<v4f>(yo + 8192) + lds_ld<v4f>(yo + 24576);
    const v2u u0 = lds_ld<v2u>(LDS_UIMG + 512 + tid * 8), u1 = lds_ld<v2u>(LDS_UIMG + UCH + 512 + tid * 8);
    const float k0 = bf_lo(dsk2), k1 = bf_hi(dsk2);
    v4u o;
#pragma unroll
    for (int e = 0; e < 4; ++e) {
      const float a0 = (e & 1) ? bf_hi(u0[e >> 1]) : bf_lo(u0[e >> 1]), a1 = (e & 1) ? bf_hi(u1[e >> 1]) : bf_lo(u1[e >> 1]);
      const u32 yy = pk_bf(y0[e], y1[e]), sk = pk_bf(a0 * k0, a1 * k1);
      o[e] = pk_bf(bf_lo(yy) + bf_lo(sk), bf_hi(yy) + bf_hi(sk));
    }
    __builtin_amdgcn_raw_buffer_store_b128(o, rws, WS_A2 + cb * 8192 + tid * 16, 0, 16);
  }
  if (w == 0) wait_flags(hfp, tag, hflag, 3);
  WAITL0(); BAR();
  __builtin_amdgcn_fence(__ATOMIC_ACQUIRE, "wavefront");
  STAMP(8);

  {
    v4u hf[2][7];
    const u32 hv = WS_H + kr * 7 * 1024 + l * 16;
    auto ldh = [&](int buf, int tt) {
      const u32 so = (m2 * 4 + ((tt + nt) & 3)) * (56 * 1024);
#pragma unroll
      for (int i = 0; i < 7; ++i) hf[buf][i] = __builtin_amdgcn_raw_buffer_load_b128(rws, hv + i * 1024, so, 16);
    };
    ldh(0, 0);
    ldh(1, 1);
    SB0();
    v8bf wfr[7][4];
#pragma unroll
    for (int i = 0; i < 7; ++i)
#pragma unroll
      for (int c = 0; c < 4; ++c) {
        v4u x;
#pragma unroll
        for (int m = 0; m < 4; ++m)
          x[m] = __builtin_amdgcn_perm(wraw[i][2 * m + 1][c >> 1], wraw[i][2 * m][c >> 1], (c & 1) ? 0x07060302u : 0x05040100u);
        wfr[i][c] = as_frag(x);
      }
    SB0();
    v2f bs[4];
    u32 aflag = 0;
    v4u a4;
    const gu32* afp = (const gu32*)(ws + WS_FLAG_A + (32 * nt + (l & 31)) * 64);
#pragma unroll
    for (int tt = 0; tt < 4; ++tt) {
      v4f acc[4] = {};
#pragma unroll
      for (int i = 0; i < 7; ++i)
#pragma unroll
        for (int c = 0; c < 4; ++c)
          acc[c] = __builtin_amdgcn_mfma_f32_16x16x32_bf16(wfr[i][c], as_frag(hf[tt & 1][i]), acc[c], 0, 0, 0);
      SB0();
      if (tt == 2) {
        wait_flags(afp, tag, aflag, 4);
        __builtin_amdgcn_fence(__ATOMIC_ACQUIRE, "wavefront");
        a4 = __builtin_amdgcn_raw_buffer_load_b128(rws, WS_A2 + (32 * nt + (tid >> 4)) * 8192 + (64 * m2 + 4 * (tid & 15)) * 4, 0, 16);
      }
      if (tt < 2) ldh(tt & 1, tt + 2);
      if (tt == 1) aflag = __hip_atomic_load(afp, RLX_AGENT);
      SB0();
      const u32 pw = LDS_P + (tt & 1) * PBUF + w * PWAVE + (l & 15) * PROW + (l >> 4) * 64;
#pragma unroll
      for (int r = 0; r < 4; ++r) lds_st<v4f>(pw + r * 16, (v4f){acc[0][r], acc[1][r], acc[2][r], acc[3][r]});
      if (tt == 3) lds_st<v4u>(LDS_AST + (tid >> 4) * 272 + (tid & 15) * 16, a4);
      WAITL0(); BAR();
      if (tt == 0 && tid == 0) __hip_atomic_store((gu32*)(ws + WS_FLAG_A + cb * 64), tag, RLX_AGENT);
      const u32 prd = LDS_P + (tt & 1) * PBUF + (tid >> 5) * PROW + (tid & 31) * 8;
      v2f s = lds_ld<v2f>(prd);
#pragma unroll
      for (int k = 1; k < 8; ++k) s += lds_ld<v2f>(prd + k * PWAVE);
      bs[tt] = s;
      if (tt == 0) STAMP(9);
    }
    STAMP(10);
    const rsrc_t ro = mk_rsrc(g.out, L * D * 2);
#pragma unroll
    for (int tt = 0; tt < 4; ++tt) {
      const int t = 16 * ((tt + nt) & 3) + (tid >> 5);
      const u32 av = lds_ld<u32>(LDS_AST + (tid & 31) * 272 + t * 4);
      const float gt = __uint_as_float(gate4[tt]), og = rbf(1.f - gt);
      const u32 bb = pk_bf(bs[tt].x, bs[tt].y);
      const u32 ga = pk_bf(gt * bf_lo(av), gt * bf_hi(av)), gb = pk_bf(og * bf_lo(bb), og * bf_hi(bb));
      __builtin_amdgcn_raw_buffer_store_b32(pk_bf(bf_lo(ga) + bf_lo(gb), bf_hi(ga) + bf_hi(gb)), ro,
                                            ((64 * m2 + t) * D + 64 * nt + 2 * (tid & 31)) * 2, 0, 0);
    }
  }
  tstart = __hip_atomic_load((GAS u64*)(g.ws + 512), RLX_AGENT);
  for (int it = 0; it < 100000 && __builtin_amdgcn_s_memrealtime() - tstart < 4000; ++it) __builtin_amdgcn_s_sleep(2);
}

extern "C" void kernel_launch(void* const* d_in, const int* in_sizes, int n_in, void* d_out, int out_size, void* d_ws,
                              size_t ws_size, hipStream_t stream) {
  Args a;
  a.u = (const u16*)d_in[0];
  a.are = (const u16*)d_in[1]; a.aim = (const u16*)d_in[2];
  a.bre = (const u16*)d_in[3]; a.bim = (const u16*)d_in[4];
  a.cre = (const u16*)d_in[5]; a.cim = (const u16*)d_in[6];
  a.ldt = (const u16*)d_in[7]; a.dsk = (const u16*)d_in[8];
  a.wg = (const u16*)d_in[10]; a.wu = (const u16*)d_in[11]; a.wd = (const u16*)d_in[12];
  a.gw = (const u16*)d_in[13];
  a.out = (u16*)d_out;
  a.ws = (char*)d_ws;
  (void)hipFuncSetAttribute((const void*)gated_mix_kernel, hipFuncAttributeMaxDynamicSharedMemorySize, LDS_TOTAL);
  gated_mix_kernel<<<NMAIN + NHELP, 512, LDS_TOTAL, stream>>>(a);
}
```

```cpp
#include <hip/hip_runtime.h>

typedef unsigned short u16;
typedef unsigned u32;
typedef unsigned long long u64;
typedef u32 v4u __attribute__((ext_vector_type(4)));
typedef u32 v2u __attribute__((ext_vector_type(2)));
typedef float v2f __attribute__((ext_vector_type(2)));
typedef float v4f __attribute__((ext_vector_type(4)));
typedef float v16f __attribute__((ext_vector_type(16)));
typedef __bf16 v8bf __attribute__((ext_vector_type(8)));
typedef __bf16 v2bf __attribute__((ext_vector_type(2)));
typedef short v4s __attribute__((ext_vector_type(4)));
#define LAS __attribute__((address_space(3)))
#define GAS __attribute__((address_space(1)))
typedef GAS u32 gu32;
typedef __amdgpu_buffer_rsrc_t rsrc_t;

#define AFFINE 0

constexpr int L = 2048, D = 448, NS = 256, F = 1792;
constexpr int NMAIN = 224, NHELP = 32;

constexpr u32 WS_EPOCH = 0;
constexpr u32 WS_FLAG_H = 1024;
constexpr u32 WS_FLAG_A = WS_FLAG_H + 256 * 64;
constexpr u32 WS_FLAG_U = WS_FLAG_A + 256 * 64;
constexpr u32 WS_GATE = 65536;
constexpr u32 WS_UT = 131072;
constexpr u32 WS_A2 = WS_UT + D * L * 2;
constexpr u32 WS_H = WS_A2 + D * L * 2;
constexpr u32 WS_END = WS_H + L * F * 2;
constexpr u32 WS_STAMP = 16u << 20;
constexpr u32 TAG_STEP = 0x9E3779B9u;

constexpr u32 SLOT = 49152;
constexpr u32 LDS_TAB = 3 * SLOT;
constexpr u32 LDS_TOTAL = LDS_TAB + 2 * 672 * 2;
constexpr u32 LDS_SCR = 2 * SLOT;
constexpr u32 KCOPY = 4160, KCH = 4 * KCOPY;
constexpr u32 LDS_KIMG = SLOT;
constexpr u32 UCH = 512 + 4096;
constexpr u32 LDS_UIMG = LDS_KIMG + 2 * KCH;
constexpr u32 LDS_Y = 2 * SLOT;
constexpr u32 PROW = 272, PWAVE = 16 * PROW, PBUF = 8 * PWAVE;
constexpr u32 LDS_P = 0;
constexpr u32 LDS_AST = 2 * PBUF;

#define SB0() __builtin_amdgcn_sched_barrier(0)
#define WAITV(n) asm volatile("s_waitcnt vmcnt(" #n ")" ::: "memory")
#define WAITL0() asm volatile("s_waitcnt lgkmcnt(0)" ::: "memory")
#define BAR() __builtin_amdgcn_s_barrier()
#define RLX_AGENT __ATOMIC_RELAXED, __HIP_MEMORY_SCOPE_AGENT
constexpr int SPIN_CAP = 1 << 18;

#define STAMP(i) do { } while (0)

extern __shared__ __attribute__((aligned(16))) char smem[];

__device__ __forceinline__ float bf_lo(u32 x) { return __uint_as_float(x << 16); }
__device__ __forceinline__ float bf_hi(u32 x) { return __uint_as_float(x & 0xffff0000u); }
__device__ __forceinline__ u32 pk_bf(float lo, float hi) {
  v2f v = {lo, hi};
  return __builtin_bit_cast(u32, __builtin_convertvector(v, v2bf));
}
__device__ __forceinline__ float rbf(float x) { return bf_lo(pk_bf(x, x)); }
template <class T> __device__ __forceinline__ T lds_ld(u32 off) { return *(const LAS T*)((LAS char*)smem + off); }
template <class T> __device__ __forceinline__ void lds_st(u32 off, T v) { *(LAS T*)((LAS char*)smem + off) = v; }
__device__ __forceinline__ v2u lds_tr(u32 off) {
  return __builtin_bit_cast(v2u, __builtin_amdgcn_ds_read_tr16_b64_v4i16((LAS v4s*)((LAS char*)smem + off)));
}
__device__ __forceinline__ v8bf as_frag(v4u x) { return __builtin_bit_cast(v8bf, x); }
__device__ __forceinline__ v8bf as_frag(v2u a, v2u b) { v4u x = {a.x, a.y, b.x, b.y}; return __builtin_bit_cast(v8bf, x); }
__device__ __forceinline__ rsrc_t mk_rsrc(const void* p, u32 bytes) {
  return __builtin_amdgcn_make_buffer_rsrc((void*)p, (short)0, (int)bytes, 0x00020000);
}
__device__ __forceinline__ void dma16(rsrc_t r, u32 voff, u32 soff, u32 lds) {
  asm volatile("s_mov_b32 m0, %0\n\ts_nop 0\n\tbuffer_load_dwordx4 %1, %2, %3 offen lds"
               :: "s"(lds), "v"(voff), "s"(r), "s"(soff) : "memory");
}
typedef v2f cpx;
__device__ __forceinline__ cpx cmul(cpx a, cpx b) {
  cpx r;
  asm("v_pk_mul_f32 %0, %1, %2 op_sel:[0,0] op_sel_hi:[0,1]" : "=v"(r) : "v"(a), "v"(b));
  asm("v_pk_fma_f32 %0, %1, %2, %0 op_sel:[1,1,0] op_sel_hi:[1,0,1] neg_lo:[1,0,0]" : "+v"(r) : "v"(a), "v"(b));
  return r;
}
__device__ __forceinline__ u32 opaque(u32 x) { asm volatile("" : "+v"(x)); return x; }
__device__ __forceinline__ cpx cpow_l(float a2, float bf, float l) {
  float m = __builtin_amdgcn_exp2f(a2 * l), ph = __builtin_amdgcn_fractf(bf * l);
  return (cpx){m * __builtin_amdgcn_cosf(ph), m * __builtin_amdgcn_sinf(ph)};
}
__device__ __forceinline__ void wait_flags(const gu32* p, u32 tag, u32 first, int code = 0) {
  u32 v = first;
#pragma unroll 1
  for (int s = 0; s < SPIN_CAP && !__all(v == tag); ++s) {
    __builtin_amdgcn_s_sleep(1);
    v = __hip_atomic_load(p, RLX_AGENT);
  }
}

template <int V> struct ic { static constexpr int value = V; };
struct Args {
  const u16 *u, *are, *aim, *bre, *bim, *cre, *cim, *ldt, *dsk, *wg, *wu, *wd, *gw;
  u16* out;
  char* ws;
};

template <int HSET>
__device__ __forceinline__ void conv_q(u32 ka, u32 ka8, u32 ub, v4f (&acc)[4]) {
  constexpr int NP = HSET == 0 ? 8 : 7;
  constexpr int CS[4] = {HSET == 0 ? 7 : 6, HSET == 0 ? 4 : 5, HSET == 0 ? 3 : 2, HSET == 0 ? 0 : 1};
  v8bf A[NP], B[NP];
#pragma unroll
  for (int p = 0; p < NP; ++p) {
    A[p] = as_frag(lds_ld<v2u>(ka + 512 * (7 - p)), lds_ld<v2u>(ka8 + 512 * (7 - p)));
    B[NP - 1 - p] = as_frag(lds_ld<v4u>(ub + 512 * (NP - 1 - p)));
  }
#pragma unroll
  for (int p = 0; p < NP; ++p)
#pragma unroll
    for (int i = 0; i < 4; ++i)
      if (p <= CS[i]) acc[i] = __builtin_amdgcn_mfma_f32_16x16x32_bf16(A[p], B[CS[i] - p], acc[i], 0, 0, 0);
}

__global__ __launch_bounds__(512, 2) void gated_mix_kernel(Args g_) {
  u64 tstart; asm volatile("s_memrealtime %0" : "=s"(tstart) :: "memory");
  SB0();
#if defined(__HIP_DEVICE_COMPILE__)
  const Args g = *(const __attribute__((address_space(4))) Args*)__builtin_amdgcn_kernarg_segment_ptr();
#else
  const Args g = g_;
#endif
  const int b = blockIdx.x, xcd = b & 7, bi = b >> 3;
  const int tid = threadIdx.x, l = tid & 63;
  const int w = __builtin_amdgcn_readfirstlane(tid >> 6);
  char* ws = g.ws;
  const rsrc_t rws = mk_rsrc(ws, WS_END);
  asm volatile("" :: "s"(g.u), "s"(g.are), "s"(g.aim), "s"(g.bre), "s"(g.bim), "s"(g.cre), "s"(g.cim), "s"(g.ldt),
               "s"(g.dsk), "s"(g.wg), "s"(g.wu), "s"(g.wd), "s"(g.gw), "s"(g.out), "s"(g.ws));
  STAMP(0);

  if (bi >= 28) {
    const int hb = b - NMAIN, t0 = 64 * hb;
    const rsrc_t rx = mk_rsrc(g.u, L * D * 2), rwd = mk_rsrc(g.wd, F * D * 2), rgw = mk_rsrc(g.gw, D * 2);
#pragma unroll
    for (int j = 0; j < 7; ++j) dma16(rx, l * 16, t0 * 896 + (w * 7 + j) * 1024, (w * 7 + j) * 1024);
    const u32 epoch_h = __hip_atomic_load((const gu32*)(ws + WS_EPOCH), RLX_AGENT);
    v4u gwv[7], pf[7];
#pragma unroll
    for (int j = 0; j < 7; ++j) gwv[j] = __builtin_amdgcn_raw_buffer_load_b128(rgw, ((l & 7) + 8 * j) * 16, 0, 0);
#pragma unroll
    for (int j = 0; j < 7; ++j)
      pf[j] = __builtin_amdgcn_raw_buffer_load_b128(rwd, (w * 7 + j < 49) ? hb * 50176 + (w * 7 + j) * 1024 + l * 16 : 0x7ffffff0, 0, 0);
    WAITV(0);
    const u32 tag = __builtin_amdgcn_readfirstlane(epoch_h) + TAG_STEP;
#pragma unroll
    for (int j = 0; j < 7; ++j) asm volatile("" :: "v"(pf[j]));
    BAR();
    STAMP(1);
    {
      const int G = l >> 4, q = (l & 15) >> 2, p = l & 3;
#pragma unroll
      for (int j = 0; j < 7; ++j) {
        const int unit = w * 7 + j, dt = unit >> 1, hf = unit & 1;
        const u32 a = (8 * (G + 4 * hf) + q) * 896 + (16 * dt + 4 * p) * 2;
        v2u lo = lds_tr(a), hi = lds_tr(a + 4 * 896);
        v4u v = {lo.x, lo.y, hi.x, hi.y};
        __builtin_amdgcn_raw_buffer_store_b128(v, rws, WS_UT + ((16 * dt + (l & 15)) * L + t0 + 8 * (G + 4 * hf)) * 2, 0, 16);
      }
    }
    {
      const int r = 8 * w + (l >> 3);
      float s = 0.f;
#pragma unroll
      for (int j = 0; j < 7; ++j) {
        v4u xv = lds_ld<v4u>(r * 896 + ((l & 7) + 8 * j) * 16);
#pragma unroll
        for (int e = 0; e < 4; ++e) {
          u32 pr = pk_bf(bf_lo(xv[e]) * bf_lo(gwv[j][e]), bf_hi(xv[e]) * bf_hi(gwv[j][e]));
          s += bf_lo(pr) + bf_hi(pr);
        }
      }
      s += __shfl_xor(s, 1); s += __shfl_xor(s, 2); s += __shfl_xor(s, 4);
      s = rbf(s);
      float gt = rbf(__builtin_amdgcn_rcpf(1.f + __builtin_amdgcn_exp2f(-1.44269504f * s)));
      if ((l & 7) == 0) __hip_atomic_store((gu32*)(ws + WS_GATE) + t0 + r, __float_as_uint(gt), RLX_AGENT);
    }
    WAITV(0);
    BAR();
    if (tid == 0) __hip_atomic_store((gu32*)(ws + WS_FLAG_U + hb * 64), tag, RLX_AGENT);
    STAMP(2);
    if (hb == NHELP - 1) {
      if (w < 4) {
        const gu32* p = (const gu32*)(ws + (tid < NMAIN ? WS_FLAG_H + tid * 64 : WS_FLAG_U + (tid - NMAIN) * 64));
        wait_flags(p, tag, ~tag, 1);
      }
      BAR();
      if (tid == 0) __hip_atomic_store((gu32*)(ws + WS_EPOCH), tag, RLX_AGENT);
    }
    return;
  }

  const int mt = (xcd & 3) * 4 + (bi & 3), ft = (xcd >> 2) * 7 + (bi >> 2);
  const int rot = ((bi >> 2) + 2 * (bi & 3) + (xcd & 3)) % 7;
  const int cb = xcd * 28 + bi;
  const int m2 = 4 * xcd + (bi & 3), nt = bi >> 2;
  const int wt = w & 1, wf = w >> 1;

  u32 p_are, p_aim, p_bre, p_bim, p_cre, p_cim, ldt2, dsk_v, epoch_v;
  {
    const u32 po = ((2 * cb + (w >> 2)) * NS + 64 * (w & 3) + l) * 2;
    asm volatile("global_load_ushort %0, %1, %2" : "=v"(p_are) : "v"(po), "s"(g.are) : "memory");
    asm volatile("global_load_ushort %0, %1, %2" : "=v"(p_aim) : "v"(po), "s"(g.aim) : "memory");
    asm volatile("global_load_ushort %0, %1, %2" : "=v"(p_bre) : "v"(po), "s"(g.bre) : "memory");
    asm volatile("global_load_ushort %0, %1, %2" : "=v"(p_bim) : "v"(po), "s"(g.bim) : "memory");
    asm volatile("global_load_ushort %0, %1, %2" : "=v"(p_cre) : "v"(po), "s"(g.cre) : "memory");
    asm volatile("global_load_ushort %0, %1, %2" : "=v"(p_cim) : "v"(po), "s"(g.cim) : "memory");
    asm volatile("global_load_dword %0, %1, %2" : "=v"(ldt2) : "v"(cb * 4), "s"(g.ldt) : "memory");
    asm volatile("global_load_dword %0, %1, %2" : "=v"(dsk_v) : "v"(cb * 4), "s"(g.dsk) : "memory");
    asm volatile("global_load_dword %0, %1, %2 sc1" : "=v"(epoch_v) : "v"(WS_EPOCH), "s"(ws) : "memory");
  }
  const rsrc_t rx = mk_rsrc(g.u, L * D * 2), rwg = mk_rsrc(g.wg, D * F * 2), rwu = mk_rsrc(g.wu, D * F * 2);
  const u32 vx0 = (l >> 3) * 896 + (((l & 7) ^ (l >> 4)) * 16);
  const u32 vx1 = (8 + (l >> 3)) * 896 + (((l & 7) ^ (4 + (l >> 4))) * 16);
  const u32 vw = (l >> 4) * (F * 2) + (((l & 15) ^ (4 * (l >> 4))) * 16);
  const u32 sx = (mt * 128 + 16 * w) * 896, sw = 8 * w * (F * 2) + ft * 256;
  auto stage = [&](int s) {
    int sk = s + rot; sk = sk >= 7 ? sk - 7 : sk;
    const u32 base = (s % 3) * SLOT + w * 2048;
    dma16(rx, vx0, sx + sk * 128, base);
    dma16(rx, vx1, sx + sk * 128, base + 1024);
    dma16(rwg, vw, sw + sk * 64 * (F * 2), base + 16384);
    dma16(rwg, vw, sw + (sk * 64 + 4) * (F * 2), base + 16384 + 1024);
    dma16(rwu, vw, sw + sk * 64 * (F * 2), base + 32768);
    dma16(rwu, vw, sw + (sk * 64 + 4) * (F * 2), base + 32768 + 1024);
  };
  stage(0);
  stage(1);
  asm volatile("s_waitcnt vmcnt(12)" : "+v"(p_are), "+v"(p_aim), "+v"(p_bre), "+v"(p_bim), "+v"(p_cre), "+v"(p_cim),
               "+v"(ldt2), "+v"(dsk_v), "+v"(epoch_v) :: "memory");
  const u32 tag = __builtin_amdgcn_readfirstlane(epoch_v) + TAG_STEP, dsk2 = __builtin_amdgcn_readfirstlane(dsk_v);
  STAMP(1);

  {
    const float ld = (w >> 2) ? bf_hi(ldt2) : bf_lo(ldt2);
    const float dt = rbf(__builtin_amdgcn_exp2f(ld * 1.44269504f));
    const float a2 = (dt * bf_lo(p_are)) * 1.44269504f, bf = (dt * bf_lo(p_aim)) * 0.15915494f;
    const cpx cbv = cmul((cpx){bf_lo(p_cre), -bf_lo(p_cim)}, (cpx){dt * bf_lo(p_bre), -dt * bf_lo(p_bim)});
    const cpx w1 = cpow_l(a2, -bf, 1.f), w8 = cpow_l(a2, bf, 8.f), w256 = cpow_l(a2, -bf, 256.f);
    const cpx w16 = cmul(w8, w8);
    const u32 swr = opaque(LDS_SCR + w * 4608 + l * 4), srd = opaque(LDS_SCR + w * 4608 + (l & 15) * 288 + (l >> 4) * 16);
    v8bf J[2][4], Hf[4];
    cpx e = {1.f, 0.f}, o = w8;
#pragma unroll
    for (int y = 0; y < 2; ++y) {
#pragma unroll
      for (int r = 0; r < 16; r += 2) {
        lds_st<u32>(swr + r * 288, pk_bf(e.x, e.y));
        lds_st<u32>(swr + (r + 1) * 288, pk_bf(o.x, o.y));
        if (y == 0 || r < 14) { e = (y == 0 && r == 0) ? w16 : cmul(e, w16); o = cmul(o, w16); }
      }
#pragma unroll
      for (int k = 0; k < 4; ++k) J[y][k] = as_frag(lds_ld<v4u>(srd + k * 64));
    }
    STAMP(12);
    cpx c[8];
    c[0] = cbv;
#pragma unroll
    for (int r = 1; r < 8; ++r) c[r] = cmul(c[r - 1], w1);
    cpx d0 = cmul(c[0], w256), d2 = cmul(c[2], w256), d4 = cmul(c[4], w256), d6 = cmul(c[6], w256);
    cpx e0 = cmul(d0, w256), e4 = cmul(d4, w256), f0 = cmul(e0, w256), f4 = cmul(e4, w256);
#pragma unroll
    for (int r = 0; r < 8; ++r) lds_st<u32>(swr + r * 288, pk_bf(c[r].x, c[r].y));
    lds_st<u32>(swr + 8 * 288, pk_bf(d0.x, d0.y));
    lds_st<u32>(swr + 9 * 288, pk_bf(d2.x, d2.y));
    lds_st<u32>(swr + 10 * 288, pk_bf(d4.x, d4.y));
    lds_st<u32>(swr + 11 * 288, pk_bf(d6.x, d6.y));
    lds_st<u32>(swr + 12 * 288, pk_bf(e0.x, e0.y));
    lds_st<u32>(swr + 13 * 288, pk_bf(e4.x, e4.y));
    lds_st<u32>(swr + 14 * 288, pk_bf(f0.x, f0.y));
    lds_st<u32>(swr + 15 * 288, pk_bf(f4.x, f4.y));
    v4f kc[2][2] = {};
#pragma unroll
    for (int k = 0; k < 4; ++k) Hf[k] = as_frag(lds_ld<v4u>(srd + k * 64));
#pragma unroll
    for (int k = 0; k < 4; ++k) {
      kc[0][0] = __builtin_amdgcn_mfma_f32_16x16x32_bf16(Hf[k], J[0][k], kc[0][0], 0, 0, 0);
      kc[0][1] = __builtin_amdgcn_mfma_f32_16x16x32_bf16(Hf[k], J[1][k], kc[0][1], 0, 0, 0);
    }
    cpx q = f0;
#pragma unroll
    for (int r = 0; r < 5; ++r) { q = cmul(q, w256); lds_st<u32>(swr + r * 288, pk_bf(q.x, q.y)); }
#pragma unroll
    for (int k = 0; k < 4; ++k) Hf[k] = as_frag(lds_ld<v4u>(srd + k * 64));
#pragma unroll
    for (int k = 0; k < 4; ++k) {
      kc[1][0] = __builtin_amdgcn_mfma_f32_16x16x32_bf16(Hf[k], J[0][k], kc[1][0], 0, 0, 0);
      kc[1][1] = __builtin_amdgcn_mfma_f32_16x16x32_bf16(Hf[k], J[1][k], kc[1][1], 0, 0, 0);
    }
#pragma unroll
    for (int t = 0; t < 4; ++t) lds_st<v4f>(LDS_SCR + w * 4608 + t * 1024 + l * 16, kc[t >> 1][t & 1]);
    STAMP(13);
    WAITL0(); BAR();
    const u32 pr = LDS_SCR + (w & 4) * 4608 + (w & 3) * 1024 + l * 16;
    v4f s = (lds_ld<v4f>(pr) + lds_ld<v4f>(pr + 4608)) + (lds_ld<v4f>(pr + 2 * 4608) + lds_ld<v4f>(pr + 3 * 4608));
    const int gq = l >> 4, j = (l & 15) + 16 * (w & 1);
    const u32 tb = LDS_TAB + (w >> 2) * 1344;
    const u32 p01 = pk_bf(s[0], s[1]), p23 = pk_bf(s[2], s[3]);
    int i0, i1, i2, i3;
    if (!(w & 2)) {
      i0 = gq < 2 ? 8 * j + 4 * gq : gq == 2 ? 256 + 4 * j : 384 + 2 * j;
      i1 = i0 + 1; i2 = i0 + (gq == 3 ? 64 : 2); i3 = i2 + 1;
    } else {
      i0 = gq == 0 ? 512 + j : (gq == 1 && j == 0) ? 640 : 650;
      i1 = gq == 0 ? 544 + j : 650; i2 = gq == 0 ? 576 + j : 650; i3 = gq == 0 ? 608 + j : 650;
    }
    lds_st<u16>(tb + 2 * i0, (u16)p01); lds_st<u16>(tb + 2 * i1, (u16)(p01 >> 16));
    lds_st<u16>(tb + 2 * i2, (u16)p23); lds_st<u16>(tb + 2 * i3, (u16)(p23 >> 16));
    if (l == 0) lds_st<u16>(tb + 2 * 641, (u16)0);
    WAITL0(); BAR();
  }
  stage(2);
  STAMP(2);

  v16f accg[2] = {}, accu[2] = {};
  u32 uflag = 0, gate4[4];
  v4u ut4;
  {
    u32 xa[4];
#pragma unroll
    for (int s = 0; s < 4; ++s)
      xa[s] = (8 * wt + ((l & 31) >> 3)) * 1024 + (l & 7) * 128 + (((2 * s + (l >> 5)) ^ ((l >> 1) & 7)) * 16);
    const int G = l >> 4, q = (l & 15) >> 2, p = l & 3;
    const u32 wa = 16384 + (8 * (G >> 1) + q) * 256 + (((4 * wf + 2 * (G & 1) + (p >> 1)) ^ (4 * q)) * 16) + 8 * (p & 1);
    v8bf fg[2], fu[2], fx0[2], fx1[2];
    auto rd = [&](int buf, int st, int ks) {
      const u32 sb = (st % 3) * SLOT;
      fg[buf] = as_frag(lds_tr(sb + wa + ks * 4096), lds_tr(sb + wa + ks * 4096 + 1024));
      fx0[buf] = as_frag(lds_ld<v4u>(sb + xa[ks]));
      fu[buf] = as_frag(lds_tr(sb + wa + ks * 4096 + 16384), lds_tr(sb + wa + ks * 4096 + 16384 + 1024));
      fx1[buf] = as_frag(lds_ld<v4u>(sb + xa[ks] + 4096));
    };
    auto mm = [&](int buf) {
      accg[0] = __builtin_amdgcn_mfma_f32_32x32x16_bf16(fg[buf], fx0[buf], accg[0], 0, 0, 0);
      accu[0] = __builtin_amdgcn_mfma_f32_32x32x16_bf16(fu[buf], fx0[buf], accu[0], 0, 0, 0);
      accg[1] = __builtin_amdgcn_mfma_f32_32x32x16_bf16(fg[buf], fx1[buf], accg[1], 0, 0, 0);
      accu[1] = __builtin_amdgcn_mfma_f32_32x32x16_bf16(fu[buf], fx1[buf], accu[1], 0, 0, 0);
    };
    WAITV(12); BAR();
    STAMP(3);
    rd(0, 0, 0);
#pragma unroll
    for (int s = 0; s < 7; ++s) {
      SB0(); rd(1, s, 1); SB0(); mm(0);
      if (s == 5) {
        const int ch = tid >> 8, m = tid & 255;
        const u32 tb = LDS_TAB + ch * 1344;
        u32 v[11];
#pragma unroll
        for (int j = 0; j < 11; ++j) {
          const int sv = 2047 - 8 * m - j, oct = j < 8 ? 2040 - 8 * m : 2032 - 8 * m;
          const int sh = oct >= 1024 ? 3 : oct >= 512 ? 2 : oct >= 256 ? 1 : 0;
          const int r = (sv + (sh ? (1 << (sh - 1)) - 1 + ((sv >> sh) & 1) : 0)) >> sh;
          const int idx = sv < 0 ? 641 : r + 128 * sh;
          v[j] = lds_ld<u16>(tb + 2 * idx);
        }
        u32 E[5], O[5];
#pragma unroll
        for (int j = 0; j < 5; ++j) { E[j] = v[2 * j] | (v[2 * j + 1] << 16); O[j] = v[2 * j + 1] | (v[2 * j + 2] << 16); }
        const u32 kb = LDS_KIMG + ch * KCH + m * 16;
        lds_st<v4u>(kb + 3 * KCOPY, (v4u){E[0], E[1], E[2], E[3]});
        lds_st<v4u>(kb + 2 * KCOPY, (v4u){O[0], O[1], O[2], O[3]});
        lds_st<v4u>(kb + 1 * KCOPY, (v4u){E[1], E[2], E[3], E[4]});
        lds_st<v4u>(kb, (v4u){O[1], O[2], O[3], O[4]});
        if (tid < 16) lds_st<v4u>(LDS_KIMG + (tid >> 1) * KCOPY + 4096 + (tid & 1) * 16, (v4u){0, 0, 0, 0});
        if (tid >= 64 && tid < 128) lds_st<v4u>(LDS_UIMG + ((tid >> 5) & 1) * UCH + (tid & 31) * 16, (v4u){0, 0, 0, 0});
      }
      SB0(); rd(0, s, 2); SB0(); mm(1);
      SB0(); rd(1, s, 3); SB0(); mm(0);
      SB0();
      if (s < 6) {
        WAITL0();
        if (s < 5) WAITV(6); else WAITV(0);
        BAR();
        if (s + 3 < 7) stage(s + 3);
        if (s == 4) uflag = __hip_atomic_load((const gu32*)(ws + WS_FLAG_U + (l & 31) * 64), RLX_AGENT);
        if (s == 5) {
          wait_flags((const gu32*)(ws + WS_FLAG_U + (l & 31) * 64), tag, uflag, 2);
          __builtin_amdgcn_fence(__ATOMIC_ACQUIRE, "wavefront");
          ut4 = __builtin_amdgcn_raw_buffer_load_b128(rws, WS_UT + (2 * cb) * (L * 2) + tid * 16, 0, 16);
#pragma unroll
          for (int t = 0; t < 4; ++t)
            gate4[t] = __builtin_amdgcn_raw_buffer_load_b32(rws, WS_GATE + (64 * m2 + 16 * ((t + nt) & 3) + (tid >> 5)) * 4, 0, 16);
        }
        rd(0, s + 1, 0);
        SB0();
      }
      mm(1);
    }
  }
  STAMP(4);
  lds_st<v4u>(LDS_UIMG + (tid >> 8) * UCH + 512 + (tid & 255) * 16, ut4);

  {
    const u32 hb = WS_H + ((mt * 8 + 4 * wt + ((l & 31) >> 4)) * 56 + ft * 4 + wf) * 1024 + (l & 15) * 16 + (l >> 5) * 256;
#pragma unroll
    for (int j = 0; j < 2; ++j)
#pragma unroll
      for (int s = 0; s < 2; ++s) {
        v4u o;
#pragma unroll
        for (int e = 0; e < 4; ++e) {
          float h[2];
#pragma unroll
          for (int z = 0; z < 2; ++z) {
            const float gv = accg[j][8 * s + 2 * e + z], uv = accu[j][8 * s + 2 * e + z];
            h[z] = (gv * uv) * __builtin_amdgcn_rcpf(1.f + __builtin_amdgcn_exp2f(-1.44269504f * gv));
          }
          o[e] = pk_bf(h[0], h[1]);
        }
        __builtin_amdgcn_raw_buffer_store_b128(o, rws, hb + j * (2 * 56 * 1024) + s * 512, 0, 16);
      }
  }
  asm volatile("" ::: "memory");
  STAMP(5);
  const int kr = (w + 4 * (nt >> 2)) & 7;
  v2u wraw[7][8];
  {
    const rsrc_t rwd = mk_rsrc(g.wd, F * D * 2);
    const int gq = l >> 4;
    const u32 vo = (16 * (gq >> 1) + 4 * (gq & 1)) * 896 + (l & 15) * 8;
    const u32 so = kr * 7 * 32 * 896 + nt * 128;
#pragma unroll
    for (int i = 0; i < 7; ++i)
#pragma unroll
      for (int jj = 0; jj < 8; ++jj)
        wraw[i][jj] = __builtin_amdgcn_raw_buffer_load_b64(rwd, vo + (jj & 3) * 896, so + (32 * i + 8 * (jj >> 2)) * 896, 0);
  }
  asm volatile("" ::: "memory");
  WAITL0(); BAR();
  STAMP(6);

  {
    const int ch = w >> 2, hs = w & 1, z = (w >> 1) & 1;
    const int a = l & 15, gq = l >> 4;
    u32 ka = LDS_KIMG + ch * KCH + (a & 3) * KCOPY + 2 * (2028 + 8 * gq - 4 * (a >> 2)) - 512 * 7 - 256 * z;
    u32 ub = LDS_UIMG + ch * UCH + 2 * (240 + 16 * a + 8 * gq) - 256 * z;
    v4f acc[4] = {};
    auto run = [&](auto hset) {
#pragma unroll 1
      for (int q = 0; q < 4; ++q) {
        conv_q<decltype(hset)::value>(opaque(ka), opaque(ka + 8), opaque(ub), acc);
        ka -= 64; ub -= 64;
        if (q == 1) {
          WAITV(56); BAR();
          if (tid == 0) __hip_atomic_store((gu32*)(ws + WS_FLAG_H + (mt * 14 + ft) * 64), tag, RLX_AGENT);
        }
      }
    };
    if (hs == 0) run(ic<0>{}); else run(ic<1>{});
    const u32 yb = LDS_Y + (z * 2 + ch) * 8192 + (16 * a + 4 * (gq ^ ((a >> 1) & 3))) * 4;
#pragma unroll
    for (int i = 0; i < 4; ++i) {
      const int c = hs == 0 ? (i == 0 ? 7 : i == 1 ? 4 : i == 2 ? 3 : 0) : (i == 0 ? 6 : i == 1 ? 5 : i == 2 ? 2 : 1);
      lds_st<v4f>(yb + c * 1024, acc[i]);
    }
  }
  WAITL0(); BAR();
  STAMP(7);
  u32 hflag = tag;
  const gu32* hfp = (const gu32*)(ws + WS_FLAG_H + ((m2 >> 1) * 14 + (l < 14 ? l : 0)) * 64);
  if (w == 0) hflag = __hip_atomic_load(hfp, RLX_AGENT);
  {
    const u32 yo = LDS_Y + (tid >> 2) * 64 + (((tid & 3) ^ ((tid >> 3) & 3)) * 16);
    const v4f y0 = lds_ld<v4f>(yo) + lds_ld<v4f>(yo + 16384), y1 = lds_ld<v4f>(yo + 8192) + lds_ld<v4f>(yo + 24576);
    const v2u u0 = lds_ld<v2u>(LDS_UIMG + 512 + tid * 8), u1 = lds_ld<v2u>(LDS_UIMG + UCH + 512 + tid * 8);
    const float k0 = bf_lo(dsk2), k1 = bf_hi(dsk2);
    v4u o;
#pragma unroll
    for (int e = 0; e < 4; ++e) {
      const float a0 = (e & 1) ? bf_hi(u0[e >> 1]) : bf_lo(u0[e >> 1]), a1 = (e & 1) ? bf_hi(u1[e >> 1]) : bf_lo(u1[e >> 1]);
      const u32 yy = pk_bf(y0[e], y1[e]), sk = pk_bf(a0 * k0, a1 * k1);
      o[e] = pk_bf(bf_lo(yy) + bf_lo(sk), bf_hi(yy) + bf_hi(sk));
    }
    __builtin_amdgcn_raw_buffer_store_b128(o, rws, WS_A2 + cb * 8192 + tid * 16, 0, 16);
  }
  if (w == 0) wait_flags(hfp, tag, hflag, 3);
  WAITL0(); BAR();
  __builtin_amdgcn_fence(__ATOMIC_ACQUIRE, "wavefront");
  STAMP(8);

  {
    v4u hf[2][7];
    const u32 hv = WS_H + kr * 7 * 1024 + l * 16;
    auto ldh = [&](int buf, int tt) {
      const u32 so = (m2 * 4 + ((tt + nt) & 3)) * (56 * 1024);
#pragma unroll
      for (int i = 0; i < 7; ++i) hf[buf][i] = __builtin_amdgcn_raw_buffer_load_b128(rws, hv + i * 1024, so, 16);
    };
    ldh(0, 0);
    ldh(1, 1);
    SB0();
    v8bf wfr[7][4];
#pragma unroll
    for (int i = 0; i < 7; ++i)
#pragma unroll
      for (int c = 0; c < 4; ++c) {
        v4u x;
#pragma unroll
        for (int m = 0; m < 4; ++m)
          x[m] = __builtin_amdgcn_perm(wraw[i][2 * m + 1][c >> 1], wraw[i][2 * m][c >> 1], (c & 1) ? 0x07060302u : 0x05040100u);
        wfr[i][c] = as_frag(x);
      }
    SB0();
    v2f bs[4];
    u32 aflag = 0;
    v4u a4;
    const gu32* afp = (const gu32*)(ws + WS_FLAG_A + (32 * nt + (l & 31)) * 64);
#pragma unroll
    for (int tt = 0; tt < 4; ++tt) {
      v4f acc[4] = {};
#pragma unroll
      for (int i = 0; i < 7; ++i)
#pragma unroll
        for (int c = 0; c < 4; ++c)
          acc[c] = __builtin_amdgcn_mfma_f32_16x16x32_bf16(wfr[i][c], as_frag(hf[tt & 1][i]), acc[c], 0, 0, 0);
      SB0();
      if (tt == 2) {
        wait_flags(afp, tag, aflag, 4);
        __builtin_amdgcn_fence(__ATOMIC_ACQUIRE, "wavefront");
        a4 = __builtin_amdgcn_raw_buffer_load_b128(rws, WS_A2 + (32 * nt + (tid >> 4)) * 8192 + (64 * m2 + 4 * (tid & 15)) * 4, 0, 16);
      }
      if (tt < 2) ldh(tt & 1, tt + 2);
      if (tt == 1) aflag = __hip_atomic_load(afp, RLX_AGENT);
      SB0();
      const u32 pw = LDS_P + (tt & 1) * PBUF + w * PWAVE + (l & 15) * PROW + (l >> 4) * 64;
#pragma unroll
      for (int r = 0; r < 4; ++r) lds_st<v4f>(pw + r * 16, (v4f){acc[0][r], acc[1][r], acc[2][r], acc[3][r]});
      if (tt == 3) lds_st<v4u>(LDS_AST + (tid >> 4) * 272 + (tid & 15) * 16, a4);
      WAITL0(); BAR();
      if (tt == 0 && tid == 0) __hip_atomic_store((gu32*)(ws + WS_FLAG_A + cb * 64), tag, RLX_AGENT);
      const u32 prd = LDS_P + (tt & 1) * PBUF + (tid >> 5) * PROW + (tid & 31) * 8;
      v2f s = lds_ld<v2f>(prd);
#pragma unroll
      for (int k = 1; k < 8; ++k) s += lds_ld<v2f>(prd + k * PWAVE);
      bs[tt] = s;
      if (tt == 0) STAMP(9);
    }
    STAMP(10);
    const rsrc_t ro = mk_rsrc(g.out, L * D * 2);
#pragma unroll
    for (int tt = 0; tt < 4; ++tt) {
      const int t = 16 * ((tt + nt) & 3) + (tid >> 5);
      const u32 av = lds_ld<u32>(LDS_AST + (tid & 31) * 272 + t * 4);
      const float gt = __uint_as_float(gate4[tt]), og = rbf(1.f - gt);
      const u32 bb = pk_bf(bs[tt].x, bs[tt].y);
      const u32 ga = pk_bf(gt * bf_lo(av), gt * bf_hi(av)), gb = pk_bf(og * bf_lo(bb), og * bf_hi(bb));
      __builtin_amdgcn_raw_buffer_store_b32(pk_bf(bf_lo(ga) + bf_lo(gb), bf_hi(ga) + bf_hi(gb)), ro,
                                            ((64 * m2 + t) * D + 64 * nt + 2 * (tid & 31)) * 2, 0, 0);
    }
  }
  while (__builtin_amdgcn_s_memrealtime() - tstart < 4000) __builtin_amdgcn_s_sleep(2);
}

__global__ __launch_bounds__(512, 2) void xk(char* p) { asm volatile("v_mov_b32 v250, 0" ::: "v250"); if (p == nullptr) __builtin_trap(); }
extern "C" void kernel_launch(void* const* d_in, const int* in_sizes, int n_in, void* d_out, int out_size, void* d_ws,
                              size_t ws_size, hipStream_t stream) {
  Args a;
  a.u = (const u16*)d_in[0];
  a.are = (const u16*)d_in[1]; a.aim = (const u16*)d_in[2];
  a.bre = (const u16*)d_in[3]; a.bim = (const u16*)d_in[4];
  a.cre = (const u16*)d_in[5]; a.cim = (const u16*)d_in[6];
  a.ldt = (const u16*)d_in[7]; a.dsk = (const u16*)d_in[8];
  a.wg = (const u16*)d_in[10]; a.wu = (const u16*)d_in[11]; a.wd = (const u16*)d_in[12];
  a.gw = (const u16*)d_in[13];
  a.out = (u16*)d_out;
  a.ws = (char*)d_ws;
  (void)hipFuncSetAttribute((const void*)gated_mix_kernel, hipFuncAttributeMaxDynamicSharedMemorySize, LDS_TOTAL);
  gated_mix_kernel<<<NMAIN + NHELP, 512, LDS_TOTAL, stream>>>(a);
  (void)hipFuncSetAttribute((const void*)xk, hipFuncAttributeMaxDynamicSharedMemorySize, LDS_TOTAL);
  xk<<<256,512,LDS_TOTAL,stream>>>((char*)d_ws);
}
```

```cpp
#include <hip/hip_runtime.h>

typedef unsigned short u16;
typedef unsigned u32;
typedef unsigned long long u64;
typedef u32 v4u __attribute__((ext_vector_type(4)));
typedef u32 v2u __attribute__((ext_vector_type(2)));
typedef float v2f __attribute__((ext_vector_type(2)));
typedef float v4f __attribute__((ext_vector_type(4)));
typedef float v16f __attribute__((ext_vector_type(16)));
typedef __bf16 v8bf __attribute__((ext_vector_type(8)));
typedef __bf16 v2bf __attribute__((ext_vector_type(2)));
typedef short v4s __attribute__((ext_vector_type(4)));
#define LAS __attribute__((address_space(3)))
#define GAS __attribute__((address_space(1)))
typedef GAS u32 gu32;
typedef __amdgpu_buffer_rsrc_t rsrc_t;

#define AFFINE 0

constexpr int L = 2048, D = 448, NS = 256, F = 1792;
constexpr int NMAIN = 224, NHELP = 32;

constexpr u32 WS_EPOCH = 0;
constexpr u32 WS_FLAG_W = 1024;
constexpr u32 WS_FLAG_G = WS_FLAG_W + 1024;
constexpr u32 WS_FLAG_U = WS_FLAG_G + 1024;
constexpr u32 WS_GATE = 8192;
#define HFS 32
constexpr u32 WS_FLAG_H = 16384;
constexpr u32 WS_UT = 262144;
constexpr u32 WS_A2 = WS_UT + D * L * 2;
constexpr u32 WS_H = WS_A2 + D * L * 4;
constexpr u32 WS_WT = WS_H + L * F * 2;
constexpr u32 WS_END = WS_WT + F * D * 2;
constexpr u32 WS_STAMP = 16u << 20;
constexpr u32 TAG_STEP = 0x9E3779B9u;

constexpr u32 LDS_X = 0;
constexpr u32 LDS_W = 32768;
constexpr u32 LDS_SCR = 131072;
constexpr u32 LDS_TOTAL = 163840;
constexpr u32 LDS_PAR = LDS_W + 2 * 32768 + 16384;
constexpr u32 LDS_WST = LDS_W + 2 * 32768;
constexpr u32 KCOPY = 4160, KCH = 4 * KCOPY;
constexpr u32 LDS_KIMG0 = LDS_W + 32768, LDS_KIMG1 = LDS_SCR + 4096;
constexpr u32 UCH = 512 + 4096;
constexpr u32 LDS_UIMG = LDS_KIMG0 + KCH;
constexpr u32 LDS_TAB = LDS_SCR;
constexpr u32 LDS_Y = 0;
constexpr u32 PROW = 272, PWAVE = 16 * PROW, PBUF = 8 * PWAVE;
constexpr u32 LDS_P = 0;
constexpr u32 LDS_AST = 98304;

#define SB0() __builtin_amdgcn_sched_barrier(0)
#define WAITV(n) asm volatile("s_waitcnt vmcnt(" #n ")" ::: "memory")
#define WAITL0() asm volatile("s_waitcnt lgkmcnt(0)" ::: "memory")
#define BAR() __builtin_amdgcn_s_barrier()
#define PRIO(n) __builtin_amdgcn_s_setprio(n)
#define RLX_AGENT __ATOMIC_RELAXED, __HIP_MEMORY_SCOPE_AGENT
constexpr int CODE_KB = 18;
constexpr int SPIN_CAP = 1 << 18;

#define STAMP(i) do { } while (0)
#define WST(i) do { } while (0)

extern __shared__ __attribute__((aligned(16))) char smem[];

__device__ __forceinline__ float bf_lo(u32 x) { return __uint_as_float(x << 16); }
__device__ __forceinline__ float bf_hi(u32 x) { return __uint_as_float(x & 0xffff0000u); }
__device__ __forceinline__ u32 pk_bf(float lo, float hi) {
  v2f v = {lo, hi};
  return __builtin_bit_cast(u32, __builtin_convertvector(v, v2bf));
}
__device__ __forceinline__ float dot2bf(u32 a, u32 b, float c) {
  return __builtin_amdgcn_fdot2_f32_bf16(__builtin_bit_cast(v2bf, a), __builtin_bit_cast(v2bf, b), c, false);
}
__device__ __forceinline__ float rbf(float x) { return bf_lo(pk_bf(x, x)); }
template <class T> __device__ __forceinline__ T lds_ld(u32 off) { return *(const LAS T*)((LAS char*)smem + off); }
template <class T> __device__ __forceinline__ void lds_st(u32 off, T v) { *(LAS T*)((LAS char*)smem + off) = v; }
__device__ __forceinline__ v2u lds_tr(u32 off) {
  return __builtin_bit_cast(v2u, __builtin_amdgcn_ds_read_tr16_b64_v4i16((LAS v4s*)((LAS char*)smem + off)));
}
__device__ __forceinline__ v8bf as_frag(v4u x) { return __builtin_bit_cast(v8bf, x); }
__device__ __forceinline__ v8bf as_frag(v2u a, v2u b) { v4u x = {a.x, a.y, b.x, b.y}; return __builtin_bit_cast(v8bf, x); }
__device__ __forceinline__ rsrc_t mk_rsrc(const void* p, u32 bytes) {
  return __builtin_amdgcn_make_buffer_rsrc((void*)p, (short)0, (int)bytes, 0x00020000);
}
template <int AUX> __device__ __forceinline__ void st16(v4u v, rsrc_t r, u32 voff) {
  __builtin_amdgcn_raw_buffer_store_b128(v, r, voff, 0, AUX);
}
__device__ __forceinline__ void dma16(rsrc_t r, u32 voff, u32 soff, u32 lds) {
  asm volatile("s_mov_b32 m0, %0\n\ts_nop 0\n\tbuffer_load_dwordx4 %1, %2, %3 offen lds"
               :: "s"(lds), "v"(voff), "s"(r), "s"(soff) : "memory");
}
template <int LO, int SO> __device__ __forceinline__ void dma16i(rsrc_t r, u32 voff, u32 soff, u32 lds) {
  u32 t;
  asm volatile("s_add_u32 m0, %1, %5\n\ts_add_u32 %0, %4, %6\n\tbuffer_load_dwordx4 %2, %3, %0 offen lds"
               : "=&s"(t) : "s"(lds), "v"(voff), "s"(r), "s"(soff), "i"(LO), "i"(SO) : "memory", "scc");
}
__device__ __forceinline__ void dma4_sc1(rsrc_t r, u32 voff, u32 soff, u32 lds) {
  asm volatile("s_mov_b32 m0, %0\n\ts_nop 0\n\tbuffer_load_dword %1, %2, %3 offen sc1 lds"
               :: "s"(lds), "v"(voff), "s"(r), "s"(soff) : "memory");
}
typedef v2f cpx;
__device__ __forceinline__ cpx cmul(cpx a, cpx b) {
  cpx r;
  asm volatile("v_pk_mul_f32 %0, %1, %2 op_sel:[0,0] op_sel_hi:[0,1]" : "=v"(r) : "v"(a), "v"(b));
  asm volatile("v_pk_fma_f32 %0, %1, %2, %0 op_sel:[1,1,0] op_sel_hi:[1,0,1] neg_lo:[1,0,0]" : "+v"(r) : "v"(a), "v"(b));
  return r;
}
#define CM_MUL(d, a, b) "v_pk_mul_f32 " d ", " a ", " b " op_sel:[0,0] op_sel_hi:[0,1]\n\t"
#define CM_FMA(d, a, b) "v_pk_fma_f32 " d ", " a ", " b ", " d " op_sel:[1,1,0] op_sel_hi:[1,0,1] neg_lo:[1,0,0]\n\t"
__device__ __forceinline__ void cmul4(cpx& r0, cpx& r1, cpx& r2, cpx& r3, cpx a0, cpx a1, cpx a2, cpx a3, cpx b) {
  asm volatile(CM_MUL("%0", "%4", "%8") CM_MUL("%1", "%5", "%8") CM_MUL("%2", "%6", "%8") CM_MUL("%3", "%7", "%8")
      CM_FMA("%0", "%4", "%8") CM_FMA("%1", "%5", "%8") CM_FMA("%2", "%6", "%8") CM_FMA("%3", "%7", "%8")
      : "=&v"(r0), "=&v"(r1), "=&v"(r2), "=&v"(r3) : "v"(a0), "v"(a1), "v"(a2), "v"(a3), "v"(b));
}
__device__ __forceinline__ void cmul2(cpx& r0, cpx& r1, cpx a0, cpx b0, cpx a1, cpx b1) {
  asm volatile(CM_MUL("%0", "%2", "%3") CM_MUL("%1", "%4", "%5") CM_FMA("%0", "%2", "%3") CM_FMA("%1", "%4", "%5")
      : "=&v"(r0), "=&v"(r1) : "v"(a0), "v"(b0), "v"(a1), "v"(b1));
}
__device__ __forceinline__ void cmul2s(cpx& r0, cpx& r1, cpx a0, cpx b0, cpx a1, cpx b1) {
  float x0, y0, x1, y1;
  asm volatile("v_mul_f32 %0, %4, %6\n\tv_mul_f32 %1, %4, %7\n\tv_mul_f32 %2, %8, %10\n\tv_mul_f32 %3, %8, %11\n\t"
               "v_fma_f32 %0, -%5, %7, %0\n\tv_fmac_f32 %1, %5, %6\n\tv_fma_f32 %2, -%9, %11, %2\n\tv_fmac_f32 %3, %9, %10"
               : "=&v"(x0), "=&v"(y0), "=&v"(x1), "=&v"(y1)
               : "v"(a0.x), "v"(a0.y), "v"(b0.x), "v"(b0.y), "v"(a1.x), "v"(a1.y), "v"(b1.x), "v"(b1.y));
  r0 = (cpx){x0, y0}; r1 = (cpx){x1, y1};
}
__device__ __forceinline__ cpx cmuls(cpx a, cpx b) {
  float x, y;
  asm volatile("v_mul_f32 %0, %2, %4\n\tv_mul_f32 %1, %2, %5\n\tv_fma_f32 %0, -%3, %5, %0\n\tv_fmac_f32 %1, %3, %4"
               : "=&v"(x), "=&v"(y) : "v"(a.x), "v"(a.y), "v"(b.x), "v"(b.y));
  return (cpx){x, y};
}
__device__ __forceinline__ v4f add4s(v4f a, v4f b) {
  float x, y, z, t;
  asm("v_add_f32 %0, %4, %8\n\tv_add_f32 %1, %5, %9\n\tv_add_f32 %2, %6, %10\n\tv_add_f32 %3, %7, %11"
      : "=&v"(x), "=&v"(y), "=&v"(z), "=&v"(t)
      : "v"(a.x), "v"(a.y), "v"(a.z), "v"(a.w), "v"(b.x), "v"(b.y), "v"(b.z), "v"(b.w));
  return (v4f){x, y, z, t};
}
template <int R> __device__ __forceinline__ void rows4(v4u& keep, u32 b0, u32 b1, cpx a, cpx b, cpx c, cpx d) {
  u32 t0, t1, t2, t3;
  asm volatile("v_cvt_pk_bf16_f32 %0, %6, %7\n\tv_cvt_pk_bf16_f32 %1, %8, %9\n\tv_cvt_pk_bf16_f32 %2, %10, %11\n\t"
               "v_cvt_pk_bf16_f32 %3, %12, %13\n\tds_write2st64_b32 %4, %0, %1 offset0:%18 offset1:%19\n\t"
               "ds_write2st64_b32 %5, %2, %3 offset0:%20 offset1:%21"
               : "=&v"(t0), "=&v"(t1), "=&v"(t2), "=&v"(t3)
               : "v"(b0), "v"(b1), "v"(a.x), "v"(a.y), "v"(b.x), "v"(b.y), "v"(c.x), "v"(c.y), "v"(d.x), "v"(d.y),
                 "v"(keep.x), "v"(keep.y), "v"(keep.z), "v"(keep.w), "i"(R), "i"(R + 1), "i"(R + 2), "i"(R + 3) : "memory");
  keep = (v4u){t0, t1, t2, t3};
}
__device__ __forceinline__ u32 opaque(u32 x) { asm volatile("" : "+v"(x)); return x; }
__device__ __forceinline__ cpx cpow_l(float a2, float bf, float l) {
  float m = __builtin_amdgcn_exp2f(a2 * l), ph = __builtin_amdgcn_fractf(bf * l);
  return (cpx){m * __builtin_amdgcn_cosf(ph), m * __builtin_amdgcn_sinf(ph)};
}
__device__ __forceinline__ void wait_flags(const gu32* p, u32 tag, u32 first, int code = 0) {
  if (__all(first == tag)) return;
  u32 v;
#pragma unroll 1
  for (int s = 0; s < SPIN_CAP; ++s) {
    v = __hip_atomic_load(p, RLX_AGENT);
    if (__all(v == tag)) break;
    __builtin_amdgcn_s_sleep(1);
  }
}

template <int V> struct ic { static constexpr int value = V; };
struct Args {
  const u16* par[8];
  const u16 *u, *wg, *wu, *wd, *gw;
  u16* out;
  char* ws;
};

__global__ __launch_bounds__(512, 2) void gated_mix_kernel(Args g) {
  u64 tstart; asm volatile("s_memrealtime %0\n\ts_waitcnt lgkmcnt(0)" : "=s"(tstart) :: "memory");
  const int b = blockIdx.x, xcd = b & 7, bi = b >> 3;
  const int tid = threadIdx.x, l = tid & 63;
  const int w = __builtin_amdgcn_readfirstlane(tid >> 6);
  asm volatile("s_cmp_ge_u32 %0, %3\n\ts_cbranch_scc1 1f\n\ts_getpc_b64 s[4:5]\n\ts_mov_b32 m0, %2\n\ts_and_b32 s4, s4, 0xffffff00\n\t"
               "global_load_lds_dwordx4 %1, s[4:5]\n1:"
               :: "s"(bi * 8 + w), "v"((bi * 8 + w) * 1024 + l * 16), "s"(LDS_SCR + w * 4096), "i"(CODE_KB) : "s4", "s5", "scc", "memory");
  const u64 par_w = ((const __attribute__((address_space(4))) u64*)__builtin_amdgcn_kernarg_segment_ptr())[w];
  asm volatile("" :: "s"(par_w), "s"(g.u), "s"(g.wg), "s"(g.wu), "s"(g.wd), "s"(g.gw), "s"(g.out), "s"(g.ws));
  char* ws = g.ws;
  const rsrc_t rws = mk_rsrc(ws, WS_END);
  const rsrc_t rx = mk_rsrc(g.u, L * D * 2);
  STAMP(0);

  if (bi >= 28) {
    const int hb = b - NMAIN, t0 = 64 * hb;
    constexpr u32 HX = 0, HG = 64 * 896;
    const rsrc_t rgw = mk_rsrc(g.gw, D * 2);
#pragma unroll
    for (int j = 0; j < 7; ++j) dma16(rx, l * 16, (t0 + 8 * w) * 896 + j * 1024, HX + 8 * w * 896 + j * 1024);
    if (w == 0) { dma4_sc1(rws, 0, WS_EPOCH, HG + 1024); dma16(rgw, l * 16, 0, HG); }
    WAITV(0); BAR();
    STAMP(1);
    const u32 tag = __builtin_amdgcn_readfirstlane(lds_ld<u32>(HG + 1024)) + TAG_STEP;
    {
      const int r = 8 * w + (l >> 3);
      const u32 xa = opaque(HX + r * 896 + (l & 7) * 16), ga = opaque(HG + (l & 7) * 16);
      float s0 = 0.f, s1 = 0.f;
#pragma unroll
      for (int j = 0; j < 7; ++j) {
        const v4u xv = lds_ld<v4u>(xa + 128 * j), gv = lds_ld<v4u>(ga + 128 * j);
#pragma unroll
        for (int e = 0; e < 4; e += 2) {
          s0 = dot2bf(xv[e], gv[e], s0);
          s1 = dot2bf(xv[e + 1], gv[e + 1], s1);
        }
      }
      float s = s0 + s1;
      s += __shfl_xor(s, 1); s += __shfl_xor(s, 2); s += __shfl_xor(s, 4);
      s = rbf(s);
      float gt = rbf(__builtin_amdgcn_rcpf(1.f + __builtin_amdgcn_exp2f(-1.44269504f * s)));
      if ((l & 7) == 0) __hip_atomic_store((gu32*)(ws + WS_GATE) + t0 + r, __float_as_uint(gt), RLX_AGENT);
    }
    WAITV(0); BAR();
    if (tid == 0) __hip_atomic_store((gu32*)(ws + WS_FLAG_G) + hb, tag, RLX_AGENT);
    STAMP(2);
    if (hb == NHELP - 1) {
      if (w < 4) wait_flags((const gu32*)(ws + (tid < NMAIN ? WS_FLAG_W : WS_FLAG_G - NMAIN * 4)) + tid, tag, ~tag, 1);
      BAR();
      if (tid == 0) __hip_atomic_store((gu32*)(ws + WS_EPOCH), tag, RLX_AGENT);
    }
    return;
  }

  const int cb = xcd * 28 + bi;
  const u32 vl16 = opaque(l * 16);
  {
    const rsrc_t rp = mk_rsrc((const void*)par_w, w < 6 ? D * NS * 2 : D * 2);
    dma16(rp, vl16, w < 6 ? cb * 1024 : (cb * 4) & ~15, LDS_PAR + w * 1024);
    if (w == 0) dma4_sc1(rws, 0, WS_EPOCH, LDS_PAR + 8192);
  }
  const int mt = (xcd & 3) * 4 + (bi & 3), ft = (xcd >> 2) * 7 + (bi >> 2);
  const int rot = ((bi >> 2) + 2 * (bi & 3) + (xcd & 3)) % 7;
  const int m2 = 4 * xcd + (bi & 3), nt = bi >> 2;
  const int wt = w & 1, wf = w >> 1;

  const rsrc_t rwg = mk_rsrc(g.wg, D * F * 2), rwu = mk_rsrc(g.wu, D * F * 2);
  const u32 vx0 = (l >> 3) * 896 + (((l & 7) ^ (l >> 4)) * 16), vx1 = (vx0 ^ 64) + 8 * 896;
  const u32 vw = (l >> 4) * (F * 2) + (((l & 15) ^ (4 * (l >> 4))) * 16);
  const u32 sx = (mt * 128 + 16 * w) * 896, sw = 8 * w * (F * 2) + ft * 256, lw = w * 2048;
  auto piece = [&](auto S, auto I) {
    constexpr int s = decltype(S)::value, i = decltype(I)::value;
    int sk = s + rot; sk = sk >= 7 ? sk - 7 : sk;
    if constexpr (i < 2) dma16i<LDS_X + (s & 1) * 16384 + i * 1024, 0>(rx, i ? vx1 : vx0, sx + sk * 128, lw);
    else dma16i<LDS_W + (s % 3) * 32768 + (i >= 4) * 16384 + (i & 1) * 1024, 4 * (i & 1) * F * 2>(i < 4 ? rwg : rwu, vw, sw + sk * (64 * F * 2), lw);
  };
#define PIECE(s, i) piece(ic<(s)>{}, ic<(i)>{})
  PIECE(0, 0); PIECE(0, 1); PIECE(0, 2); PIECE(0, 3); PIECE(0, 4); PIECE(0, 5);
  const int ct = 7 * (cb & 3) + (w < 7 ? w : 6);
  dma16(mk_rsrc(g.wd, F * D * 2), (l >> 1) * 896 + (l & 1) * 16, 32 * (cb >> 2) * 896 + ct * 32, LDS_WST + lw);
  WAITV(7); BAR();
  STAMP(1);

  const u32 srd0 = opaque(LDS_SCR + w * 4096 + (l & 15) * 256 + (((l >> 4) ^ ((l & 15) >> 1)) * 16)), srd1 = opaque(srd0 ^ 64);
  auto frags = [&](v8bf (&f)[4]) {
#pragma unroll
    for (int k = 0; k < 4; ++k) f[k] = as_frag(lds_ld<v4u>(((k & 1) ? srd1 : srd0) + (k >> 1) * 128));
  };
  u32 tag, dsk2;
  {
    cpx c0, w1, w2, w256, w512;
    u32 swr[8];
    v8bf J[2][4], Hf[4], Hg[4];
    v4u keep = {0, 0, 0, 0};
    const u32 po = LDS_PAR + (w >> 2) * 512 + (w & 3) * 128 + l * 2;
    const float ld = bf_lo(lds_ld<u16>(LDS_PAR + 6144 + ((cb * 4) & 15) + 2 * (w >> 2)));
    const u32 q0 = lds_ld<u16>(po), q1 = lds_ld<u16>(po + 1024), q2 = lds_ld<u16>(po + 2048), q3 = lds_ld<u16>(po + 3072);
    const u32 q4 = lds_ld<u16>(po + 4096), q5 = lds_ld<u16>(po + 5120);
    const u32 ep = lds_ld<u32>(LDS_PAR + 8192), dk = lds_ld<u32>(LDS_PAR + 7168 + ((cb * 4) & 15));
    swr[0] = opaque(LDS_SCR + w * 4096 + l * 4);
#pragma unroll
    for (int k = 1; k < 8; ++k) swr[k] = opaque(swr[0] ^ (16 * k));
    const float dt = rbf(__builtin_amdgcn_exp2f(ld * 1.44269504f));
    const float a2 = (dt * bf_lo(q0)) * 1.44269504f, bf = (dt * bf_lo(q1)) * 0.15915494f;
    const cpx w8 = cpow_l(a2, bf, 8.f);
    w1 = cpow_l(a2, -bf, 1.f); w256 = cpow_l(a2, -bf, 256.f);
    const cpx cc = {bf_lo(q4), -bf_lo(q5)}, bb = {dt * bf_lo(q2), -dt * bf_lo(q3)};
    tag = __builtin_amdgcn_readfirstlane(ep) + TAG_STEP; dsk2 = __builtin_amdgcn_readfirstlane(dk);
    cpx w16, w24, w32;
    cmul2(w16, c0, w8, w8, cc, bb);
    cmul2(w24, w32, w16, w8, w16, w16);
    cpx j0 = {1.f, 0.f}, j1 = w8, j2 = w16, j3 = w24;
#define JSTEP(k) rows4<((k) & 3) * 4>(keep, swr[((k) & 3) * 2], swr[((k) & 3) * 2 + 1], j0, j1, j2, j3)
#define JNEXT() cmul4(j0, j1, j2, j3, j0, j1, j2, j3, w32)
    JSTEP(0); JNEXT();
    JSTEP(1); JNEXT();
    JSTEP(2); JNEXT();
    JSTEP(3); frags(J[0]); JNEXT();
    JSTEP(4); JNEXT();
    JSTEP(5); JNEXT();
    JSTEP(6); JNEXT();
    cmul2(w2, w512, w1, w1, w256, w256);
    JSTEP(7); frags(J[1]);
    cpx c1, c2, c3, c4, c5, c6, c7, w4, d0, d2, d4, d6, e0, e4, f0, f4, g0, g1, g2, g3;
    cmul2(c1, c2, c0, w1, c0, w2);
    cmul2(c3, w4, c2, w1, w2, w2);
    cmul4(c4, c5, c6, c7, c0, c1, c2, c3, w4);
    rows4<0>(keep, swr[0], swr[1], c0, c1, c2, c3);
    cmul4(d0, d2, d4, d6, c0, c2, c4, c6, w256);
    rows4<4>(keep, swr[2], swr[3], c4, c5, c6, c7);
    cmul4(e0, e4, f0, f4, c0, c4, d0, d4, w512);
    rows4<8>(keep, swr[4], swr[5], d0, d2, d4, d6);
    cmul2(g0, g1, e0, w512, f0, w512);
    rows4<12>(keep, swr[6], swr[7], e0, e4, f0, f4);
    frags(Hf);
    cmul2(g2, g3, g0, w512, g1, w512);
    const cpx g4 = cmul(g2, w512);
    rows4<0>(keep, swr[0], swr[1], g0, g1, g2, g3);
    lds_st<u32>(swr[2] + 4 * 256, pk_bf(g4.x, g4.y));
    frags(Hg);
    SB0();
    v4f kc[4];
#pragma unroll
    for (int x = 0; x < 2; ++x)
#pragma unroll
      for (int k = 0; k < 4; ++k)
#pragma unroll
        for (int y = 0; y < 2; ++y)
          kc[2 * x + y] = __builtin_amdgcn_mfma_f32_16x16x32_bf16(x ? Hg[k] : Hf[k], J[y][k], k == 0 ? (v4f){0.f, 0.f, 0.f, 0.f} : kc[2 * x + y], 0, 0, 0);
    const u32 kpw = LDS_SCR + w * 4096 + vl16;
#pragma unroll
    for (int t = 0; t < 4; ++t) lds_st<v4f>(kpw + t * 1024, kc[t]);
  }
  STAMP(13);
  const int G = l >> 4, q = (l & 15) >> 2, p = l & 3;
  const int usl = 2 * rot + (ft >= 7);
  const u32 a_ut = opaque((4 * wf + G) * 1024 + q * 128 + ((4 * (usl & 1) + 2 * wt + (p >> 1)) ^ (4 * (G & 1) + (q >> 1))) * 16 + 8 * (p & 1));
  u32 xa[4];
#pragma unroll
  for (int s = 0; s < 4; ++s)
    xa[s] = opaque(LDS_X + (8 * wt + ((l & 31) >> 3)) * 1024 + (l & 7) * 128 + (((2 * s + (l >> 5)) ^ ((l >> 1) & 7)) * 16));
  u32 wa[3];
  wa[0] = opaque(LDS_W + (8 * (G >> 1) + q) * 256 + (((4 * wf + 2 * (G & 1) + (p >> 1)) ^ (4 * q)) * 16) + 8 * (p & 1));
  wa[1] = opaque(wa[0] + 32768); wa[2] = opaque(wa[0] + 65536);
  {
    const u32 a = LDS_WST + lw + (16 * (G >> 1) + 4 * (G & 1) + q) * 32 + p * 8;
    const u32 o = WS_WT + (((ct >> 2) * 56 + (cb >> 2)) * 4 + (ct & 3)) * 1024 + vl16;
    PIECE(1, 0); PIECE(1, 1); PIECE(1, 2); PIECE(1, 3); PIECE(1, 4); PIECE(1, 5);
    WAITV(6);
    const v2u lo = lds_tr(a), hi = lds_tr(a + 256);
    st16<16>((v4u){lo.x, lo.y, hi.x, hi.y}, rws, o);
  }
  STAMP(2);

  v16f accg[2] = {}, accu[2] = {};
  v4u ut4;
  v4u wfr[28];
  const int kr = (w + 4 * (nt >> 2) + 2 * (bi & 3)) & 7;
  const u32 wd_so = WS_WT + (nt * 56 + kr * 7) * 4096;
  auto ldw = [&](int i) { wfr[i] = __builtin_amdgcn_raw_buffer_load_b128(rws, vl16 + (i & 3) * 1024, wd_so + (i >> 2) * 4096, 16); };
  u32 hadr;
  v2f nl2e = {-1.44269504f, -1.44269504f};
  v2f sp[2], se[2];
  v4u so;
  float qe[4], qp[4];
  auto silu_slice = [&](int j, int i) {
    const int r = 4 * (i >> 2), ph = i & 3;
    if (j == 0) {
      if (ph == 0)
        asm volatile("v_mul_f32 %0, 0xbfb8aa3b, %4\n\tv_mul_f32 %1, 0xbfb8aa3b, %5\n\tv_mul_f32 %2, 0xbfb8aa3b, %6\n\t"
                     "v_mul_f32 %3, 0xbfb8aa3b, %7\n\tv_exp_f32 %0, %0\n\tv_exp_f32 %1, %1"
                     : "=&v"(qe[0]), "=&v"(qe[1]), "=&v"(qe[2]), "=&v"(qe[3])
                     : "v"(accg[0][r]), "v"(accg[0][r + 1]), "v"(accg[0][r + 2]), "v"(accg[0][r + 3]));
      else if (ph == 1)
        asm volatile("v_exp_f32 %2, %2\n\tv_exp_f32 %3, %3\n\tv_add_f32 %0, 1.0, %0\n\tv_add_f32 %1, 1.0, %1\n\t"
                     "v_add_f32 %2, 1.0, %2\n\tv_add_f32 %3, 1.0, %3"
                     : "+v"(qe[0]), "+v"(qe[1]), "+v"(qe[2]), "+v"(qe[3]));
      else if (ph == 2)
        asm volatile("v_rcp_f32 %0, %0\n\tv_rcp_f32 %1, %1\n\tv_mul_f32 %3, %7, %11\n\tv_mul_f32 %4, %8, %12\n\t"
                     "v_mul_f32 %5, %9, %13\n\tv_rcp_f32 %2, %2\n\tv_mul_f32 %6, %10, %14"
                     : "+v"(qe[0]), "+v"(qe[1]), "+v"(qe[2]), "=&v"(qp[0]), "=&v"(qp[1]), "=&v"(qp[2]), "=&v"(qp[3])
                     : "v"(accg[0][r]), "v"(accg[0][r + 1]), "v"(accg[0][r + 2]), "v"(accg[0][r + 3]),
                       "v"(accu[0][r]), "v"(accu[0][r + 1]), "v"(accu[0][r + 2]), "v"(accu[0][r + 3]));
      else {
        u32 o0, o1;
        asm volatile("v_rcp_f32 %5, %5\n\tv_mul_f32 %2, %2, %6\n\tv_mul_f32 %3, %3, %7\n\tv_mul_f32 %4, %4, %8\n\t"
                     "v_cvt_pk_bf16_f32 %0, %2, %3\n\tv_mul_f32 %5, %5, %9\n\ts_nop 0\n\tv_cvt_pk_bf16_f32 %1, %4, %5"
                     : "=&v"(o0), "=&v"(o1), "+v"(qe[0]), "+v"(qe[1]), "+v"(qe[2]), "+v"(qe[3])
                     : "v"(qp[0]), "v"(qp[1]), "v"(qp[2]), "v"(qp[3]));
        so[(r >> 1) & 3] = o0; so[((r >> 1) & 3) + 1] = o1;
        if (r & 4) st16<16>(so, rws, hadr + (r >> 3) * 512);
      }
    } else if (ph == 0) {
#pragma unroll
      for (int e = 0; e < 2; ++e) {
        const v2f gv = {accg[j][r + 2 * e], accg[j][r + 2 * e + 1]};
        se[e] = gv * nl2e;
        sp[e] = gv * (v2f){accu[j][r + 2 * e], accu[j][r + 2 * e + 1]};
      }
      se[0] = (v2f){__builtin_amdgcn_exp2f(se[0].x), __builtin_amdgcn_exp2f(se[0].y)};
    } else if (ph == 1) {
      se[1] = (v2f){__builtin_amdgcn_exp2f(se[1].x), __builtin_amdgcn_exp2f(se[1].y)};
      se[0] = se[0] + 1.f;
    } else if (ph == 2) {
      se[0] = (v2f){__builtin_amdgcn_rcpf(se[0].x), __builtin_amdgcn_rcpf(se[0].y)};
      se[1] = se[1] + 1.f;
      se[1] = (v2f){__builtin_amdgcn_rcpf(se[1].x), __builtin_amdgcn_rcpf(se[1].y)};
    } else {
      sp[0] = sp[0] * se[0]; sp[1] = sp[1] * se[1];
      so[(r >> 1) & 3] = pk_bf(sp[0].x, sp[0].y);
      so[((r >> 1) & 3) + 1] = pk_bf(sp[1].x, sp[1].y);
      if (r & 4) st16<16>(so, rws, hadr + j * (2 * 56 * 1024) + (r >> 3) * 512);
    }
  };
  {
    v8bf fg[2], fu[2], fx0[2], fx1[2];
    auto rd = [&](int buf, int st, int ks) {
      const u32 wb = wa[st % 3] + ks * 4096, xb = xa[ks] + (st & 1) * 16384;
      fg[buf] = as_frag(lds_tr(wb), lds_tr(wb + 1024));
      fx0[buf] = as_frag(lds_ld<v4u>(xb));
      fu[buf] = as_frag(lds_tr(wb + 16384), lds_tr(wb + 16384 + 1024));
      fx1[buf] = as_frag(lds_ld<v4u>(xb + 4096));
    };
    v4f ks4[4];
    u32 kp01, kp23, ki[4];
    auto kgen = [&](int n) {
      if (n == 22) {
#pragma unroll
        for (int k = 0; k < 4; ++k) ks4[k] = lds_ld<v4f>(LDS_SCR + (w & 4) * 4096 + (w & 3) * 1024 + vl16 + k * 4096);
      }
      if (n == 23) {
        const int j = (l & 15) + 16 * (w & 1);
        const u32 tb = LDS_TAB + (w >> 2) * 1344 + 8;
        const bool top = !(w & 2);
        const int i0 = top ? (G < 2 ? 8 * j + 4 * G : G == 2 ? 256 + 4 * j : 384 + 2 * j) : (G == 0 ? 512 + j : (G == 1 && j == 0) ? 640 : 650);
        const int dd = top ? 1 : G == 0 ? 32 : 0;
        ki[0] = opaque(tb + 2 * i0);
        ki[2] = opaque(ki[0] + (top ? (G == 3 ? 128 : 4) : 4 * dd));
        ki[1] = opaque(!top && G == 1 ? tb + 1300 : ki[0] + 2 * dd);
        if (!top && G == 1) ki[2] = tb + 1300;
        ki[3] = opaque(ki[2] + 2 * dd);
      }
      if (n == 24) {
        const v4f s = add4s(add4s(ks4[0], ks4[1]), add4s(ks4[2], ks4[3]));
        kp01 = opaque(pk_bf(s[0], s[1])); kp23 = opaque(pk_bf(s[2], s[3]));
      }
      if (n == 25) {
        lds_st<u16>(ki[0], (u16)kp01); lds_st<u16>(ki[1], (u16)(kp01 >> 16));
        lds_st<u16>(ki[2], (u16)kp23); lds_st<u16>(ki[3], (u16)(kp23 >> 16));
        lds_st<v2u>(LDS_TAB + (w >> 2) * 1344, (v2u){0, 0});
      }
    };
    const int xm = tid & 255, xs = 2047 - 8 * xm;
    u32 xsh[2], xwd[2], xad[2], xbs[2], xi[11], xv[11], xe[2][5], xkb, xz, uta, uia;
    auto expand = [&](int step) {
      if (step < 2) {
        const int oct = xs - 7 - 8 * step;
        xsh[step] = opaque((oct >= 1024) + (oct >= 512) + (oct >= 256));
        xwd[step] = opaque(oct >= 256);
        xad[step] = opaque(((1u << xsh[step]) >> 1) - xwd[step]);
        xbs[step] = opaque(LDS_TAB + (w >> 2) * 1344 + 8 + 256 * xsh[step]);
      } else if (step < 13) {
        const int j = step - 2, o = j >> 3;
        const u32 sv = xs - j;
        xi[j] = opaque(xbs[o] + 2 * ((sv + xad[o] + __builtin_amdgcn_ubfe(sv, xsh[o], xwd[o])) >> xsh[o]));
      } else if (step == 13) {
#pragma unroll
        for (int j = 0; j < 11; ++j) xv[j] = lds_ld<u16>(xi[j]);
      } else if (step < 16) {
        const int o = step - 14;
#pragma unroll
        for (int j = 0; j < 5; ++j) xe[o][j] = opaque(xv[2 * j + o] | (xv[2 * j + o + 1] << 16));
      } else if (step < 18) {
        const int o = step - 16;
        lds_st<v4u>(xkb + (3 - o) * KCOPY, (v4u){xe[o][0], xe[o][1], xe[o][2], xe[o][3]});
        lds_st<v4u>(xkb + (1 - o) * KCOPY, (v4u){xe[o][1], xe[o][2], xe[o][3], xe[o][4]});
      } else {
        lds_st<v4u>(xz, (v4u){0, 0, 0, 0});
      }
    };
    v2u utl, uth;
    const u32 wfo = WS_FLAG_W + 16 * (l < 56 ? l : 0);
    const gu32* ufp = (const gu32*)(ws + WS_FLAG_U) + (cb >> 4) * 16 + (l & 15);
    v4u wflag;
    u32 uflag;
    auto side = [&](auto S, auto KS, auto H) {
      constexpr int s = decltype(S)::value, ks = decltype(KS)::value, h = decltype(H)::value;
      constexpr int st = 4 * s + ks;
      if constexpr (h == 0 || h == 2) {
        constexpr int i = h >> 1;
        if constexpr (ks == 3) PIECE(s + 2, i);
        else if constexpr (s == 0) { if constexpr (ks < 2) PIECE(2, 2 + 2 * ks + i); }
        else if constexpr (ks < 2 && s < 5 && s + 2 < 7) PIECE(s + 2, 2 + 2 * ks + i);
        if constexpr (st == 3 && h == 2)
          st16<16>((v4u){utl.x, utl.y, uth.x, uth.y}, rws,
                   WS_UT + (((2 * mt + (wf >> 1)) * 28 + 2 * usl + wt) * 2 + (wf & 1)) * 1024 + (l & 15) * 64 + G * 16);
      } else {
        constexpr int v = 2 * st + (h >> 1);
        if constexpr (v == 0) kgen(22);
        if constexpr (v == 1) kgen(23);
        if constexpr (v == 3) kgen(24);
        if constexpr (v == 6) kgen(25);
        if constexpr (v >= 8 && v < 21) expand(v - 8);
        if constexpr (v == 22) expand(13);
        if constexpr (v == 25 || v == 26) expand(v - 11);
        if constexpr (v == 28) {
          xkb = opaque(((w >> 2) ? LDS_KIMG1 : LDS_KIMG0) + xm * 16);
          xz = opaque((tid & 127) < 16 ? ((tid & 8) ? LDS_KIMG1 : LDS_KIMG0) + ((tid >> 1) & 3) * KCOPY + 4096 + (tid & 1) * 16
                                       : LDS_UIMG + ((tid >> 5) & 1) * UCH + (tid & 31) * 16);
        }
        if constexpr (v == 29) {
          uta = opaque((tid >> 4) * (56 * 1024) + ((tid >> 3) & 1) * 1024 + (tid & 7) * 16);
          uia = opaque(LDS_UIMG + ((tid >> 2) & 1) * UCH + 512 + (tid >> 3) * 64 + (tid & 3) * 16);
        }
        if constexpr (v == 30) {
          hadr = opaque(WS_H + ((mt * 8 + 4 * wt + ((l & 31) >> 4)) * 56 + ft * 4 + wf) * 1024 + (l & 15) * 16 + (l >> 5) * 256);
          asm volatile("" : "+v"(nl2e));
        }
        if constexpr (v >= 38) ldw(v - 38);
      }
    };
    auto mm = [&](int buf, auto S, auto KS) {
      accg[0] = __builtin_amdgcn_mfma_f32_32x32x16_bf16(fg[buf], fx0[buf], accg[0], 0, 0, 0);
      SB0(); side(S, KS, ic<0>{}); SB0();
      accu[0] = __builtin_amdgcn_mfma_f32_32x32x16_bf16(fu[buf], fx0[buf], accu[0], 0, 0, 0);
      SB0(); side(S, KS, ic<1>{}); SB0();
      accg[1] = __builtin_amdgcn_mfma_f32_32x32x16_bf16(fg[buf], fx1[buf], accg[1], 0, 0, 0);
      SB0(); side(S, KS, ic<2>{}); SB0();
      accu[1] = __builtin_amdgcn_mfma_f32_32x32x16_bf16(fu[buf], fx1[buf], accu[1], 0, 0, 0);
      SB0(); side(S, KS, ic<3>{});
      asm volatile("" :: "v"(fg[buf]), "v"(fu[buf]), "v"(fx0[buf]), "v"(fx1[buf]));
      SB0();
    };
    v8bf tg[8], tu[8], tx[3];
    auto rd1 = [&](int n) {
      const int st = 5 + ((n >> 2) & 1), ks = n & 3;
      const u32 wb = wa[st % 3] + ks * 4096;
      if (n < 8) tg[n] = as_frag(lds_tr(wb), lds_tr(wb + 1024));
      tx[n % 3] = as_frag(lds_ld<v4u>(xa[ks] + (st & 1) * 16384 + 4096 * (n >> 3)));
      if (n < 8) tu[n] = as_frag(lds_tr(wb + 16384), lds_tr(wb + 16384 + 1024));
    };
    auto stage = [&](auto S) {
      constexpr int s = decltype(S)::value;
      PRIO(2); rd(1, s, 1); SB0(); mm(0, S, ic<0>{});
      PRIO(1); rd(0, s, 2); SB0(); mm(1, S, ic<1>{});
      PRIO(0); rd(1, s, 3); SB0(); mm(0, S, ic<2>{});
      PRIO(3);
      WAITL0();
      if constexpr (s < 2) WAITV(5);
      else if constexpr (s < 4) WAITV(4);
      else {
        asm volatile("s_waitcnt vmcnt(4)" : "+v"(wflag) :: "memory");
        if (w == 0 && !__all(wflag.x == tag && wflag.y == tag && wflag.z == tag && wflag.w == tag)) {
#pragma unroll 1
          for (int sp = 0; sp < SPIN_CAP; ++sp) {
            asm volatile("" ::: "memory");
            const v4u f = __builtin_amdgcn_raw_buffer_load_b128(rws, wfo, 0, 16);
            if (__all(f.x == tag && f.y == tag && f.z == tag && f.w == tag)) break;
          }
        }
      }
      BAR();
      if constexpr (s < 4) rd(0, s + 1, 0); else { rd1(0); rd1(1); }
      if constexpr (s == 1) { if (tid == 0) __hip_atomic_store((gu32*)(ws + WS_FLAG_W) + cb, tag, RLX_AGENT); }
      if constexpr (s == 2) { if (tid == 0) __hip_atomic_store((gu32*)(ws + WS_FLAG_U) + usl * 16 + mt, tag, RLX_AGENT); }
      if constexpr (s == 3)
        asm volatile("s_cmp_lg_u32 %3, 0\n\ts_cbranch_scc1 1f\n\tbuffer_load_dwordx4 %0, %1, %2, 0 offen sc1\n1:"
                     : "=v"(wflag) : "v"(wfo), "s"(rws), "s"(w) : "memory", "scc");
      if constexpr (s == 4)
        asm volatile("s_cmp_lg_u32 %2, 0\n\ts_cbranch_scc1 1f\n\tglobal_load_dword %0, %1, off sc1\n1:"
                     : "=v"(uflag) : "v"(ufp), "s"(w) : "memory", "scc");
      if constexpr (s == 4) __builtin_amdgcn_fence(__ATOMIC_ACQUIRE, "wavefront");
      STAMP(22 + s);
      SB0();
      mm(1, S, ic<3>{});
    };
    WAITL0(); BAR();
    STAMP(3);
    rd(0, 0, 0);
    utl = lds_tr(a_ut); uth = lds_tr((a_ut ^ 32) + 512);
    SB0();
    stage(ic<0>{}); stage(ic<1>{}); stage(ic<2>{}); stage(ic<3>{}); stage(ic<4>{});
    STAMP(20);
#pragma unroll
    for (int n = 0; n < 16; ++n) {
      const int j = n >> 3;
      SB0();
      if (n == 3) {
        asm volatile("s_waitcnt vmcnt(4)" : "+v"(uflag) :: "memory");
        if (w == 0) wait_flags(ufp, tag, uflag, 2);
        BAR();
        __builtin_amdgcn_fence(__ATOMIC_ACQUIRE, "wavefront");
        rd1(4);
        ut4 = __builtin_amdgcn_raw_buffer_load_b128(rws, uta, WS_UT + (cb >> 3) * 2048 + (cb & 7) * 128, 16);
      }
      if (n == 8) STAMP(21);
      if (n == 0) PRIO(2);
      if (n == 1) PRIO(1);
      if (n == 3) PRIO(3);
      if (n == 5) PRIO(2);
      if (n == 8) PRIO(1);
      if (n == 11) PRIO(0);
      if (n == 14) { PRIO(1); WAITL0(); BAR(); }
      if (n < 14 && n != 2) rd1(n + 2);
      SB0();
      accg[j] = __builtin_amdgcn_mfma_f32_32x32x16_bf16(tg[n & 7], tx[n % 3], accg[j], 0, 0, 0);
      SB0();
      if (n >= 8 && n < 11) expand(8 + n);
      if (n == 12) lds_st<v4u>(uia, ut4);
      if (j) silu_slice(0, 2 * n - 16);
      ldw(n + 2);
      SB0();
      accu[j] = __builtin_amdgcn_mfma_f32_32x32x16_bf16(tu[n & 7], tx[n % 3], accu[j], 0, 0, 0);
      SB0();
      if (j) silu_slice(0, 2 * n - 15);
      asm volatile("" :: "v"(tg[n & 7]), "v"(tu[n & 7]), "v"(tx[n % 3]));
    }
  }
  STAMP(4);
  const gu32* hfp = (const gu32*)(ws + WS_FLAG_H) + (m2 * 56 + 7 * kr + (l < 7 ? l : 0)) * HFS;
  u32 hflag;
  {
    const int ch = w >> 2, z = w & 3;
    const int a = l & 15;
    const u32 ka = opaque((ch ? LDS_KIMG1 : LDS_KIMG0) + (a & 3) * KCOPY + 2 * (2028 + 8 * G - 4 * (a >> 2)) - 512 * 7 - 128 * z - 64);
    const u32 ka8 = opaque(ka + 8);
    const u32 ub = opaque(LDS_UIMG + ch * UCH + 2 * (240 + 16 * a + 8 * G) - 128 * z - 64);
    const u32 yb = opaque(LDS_Y + (z * 2 + ch) * 8192 + (16 * a + 4 * (G ^ ((a >> 1) & 3))) * 4);
    v4f acc[8];
    v8bf A[8], B[8];
    auto ld = [&](int qq, int pp) {
      const u32 o = 64 - 64 * qq;
      B[7 - pp] = as_frag(lds_ld<v4u>(ub + o + 512 * (7 - pp)));
      A[pp] = as_frag(lds_ld<v2u>(ka + o + 512 * (7 - pp)), lds_ld<v2u>(ka8 + o + 512 * (7 - pp)));
    };
#pragma unroll
    for (int i = 0; i < 16; ++i) {
      silu_slice(1, i);
      if (i >= 8) ld(0, i - 8);
      SB0();
    }
    PRIO(0);
    asm volatile("" ::: "memory");
    STAMP(5);
    WST(0);
#pragma unroll
    for (int qq = 0; qq < 2; ++qq) {
#pragma unroll
      for (int pp = 0; pp < 8; ++pp) {
#pragma unroll
        for (int c = 7; c >= pp; --c)
          acc[c] = __builtin_amdgcn_mfma_f32_16x16x32_bf16(A[pp], B[c - pp], (qq == 0 && pp == 0) ? (v4f){0.f, 0.f, 0.f, 0.f} : acc[c], 0, 0, 0);
        SB0();
        if (qq == 0) ld(1, pp);
        else if (pp > 0) lds_st<v4f>(yb + (pp - 1) * 1024, acc[pp - 1]);
        if (pp < 5) ldw(18 + 5 * qq + pp);
        if (qq == 1 && pp == 5) hflag = __hip_atomic_load(hfp, RLX_AGENT);
        SB0();
      }
      if (qq == 0) {
        WAITV(5);
        if (l == 0) __hip_atomic_store((gu32*)(ws + WS_FLAG_H) + ((2 * mt + wt) * 56 + 4 * ft + wf) * HFS, tag, RLX_AGENT);
        STAMP(6);
        WST(1);
      }
    }
    lds_st<v4f>(yb + 7 * 1024, acc[7]);
  }
  const u32 yo = opaque(LDS_Y + (tid >> 2) * 64 + (((tid & 3) ^ ((tid >> 3) & 3)) * 16)), uo = opaque(LDS_UIMG + 512 + tid * 8);
  WAITL0(); BAR();
  STAMP(7);
  v4u a_out;
  {
    const v2u u0 = lds_ld<v2u>(uo), u1 = lds_ld<v2u>(uo + UCH);
    const v4f y0 = (lds_ld<v4f>(yo) + lds_ld<v4f>(yo + 16384)) + (lds_ld<v4f>(yo + 32768) + lds_ld<v4f>(yo + 49152));
    const v4f y1 = (lds_ld<v4f>(yo + 8192) + lds_ld<v4f>(yo + 24576)) + (lds_ld<v4f>(yo + 40960) + lds_ld<v4f>(yo + 57344));
    const float k0 = bf_lo(dsk2), k1 = bf_hi(dsk2);
    a_out.x = pk_bf(__builtin_fmaf(bf_lo(u0.x), k0, y0[0]), __builtin_fmaf(bf_lo(u1.x), k1, y1[0]));
    a_out.y = pk_bf(__builtin_fmaf(bf_hi(u0.x), k0, y0[1]), __builtin_fmaf(bf_hi(u1.x), k1, y1[1]));
    a_out.z = pk_bf(__builtin_fmaf(bf_lo(u0.y), k0, y0[2]), __builtin_fmaf(bf_lo(u1.y), k1, y1[2]));
    a_out.w = pk_bf(__builtin_fmaf(bf_hi(u0.y), k0, y0[3]), __builtin_fmaf(bf_hi(u1.y), k1, y1[3]));
    asm volatile("" : "+v"(a_out));
    STAMP(12);
    WST(2);
    wait_flags(hfp, tag, hflag, 3);
    __builtin_amdgcn_fence(__ATOMIC_ACQUIRE, "wavefront");
  }
  STAMP(8);
  WST(3);

  {
    v4u hf[28];
    u32 hso[4];
#pragma unroll
    for (int tt = 0; tt < 4; ++tt) hso[tt] = WS_H + ((m2 * 4 + ((tt + nt) & 3)) * 56 + kr * 7) * 1024;
    const u32 vl16b = vl16 + 4096;
    auto ldh = [&](int n) {
      const int i = n % 7;
      hf[n] = __builtin_amdgcn_raw_buffer_load_b128(rws, (i < 4 ? vl16 : vl16b) + (i & 3) * 1024, hso[n / 7], 16);
      SB0();
    };
    constexpr int AHEAD = 9;
#pragma unroll
    for (int n = 0; n < AHEAD - 1; ++n) ldh(n);
    {
      const u32 ao = vl16 + WS_A2 + cb * 16384 + w * 2048;
      st16<16>((v4u){a_out.x, tag, a_out.y, tag}, rws, ao);
      st16<16>((v4u){a_out.z, tag, a_out.w, tag}, rws, ao + 1024);
      SB0();
    }
    STAMP(27);
    WST(4);
    u32 gflag, gate_v;
    v4u a4[2];
    const gu32* gfp = (const gu32*)(ws + WS_FLAG_G) + m2;
    const u32 a_vo = opaque((tid >> 4) * 16384 + (tid & 15) * 16), a_so = WS_A2 + 32 * nt * 16384 + (m2 >> 2) * 2048 + (m2 & 3) * 256;
    auto lda = [&]() {
      a4[0] = __builtin_amdgcn_raw_buffer_load_b128(rws, a_vo, a_so, 16);
      a4[1] = __builtin_amdgcn_raw_buffer_load_b128(rws, a_vo + 1024, a_so, 16);
    };
    const int tf = 16 * ((wf + nt) & 3) + ((tid & 127) >> 3);
    const u32 pw = opaque(LDS_P + w * PWAVE + (l & 15) * PROW + G * 16);
    const u32 prd = opaque(LDS_P + (wf & 1) * PBUF + ((tid & 127) >> 3) * PROW + (tid & 7) * 32);
    u32 asw, asr, oad;
    v4f acc[2][4], b0, b1;
    auto reduce = [&]() {
#pragma unroll
      for (int h = 0; h < 2; ++h) {
        v4f r0[4], r1[4];
#pragma unroll
        for (int k = 0; k < 4; ++k) { r0[k] = lds_ld<v4f>(prd + (4 * h + k) * PWAVE); r1[k] = lds_ld<v4f>(prd + (4 * h + k) * PWAVE + 16); }
        const v4f s0 = add4s(add4s(r0[0], r0[1]), add4s(r0[2], r0[3])), s1 = add4s(add4s(r1[0], r1[1]), add4s(r1[2], r1[3]));
        b0 = h ? add4s(b0, s0) : s0; b1 = h ? add4s(b1, s1) : s1;
        SB0();
      }
    };
    u32 av[4];
    auto mix = [&]() {
      const float gt = __uint_as_float(gate_v), og = 1.f - gt;
      b0 = b0 * og; b1 = b1 * og;
      v4u o;
      o.x = pk_bf(__builtin_fmaf(gt, bf_lo(av[0]), b0[0]), __builtin_fmaf(gt, bf_hi(av[0]), b0[1]));
      o.y = pk_bf(__builtin_fmaf(gt, bf_lo(av[1]), b0[2]), __builtin_fmaf(gt, bf_hi(av[1]), b0[3]));
      o.z = pk_bf(__builtin_fmaf(gt, bf_lo(av[2]), b1[0]), __builtin_fmaf(gt, bf_hi(av[2]), b1[1]));
      o.w = pk_bf(__builtin_fmaf(gt, bf_lo(av[3]), b1[2]), __builtin_fmaf(gt, bf_hi(av[3]), b1[3]));
      st16<0>(o, mk_rsrc(g.out, L * D * 2), oad);
    };
#pragma unroll
    for (int tt = 0; tt < 4; ++tt) {
#pragma unroll
      for (int i = 0; i < 7; ++i) {
        const int n = 7 * tt + i;
        if (i == 0) PRIO(3);
        if (i == 2) PRIO(2);
        if (i == 4) PRIO(1);
        if (i == 6) PRIO(0);
        if (n + AHEAD - 1 < 28) ldh(n + AHEAD - 1);
        if (n == 1) gflag = __hip_atomic_load(gfp, RLX_AGENT);
        if (n == 12) {
          wait_flags(gfp, tag, gflag, 4);
          __builtin_amdgcn_fence(__ATOMIC_ACQUIRE, "wavefront");
          gate_v = __builtin_amdgcn_raw_buffer_load_b32(rws, tf * 4, WS_GATE + 64 * m2 * 4, 16);
          lda();
        }
        SB0();
#pragma unroll
        for (int c = 0; c < 4; ++c)
          acc[tt & 1][c] = __builtin_amdgcn_mfma_f32_16x16x32_bf16(as_frag(wfr[4 * i + c]), as_frag(hf[n]),
                                                                   i == 0 ? (v4f){0.f, 0.f, 0.f, 0.f} : acc[tt & 1][c], 0, 0, 0);
        SB0();
        if (n == 0) { STAMP(29); WST(5); }
        if (n == 2) asw = opaque(LDS_AST + (tid >> 4) * 272 + (tid & 15) * 16);
        if (n == 3) asr = opaque(LDS_AST + (tid & 7) * (4 * 272) + tf * 4);
        if (n == 4) oad = opaque((64 * m2 + tf) * (D * 2) + 128 * nt + (tid & 7) * 16);
        if (tt > 0 && i < 4) lds_st<v4f>(pw + ((tt - 1) & 1) * PBUF + i * 64, acc[(tt - 1) & 1][i]);
        if (tt > 1 && i == 0) { if (wf == tt - 2) reduce(); }
        if (n == 20) {
          if (!__all(a4[0].y == tag && a4[0].w == tag && a4[1].y == tag && a4[1].w == tag)) {
#pragma unroll 1
            for (int sp = 0; sp < SPIN_CAP; ++sp) {
              asm volatile("" ::: "memory");
              lda();
              if (__all(a4[0].y == tag && a4[0].w == tag && a4[1].y == tag && a4[1].w == tag)) break;
            }
          }
          lds_st<v4u>(asw, (v4u){a4[0].x, a4[0].z, a4[1].x, a4[1].z});
        }
        if (n == 22) {
#pragma unroll
          for (int e = 0; e < 4; ++e) av[e] = lds_ld<u32>(asr + e * 272);
        }
        if (n == 25) { WAITL0(); BAR(); if (wf == 2) reduce(); }
        if (n == 26) { if (wf < 3) mix(); }
        if (i == 6 && tt < 3) { WAITL0(); BAR(); }
        if (n > 0) asm volatile("" :: "v"(hf[n - 1]));
        SB0();
      }
      if (tt == 0) STAMP(9);
      if (tt == 1) STAMP(16);
      if (tt == 2) STAMP(17);
      if (tt == 3) STAMP(18);
    }
#pragma unroll
    for (int c = 0; c < 4; ++c) lds_st<v4f>(pw + PBUF + c * 64, acc[1][c]);
    WAITL0(); BAR();
    STAMP(10);
    if (wf == 3) { reduce(); mix(); }
  }
}

extern "C" void kernel_launch(void* const* d_in, const int* in_sizes, int n_in, void* d_out, int out_size, void* d_ws,
                              size_t ws_size, hipStream_t stream) {
  Args a;
  for (int i = 0; i < 8; ++i) a.par[i] = (const u16*)d_in[1 + i];
  a.u = (const u16*)d_in[0];
  a.wg = (const u16*)d_in[10]; a.wu = (const u16*)d_in[11]; a.wd = (const u16*)d_in[12];
  a.gw = (const u16*)d_in[13];
  a.out = (u16*)d_out;
  a.ws = (char*)d_ws;
  (void)hipFuncSetAttribute((const void*)gated_mix_kernel, hipFuncAttributeMaxDynamicSharedMemorySize, LDS_TOTAL);
  gated_mix_kernel<<<NMAIN + NHELP, 512, LDS_TOTAL, stream>>>(a);
}
```

```cpp
#include <hip/hip_runtime.h>

typedef unsigned short u16;
typedef unsigned u32;
typedef unsigned long long u64;
typedef u32 v4u __attribute__((ext_vector_type(4)));
typedef u32 v2u __attribute__((ext_vector_type(2)));
typedef float v2f __attribute__((ext_vector_type(2)));
typedef float v4f __attribute__((ext_vector_type(4)));
typedef float v16f __attribute__((ext_vector_type(16)));
typedef __bf16 v8bf __attribute__((ext_vector_type(8)));
typedef __bf16 v2bf __attribute__((ext_vector_type(2)));
typedef short v4s __attribute__((ext_vector_type(4)));
#define LAS __attribute__((address_space(3)))
#define GAS __attribute__((address_space(1)))
typedef GAS u32 gu32;
typedef __amdgpu_buffer_rsrc_t rsrc_t;

#define AFFINE 0

constexpr int L = 2048, D = 448, NS = 256, F = 1792;
constexpr int NMAIN = 224, NHELP = 32;

constexpr u32 WS_EPOCH = 0;
constexpr u32 WS_FLAG_W = 1024;
constexpr u32 WS_FLAG_G = WS_FLAG_W + 1024;
constexpr u32 WS_FLAG_U = WS_FLAG_G + 1024;
constexpr u32 WS_GATE = 245760;
#define HF_P 1
#define LDA_N 20
#define AST_N 19
#define HL_P 9
#define HFS 32
constexpr u32 WS_FLAG_H = 16384;
constexpr u32 WS_UT = 262144;
constexpr u32 WS_A2 = WS_UT + D * L * 2;
constexpr u32 WS_H = WS_A2 + D * L * 4;
constexpr u32 WS_WT = WS_H + L * F * 2;
constexpr u32 WS_END = WS_WT + F * D * 2;
constexpr u32 WS_STAMP = 16u << 20;
constexpr u32 TAG_STEP = 0x9E3779B9u;

constexpr u32 LDS_X = 0;
constexpr u32 LDS_W = 32768;
constexpr u32 LDS_SCR = 131072;
constexpr u32 LDS_TOTAL = 163840;
constexpr u32 LDS_PAR = LDS_W + 2 * 32768 + 16384;
constexpr u32 LDS_WST = LDS_W + 2 * 32768;
constexpr u32 KCOPY = 4160, KCH = 4 * KCOPY;
constexpr u32 LDS_KIMG0 = LDS_W + 32768, LDS_KIMG1 = LDS_SCR + 4096;
constexpr u32 UCH = 512 + 4096;
constexpr u32 LDS_UIMG = LDS_KIMG0 + KCH;
constexpr u32 LDS_TAB = LDS_SCR;
constexpr u32 LDS_Y = 0;
constexpr u32 PROW = 272, PWAVE = 16 * PROW, PBUF = 8 * PWAVE;
constexpr u32 LDS_P = 91648;
constexpr u32 LDS_AST = 65536;

struct ThreadTab { u32 v[512][8]; };
constexpr int round_bf16(int s) {
  const int sh = s >= 1024 ? 3 : s >= 512 ? 2 : s >= 256 ? 1 : 0;
  return sh ? ((s + (1 << (sh - 1)) - 1 + ((s >> sh) & 1)) >> sh) << sh : s;
}
constexpr int tab_pos(int s) {
  if (s < 0) return 2 * 512 + 32 * 8;
  const int v = round_bf16(s);
  int x = 0, row = 0, j = 0;
  if (v < 256) { j = v >> 3; row = v & 7; }
  else if (v < 512) { j = (v - 256) >> 3; row = 8 + ((v & 7) >> 1); }
  else if (v < 1024) { j = (v & 255) >> 3; row = 12 + 2 * ((v - 512) >> 8) + ((v & 7) >> 2); }
  else { x = 1; j = (v & 255) >> 3; row = (v - 1024) >> 8; }
  return (2 * x + (j >> 4)) * 512 + ((j & 15) + 16 * (row >> 2)) * 8 + (row & 3) * 2;
}
constexpr ThreadTab make_tab() {
  ThreadTab t{};
  for (int tid = 0; tid < 512; ++tid) {
    const int l = tid & 63, w = tid >> 6, wt = w & 1, wf = w >> 1, G = l >> 4, q = (l & 15) >> 2, p = l & 3;
    u32 e[16] = {};
    for (int j = 0; j < 11; ++j) e[j] = (tid >> 8) * 2048 + tab_pos(2047 - 8 * (tid & 255) - j);
    e[11] = ((tid & 127) < 16 ? ((tid & 8) ? LDS_KIMG1 : LDS_KIMG0) + ((tid >> 1) & 3) * KCOPY + 4096 + (tid & 1) * 16
                              : LDS_UIMG + ((tid >> 5) & 1) * UCH + (tid & 31) * 16) >> 4;
    e[12] = (LDS_UIMG + ((tid >> 2) & 1) * UCH + 512 + (tid >> 3) * 64 + (tid & 3) * 16) >> 4;
    e[13] = (4 * wf + G) * 1024 + q * 128 + ((2 * wt + (p >> 1)) ^ (4 * (G & 1) + (q >> 1))) * 16 + 8 * (p & 1);
    e[14] = LDS_X + (8 * wt + ((l & 31) >> 3)) * 1024 + (l & 7) * 128 + (((l >> 5) ^ ((l >> 1) & 7)) * 16);
    e[15] = LDS_W + (8 * (G >> 1) + q) * 256 + (((4 * wf + 2 * (G & 1) + (p >> 1)) ^ (4 * q)) * 16) + 8 * (p & 1);
    for (int k = 0; k < 8; ++k) t.v[tid][k] = e[2 * k] | (e[2 * k + 1] << 16);
  }
  return t;
}
static __device__ const ThreadTab thread_tab = make_tab();

#define SB0() __builtin_amdgcn_sched_barrier(0)
#define WAITV(n) asm volatile("s_waitcnt vmcnt(" #n ")" ::: "memory")
#define WAITL0() asm volatile("s_waitcnt lgkmcnt(0)" ::: "memory")
#define BAR() __builtin_amdgcn_s_barrier()
#define PRIO(n) __builtin_amdgcn_s_setprio(n)
#define RLX_AGENT __ATOMIC_RELAXED, __HIP_MEMORY_SCOPE_AGENT
constexpr int CODE_KB = 16;
constexpr int SPIN_CAP = 1 << 18;

#define STAMP(i) do { } while (0)
#define WST(i) do { } while (0)
#define WSC(i) do { } while (0)

extern __shared__ __attribute__((aligned(16))) char smem[];

__device__ __forceinline__ float bf_lo(u32 x) { return __uint_as_float(x << 16); }
__device__ __forceinline__ float bf_hi(u32 x) { return __uint_as_float(x & 0xffff0000u); }
__device__ __forceinline__ u32 pk_bf(float lo, float hi) {
  v2f v = {lo, hi};
  return __builtin_bit_cast(u32, __builtin_convertvector(v, v2bf));
}
__device__ __forceinline__ float dot2bf(u32 a, u32 b, float c) {
  return __builtin_amdgcn_fdot2_f32_bf16(__builtin_bit_cast(v2bf, a), __builtin_bit_cast(v2bf, b), c, false);
}
__device__ __forceinline__ float rbf(float x) { return bf_lo(pk_bf(x, x)); }
template <class T> __device__ __forceinline__ T lds_ld(u32 off) { return *(const LAS T*)((LAS char*)smem + off); }
template <class T> __device__ __forceinline__ void lds_st(u32 off, T v) { *(LAS T*)((LAS char*)smem + off) = v; }
__device__ __forceinline__ v2u lds_tr(u32 off) {
  return __builtin_bit_cast(v2u, __builtin_amdgcn_ds_read_tr16_b64_v4i16((LAS v4s*)((LAS char*)smem + off)));
}
__device__ __forceinline__ v8bf as_frag(v4u x) { return __builtin_bit_cast(v8bf, x); }
__device__ __forceinline__ v8bf as_frag(v2u a, v2u b) { v4u x = {a.x, a.y, b.x, b.y}; return __builtin_bit_cast(v8bf, x); }
__device__ __forceinline__ rsrc_t mk_rsrc(const void* p, u32 bytes) {
  return __builtin_amdgcn_make_buffer_rsrc((void*)p, (short)0, (int)bytes, 0x00020000);
}
template <int AUX> __device__ __forceinline__ void st16(v4u v, rsrc_t r, u32 voff) {
  __builtin_amdgcn_raw_buffer_store_b128(v, r, voff, 0, AUX);
}
__device__ __forceinline__ void dma16(rsrc_t r, u32 voff, u32 soff, u32 lds) {
  asm volatile("s_mov_b32 m0, %0\n\ts_nop 0\n\tbuffer_load_dwordx4 %1, %2, %3 offen lds"
               :: "s"(lds), "v"(voff), "s"(r), "s"(soff) : "memory");
}
template <int LO, int SO> __device__ __forceinline__ void dma16i(rsrc_t r, u32 voff, u32 soff, u32 lds) {
  u32 t;
  asm volatile("s_add_u32 m0, %1, %5\n\ts_add_u32 %0, %4, %6\n\tbuffer_load_dwordx4 %2, %3, %0 offen lds"
               : "=&s"(t) : "s"(lds), "v"(voff), "s"(r), "s"(soff), "i"(LO), "i"(SO) : "memory", "scc");
}
__device__ __forceinline__ void dma4_sc1(rsrc_t r, u32 voff, u32 soff, u32 lds) {
  asm volatile("s_mov_b32 m0, %0\n\ts_nop 0\n\tbuffer_load_dword %1, %2, %3 offen sc1 lds"
               :: "s"(lds), "v"(voff), "s"(r), "s"(soff) : "memory");
}
typedef v2f cpx;
__device__ __forceinline__ cpx cmul(cpx a, cpx b) {
  cpx r;
  asm volatile("v_pk_mul_f32 %0, %1, %2 op_sel:[0,0] op_sel_hi:[0,1]" : "=v"(r) : "v"(a), "v"(b));
  asm volatile("v_pk_fma_f32 %0, %1, %2, %0 op_sel:[1,1,0] op_sel_hi:[1,0,1] neg_lo:[1,0,0]" : "+v"(r) : "v"(a), "v"(b));
  return r;
}
#define CM_MUL(d, a, b) "v_pk_mul_f32 " d ", " a ", " b " op_sel:[0,0] op_sel_hi:[0,1]\n\t"
#define CM_FMA(d, a, b) "v_pk_fma_f32 " d ", " a ", " b ", " d " op_sel:[1,1,0] op_sel_hi:[1,0,1] neg_lo:[1,0,0]\n\t"
__device__ __forceinline__ void cmul4(cpx& r0, cpx& r1, cpx& r2, cpx& r3, cpx a0, cpx a1, cpx a2, cpx a3, cpx b) {
  asm volatile(CM_MUL("%0", "%4", "%8") CM_MUL("%1", "%5", "%8") CM_MUL("%2", "%6", "%8") CM_MUL("%3", "%7", "%8")
      CM_FMA("%0", "%4", "%8") CM_FMA("%1", "%5", "%8") CM_FMA("%2", "%6", "%8") CM_FMA("%3", "%7", "%8")
      : "=&v"(r0), "=&v"(r1), "=&v"(r2), "=&v"(r3) : "v"(a0), "v"(a1), "v"(a2), "v"(a3), "v"(b));
}
__device__ __forceinline__ void cmul2(cpx& r0, cpx& r1, cpx a0, cpx b0, cpx a1, cpx b1) {
  asm volatile(CM_MUL("%0", "%2", "%3") CM_MUL("%1", "%4", "%5") CM_FMA("%0", "%2", "%3") CM_FMA("%1", "%4", "%5")
      : "=&v"(r0), "=&v"(r1) : "v"(a0), "v"(b0), "v"(a1), "v"(b1));
}
__device__ __forceinline__ void cmul2s(cpx& r0, cpx& r1, cpx a0, cpx b0, cpx a1, cpx b1) {
  float x0, y0, x1, y1;
  asm volatile("v_mul_f32 %0, %4, %6\n\tv_mul_f32 %1, %4, %7\n\tv_mul_f32 %2, %8, %10\n\tv_mul_f32 %3, %8, %11\n\t"
               "v_fma_f32 %0, -%5, %7, %0\n\tv_fmac_f32 %1, %5, %6\n\tv_fma_f32 %2, -%9, %11, %2\n\tv_fmac_f32 %3, %9, %10"
               : "=&v"(x0), "=&v"(y0), "=&v"(x1), "=&v"(y1)
               : "v"(a0.x), "v"(a0.y), "v"(b0.x), "v"(b0.y), "v"(a1.x), "v"(a1.y), "v"(b1.x), "v"(b1.y));
  r0 = (cpx){x0, y0}; r1 = (cpx){x1, y1};
}
__device__ __forceinline__ cpx cmuls(cpx a, cpx b) {
  float x, y;
  asm volatile("v_mul_f32 %0, %2, %4\n\tv_mul_f32 %1, %2, %5\n\tv_fma_f32 %0, -%3, %5, %0\n\tv_fmac_f32 %1, %3, %4"
               : "=&v"(x), "=&v"(y) : "v"(a.x), "v"(a.y), "v"(b.x), "v"(b.y));
  return (cpx){x, y};
}
__device__ __forceinline__ v2f add2x2s(v2f a, v2f b, v2f c, v2f d) {
  float x, y, z, t;
  asm("v_add_f32 %0, %4, %6\n\tv_add_f32 %1, %5, %7\n\tv_add_f32 %2, %8, %10\n\tv_add_f32 %3, %9, %11\n\t"
      "v_add_f32 %0, %0, %2\n\tv_add_f32 %1, %1, %3"
      : "=&v"(x), "=&v"(y), "=&v"(z), "=&v"(t)
      : "v"(a.x), "v"(a.y), "v"(b.x), "v"(b.y), "v"(c.x), "v"(c.y), "v"(d.x), "v"(d.y));
  return (v2f){x, y};
}
__device__ __forceinline__ u32 mix2(float g, u32 a, v2f b0, v2f b1) {
  float x, y, lo, hi;
  u32 o;
  asm("v_add_f32 %1, %7, %9\n\tv_add_f32 %2, %8, %10\n\tv_lshlrev_b32 %3, 16, %6\n\tv_and_b32 %4, 0xffff0000, %6\n\t"
      "v_sub_f32 %3, %3, %1\n\tv_sub_f32 %4, %4, %2\n\tv_fmac_f32 %1, %5, %3\n\tv_fmac_f32 %2, %5, %4\n\t"
      "s_nop 0\n\tv_cvt_pk_bf16_f32 %0, %1, %2"
      : "=&v"(o), "=&v"(x), "=&v"(y), "=&v"(lo), "=&v"(hi)
      : "v"(g), "v"(a), "v"(b0.x), "v"(b0.y), "v"(b1.x), "v"(b1.y));
  return o;
}
__device__ __forceinline__ v4f add4s(v4f a, v4f b) {
  float x, y, z, t;
  asm("v_add_f32 %0, %4, %8\n\tv_add_f32 %1, %5, %9\n\tv_add_f32 %2, %6, %10\n\tv_add_f32 %3, %7, %11"
      : "=&v"(x), "=&v"(y), "=&v"(z), "=&v"(t)
      : "v"(a.x), "v"(a.y), "v"(a.z), "v"(a.w), "v"(b.x), "v"(b.y), "v"(b.z), "v"(b.w));
  return (v4f){x, y, z, t};
}
template <int R> __device__ __forceinline__ void rows4(v4u& keep, u32 b0, u32 b1, cpx a, cpx b, cpx c, cpx d) {
  u32 t0, t1, t2, t3;
  asm volatile("v_cvt_pk_bf16_f32 %0, %6, %7\n\tv_cvt_pk_bf16_f32 %1, %8, %9\n\tv_cvt_pk_bf16_f32 %2, %10, %11\n\t"
               "v_cvt_pk_bf16_f32 %3, %12, %13\n\tds_write2st64_b32 %4, %0, %1 offset0:%18 offset1:%19\n\t"
               "ds_write2st64_b32 %5, %2, %3 offset0:%20 offset1:%21"
               : "=&v"(t0), "=&v"(t1), "=&v"(t2), "=&v"(t3)
               : "v"(b0), "v"(b1), "v"(a.x), "v"(a.y), "v"(b.x), "v"(b.y), "v"(c.x), "v"(c.y), "v"(d.x), "v"(d.y),
                 "v"(keep.x), "v"(keep.y), "v"(keep.z), "v"(keep.w), "i"(R), "i"(R + 1), "i"(R + 2), "i"(R + 3) : "memory");
  keep = (v4u){t0, t1, t2, t3};
}
template <int H> __device__ __forceinline__ u32 add_half(u32 base, u32 x) {
  u32 r;
  if constexpr (H == 0) asm volatile("v_add_u32_sdwa %0, %1, %2 dst_sel:DWORD dst_unused:UNUSED_PAD src0_sel:DWORD src1_sel:WORD_0" : "=v"(r) : "s"(base), "v"(x));
  else asm volatile("v_add_u32_sdwa %0, %1, %2 dst_sel:DWORD dst_unused:UNUSED_PAD src0_sel:DWORD src1_sel:WORD_1" : "=v"(r) : "s"(base), "v"(x));
  return r;
}
__device__ __forceinline__ void flag_store(rsrc_t r, u32 soff, u32 tag_v) {
  asm volatile("s_mov_b64 exec, 1\n\tbuffer_store_dword %0, off, %1, %2 sc1\n\ts_mov_b64 exec, -1" :: "v"(tag_v), "s"(r), "s"(soff) : "memory");
}
__device__ __forceinline__ u32 opaque(u32 x) { asm volatile("" : "+v"(x)); return x; }
__device__ __forceinline__ cpx cpow_l(float a2, float bf, float l) {
  float m = __builtin_amdgcn_exp2f(a2 * l), ph = __builtin_amdgcn_fractf(bf * l);
  return (cpx){m * __builtin_amdgcn_cosf(ph), m * __builtin_amdgcn_sinf(ph)};
}
__device__ __forceinline__ void wait_flags(const gu32* p, u32 tag, u32 first, int code = 0) {
  if (__all(first == tag)) return;
  u32 v;
#pragma unroll 1
  for (int s = 0; s < SPIN_CAP; ++s) {
    v = __hip_atomic_load(p, RLX_AGENT);
    if (__all(v == tag)) break;
    __builtin_amdgcn_s_sleep(1);
  }
}

template <int V> struct ic { static constexpr int value = V; };
struct Args {
  const u16* par[8];
  const u16 *u, *wg, *wu, *wd, *gw;
  u16* out;
  char* ws;
};

__global__ __launch_bounds__(512, 2) void gated_mix_kernel(Args g) {
  const int b = blockIdx.x, xcd = b & 7, bi = b >> 3;
  const int tid = threadIdx.x, l = tid & 63;
  const int w = __builtin_amdgcn_readfirstlane(tid >> 6);
  asm volatile("s_cmp_ge_u32 %0, %3\n\ts_cbranch_scc1 1f\n\ts_getpc_b64 s[4:5]\n\ts_mov_b32 m0, %2\n\ts_and_b32 s4, s4, 0xffffff00\n\t"
               "global_load_lds_dwordx4 %1, s[4:5]\n1:"
               :: "s"(bi * 8 + w), "v"((bi * 8 + w) * 1024 + l * 16), "s"(LDS_SCR + w * 4096), "i"(CODE_KB) : "s4", "s5", "scc", "memory");
  const u64 par_w = ((const __attribute__((address_space(4))) u64*)__builtin_amdgcn_kernarg_segment_ptr())[w];
  asm volatile("" :: "s"(par_w), "s"(g.u), "s"(g.wg), "s"(g.wu), "s"(g.wd), "s"(g.gw), "s"(g.out), "s"(g.ws));
  char* ws = g.ws;
  const rsrc_t rws = mk_rsrc(ws, WS_END);
  const rsrc_t rx = mk_rsrc(g.u, L * D * 2);
  STAMP(0);

  if (bi >= 28) {
    const int hb = b - NMAIN, t0 = 64 * hb;
    constexpr u32 HX = 0, HG = 64 * 896;
    const rsrc_t rgw = mk_rsrc(g.gw, D * 2);
#pragma unroll
    for (int j = 0; j < 7; ++j) dma16(rx, l * 16, (t0 + 8 * w) * 896 + j * 1024, HX + 8 * w * 896 + j * 1024);
    if (w == 0) { dma4_sc1(rws, 0, WS_EPOCH, HG + 1024); dma16(rgw, l * 16, 0, HG); }
    WAITV(0); BAR();
    STAMP(1);
    const u32 tag = __builtin_amdgcn_readfirstlane(lds_ld<u32>(HG + 1024)) + TAG_STEP;
    {
      const int r = 8 * w + (l >> 3);
      const u32 xa = opaque(HX + r * 896 + (l & 7) * 16), ga = opaque(HG + (l & 7) * 16);
      float s0 = 0.f, s1 = 0.f;
#pragma unroll
      for (int j = 0; j < 7; ++j) {
        const v4u xv = lds_ld<v4u>(xa + 128 * j), gv = lds_ld<v4u>(ga + 128 * j);
#pragma unroll
        for (int e = 0; e < 4; e += 2) {
          s0 = dot2bf(xv[e], gv[e], s0);
          s1 = dot2bf(xv[e + 1], gv[e + 1], s1);
        }
      }
      float s = s0 + s1;
      s += __shfl_xor(s, 1); s += __shfl_xor(s, 2); s += __shfl_xor(s, 4);
      s = rbf(s);
      float gt = rbf(__builtin_amdgcn_rcpf(1.f + __builtin_amdgcn_exp2f(-1.44269504f * s)));
      if ((l & 7) < 2) __hip_atomic_store((gu32*)(ws + WS_GATE) + hb * 128 + (r & 15) * 8 + (r >> 4) + 4 * (l & 1), __float_as_uint(gt), RLX_AGENT);
    }
    WAITV(0); BAR();
    if (tid == 0) __hip_atomic_store((gu32*)(ws + WS_FLAG_G) + hb, tag, RLX_AGENT);
    STAMP(2);
    if (hb == NHELP - 1) {
      if (w < 4) wait_flags((const gu32*)(ws + (tid < NMAIN ? WS_FLAG_W : WS_FLAG_G - NMAIN * 4)) + tid, tag, ~tag, 1);
      BAR();
      if (tid == 0) __hip_atomic_store((gu32*)(ws + WS_EPOCH), tag, RLX_AGENT);
    }
    return;
  }

  const int cb = xcd * 28 + bi;
  const u32 vl16 = opaque(l * 16);
  {
    const rsrc_t rp = mk_rsrc((const void*)par_w, w < 6 ? D * NS * 2 : D * 2);
    dma16(rp, vl16, w < 6 ? cb * 1024 : (cb * 4) & ~15, LDS_PAR + w * 1024);
    if (w == 0) dma4_sc1(rws, 0, WS_EPOCH, LDS_PAR + 8192);
  }
  const int mt = (xcd & 3) * 4 + (bi & 3), ft = (xcd >> 2) * 7 + (bi >> 2);
  const int rot = ((bi >> 2) + 2 * (bi & 3) + (xcd & 3)) % 7;
  const int m2 = 4 * xcd + (bi & 3), nt = bi >> 2;
  const int wt = w & 1, wf = w >> 1;

  const rsrc_t rwg = mk_rsrc(g.wg, D * F * 2), rwu = mk_rsrc(g.wu, D * F * 2);
  const u32 vx0 = (l >> 3) * 896 + (((l & 7) ^ (l >> 4)) * 16), vx1 = (vx0 ^ 64) + 8 * 896;
  const u32 vw = (l >> 4) * (F * 2) + (((l & 15) ^ (4 * (l >> 4))) * 16);
  const u32 sx = (mt * 128 + 16 * w) * 896, sw = 8 * w * (F * 2) + ft * 256, lw = w * 2048;
  auto piece = [&](auto S, auto I) {
    constexpr int s = decltype(S)::value, i = decltype(I)::value;
    int sk = s + rot; sk = sk >= 7 ? sk - 7 : sk;
    if constexpr (i < 2) dma16i<LDS_X + (s & 1) * 16384 + i * 1024, 0>(rx, i ? vx1 : vx0, sx + sk * 128, lw);
    else dma16i<LDS_W + (s % 3) * 32768 + (i >= 4) * 16384 + (i & 1) * 1024, 4 * (i & 1) * F * 2>(i < 4 ? rwg : rwu, vw, sw + sk * (64 * F * 2), lw);
  };
#define PIECE(s, i) piece(ic<(s)>{}, ic<(i)>{})
  PIECE(0, 0); PIECE(0, 1); PIECE(0, 2); PIECE(0, 3);
  const int ct = 7 * (cb & 3) + (w < 7 ? w : 6);
  WAITV(4); BAR();
  PRIO(3);
  STAMP(1);

  const u32 srd0 = opaque(LDS_SCR + w * 4096 + (l & 15) * 256 + (((l >> 4) ^ ((l & 15) >> 1)) * 16)), srd1 = opaque(srd0 ^ 64);
  auto frags = [&](v8bf (&f)[4]) {
#pragma unroll
    for (int k = 0; k < 4; ++k) f[k] = as_frag(lds_ld<v4u>(((k & 1) ? srd1 : srd0) + (k >> 1) * 128));
  };
  u32 tag, dsk2;
  v4u tb0, tb1;
  {
    cpx c0, w1, w2, w256, w512;
    u32 swr[8];
    v8bf J[2][4], Hf[4], Hg[4];
    v4u keep = {0, 0, 0, 0};
    const u32 po = LDS_PAR + (w >> 2) * 512 + (w & 3) * 128 + l * 2;
    const float ld = bf_lo(lds_ld<u16>(LDS_PAR + 6144 + ((cb * 4) & 15) + 2 * (w >> 2)));
    const u32 q0 = lds_ld<u16>(po), q1 = lds_ld<u16>(po + 1024), q2 = lds_ld<u16>(po + 2048), q3 = lds_ld<u16>(po + 3072);
    const u32 q4 = lds_ld<u16>(po + 4096), q5 = lds_ld<u16>(po + 5120);
    const u32 ep = lds_ld<u32>(LDS_PAR + 8192), dk = lds_ld<u32>(LDS_PAR + 7168 + ((cb * 4) & 15));
    PIECE(0, 4); PIECE(0, 5);
    swr[0] = opaque(LDS_SCR + w * 4096 + l * 4);
#pragma unroll
    for (int k = 1; k < 8; ++k) swr[k] = opaque(swr[0] ^ (16 * k));
    const float dt = rbf(__builtin_amdgcn_exp2f(ld * 1.44269504f));
    const float a2 = (dt * bf_lo(q0)) * 1.44269504f, bf = (dt * bf_lo(q1)) * 0.15915494f;
    const cpx w8 = cpow_l(a2, bf, 8.f);
    w1 = cpow_l(a2, -bf, 1.f); w256 = cpow_l(a2, -bf, 256.f);
    const cpx cc = {bf_lo(q4), -bf_lo(q5)}, bb = {dt * bf_lo(q2), -dt * bf_lo(q3)};
    tag = __builtin_amdgcn_readfirstlane(ep) + TAG_STEP; dsk2 = __builtin_amdgcn_readfirstlane(dk);
    cpx w16, w24, w32;
    cmul2(w16, c0, w8, w8, cc, bb);
    cmul2(w24, w32, w16, w8, w16, w16);
    cpx j0 = {1.f, 0.f}, j1 = w8, j2 = w16, j3 = w24;
#define JSTEP(k) rows4<((k) & 3) * 4>(keep, swr[((k) & 3) * 2], swr[((k) & 3) * 2 + 1], j0, j1, j2, j3)
#define JNEXT() cmul4(j0, j1, j2, j3, j0, j1, j2, j3, w32)
    JSTEP(0); JNEXT();
    JSTEP(1); JNEXT();
    PRIO(2);
    dma16(mk_rsrc(g.wd, F * D * 2), (l >> 1) * 896 + (l & 1) * 16, 32 * (cb >> 2) * 896 + ct * 32, LDS_WST + lw);
    JSTEP(2); JNEXT();
    JSTEP(3); frags(J[0]); JNEXT();
    JSTEP(4); JNEXT();
    JSTEP(5); JNEXT();
    JSTEP(6); JNEXT();
    cmul2(w2, w512, w1, w1, w256, w256);
    PRIO(1);
    asm volatile("global_load_dwordx4 %0, %2, %3\n\tglobal_load_dwordx4 %1, %2, %3 offset:16"
                 : "=&v"(tb0), "=&v"(tb1) : "v"(tid * 32), "s"(&thread_tab.v[0][0]) : "memory");
    JSTEP(7); frags(J[1]);
    cpx c1, c2, c3, c4, c5, c6, c7, w4, d0, d2, d4, d6, e0, e4, f0, f4, g0, g1, g2, g3;
    cmul2(c1, c2, c0, w1, c0, w2);
    cmul2(c3, w4, c2, w1, w2, w2);
    cmul4(c4, c5, c6, c7, c0, c1, c2, c3, w4);
    rows4<0>(keep, swr[0], swr[1], c0, c1, c2, c3);
    cmul4(d0, d2, d4, d6, c0, c2, c4, c6, w256);
    rows4<4>(keep, swr[2], swr[3], c4, c5, c6, c7);
    cmul4(e0, e4, f0, f4, c0, c4, d0, d4, w512);
    rows4<8>(keep, swr[4], swr[5], d0, d2, d4, d6);
    cmul2(g0, g1, e0, w512, f0, w512);
    PRIO(0);
    rows4<12>(keep, swr[6], swr[7], e0, e4, f0, f4);
    frags(Hf);
    cmul2(g2, g3, g0, w512, g1, w512);
    const cpx g4 = cmul(g2, w512);
    rows4<0>(keep, swr[0], swr[1], g0, g1, g2, g3);
    lds_st<u32>(swr[2] + 4 * 256, pk_bf(g4.x, g4.y));
    frags(Hg);
    SB0();
    PIECE(1, 0); PIECE(1, 1); PIECE(1, 2); PIECE(1, 3); PIECE(1, 4); PIECE(1, 5);
    v4f kc[4];
#pragma unroll
    for (int x = 0; x < 2; ++x)
#pragma unroll
      for (int k = 0; k < 4; ++k)
#pragma unroll
        for (int y = 0; y < 2; ++y)
          kc[2 * x + y] = __builtin_amdgcn_mfma_f32_16x16x32_bf16(x ? Hg[k] : Hf[k], J[y][k], k == 0 ? (v4f){0.f, 0.f, 0.f, 0.f} : kc[2 * x + y], 0, 0, 0);
    const u32 kpw = LDS_SCR + w * 4096 + vl16;
#pragma unroll
    for (int t = 0; t < 4; ++t) lds_st<v4f>(kpw + t * 1024, kc[t]);
  }
  STAMP(13);
  const int G = l >> 4, q = (l & 15) >> 2, p = l & 3;
  const int usl = 2 * rot + (ft >= 7);
  asm volatile("s_waitcnt vmcnt(6)" : "+v"(tb0), "+v"(tb1) :: "memory");
  const u32 a_ut = (tb1.z >> 16) ^ ((usl & 1) << 6);
  u32 xa[4], wa[3];
  xa[0] = opaque(tb1.w & 0xffff);
#pragma unroll
  for (int s = 1; s < 4; ++s) xa[s] = opaque(xa[0] ^ (32 * s));
  wa[0] = opaque(tb1.w >> 16);
  wa[1] = opaque(wa[0] + 32768); wa[2] = opaque(wa[0] + 65536);
  const u32 tag_v = opaque(tag);
  STAMP(2);

  v16f accg[2] = {}, accu[2] = {};
  v4u ut4, gate4;
  v4u wfr[28];
  const int kr = w, krot = nt & 4;
  u32 koff[7];
#pragma unroll
  for (int i = 0; i < 7; ++i) koff[i] = (kr * 7 + (i + krot >= 7 ? i + krot - 7 : i + krot)) * 1024;
  const u32 wd_so = WS_WT + nt * 56 * 4096;
  auto ldw = [&](int i) { wfr[i] = __builtin_amdgcn_raw_buffer_load_b128(rws, vl16 + (i & 3) * 1024, wd_so + 4 * koff[i >> 2], 16); };
  u32 hadr, hadr2, ka, ka8, ub, yb, yo, uo, xe[2][5], xkb, xz, uia;
  auto img_writes = [&]() {
#pragma unroll
    for (int o = 0; o < 2; ++o) {
      lds_st<v4u>(xkb + (3 - o) * KCOPY, (v4u){xe[o][0], xe[o][1], xe[o][2], xe[o][3]});
      lds_st<v4u>(xkb + (1 - o) * KCOPY, (v4u){xe[o][1], xe[o][2], xe[o][3], xe[o][4]});
    }
    lds_st<v4u>(xz, (v4u){0, 0, 0, 0});
    lds_st<v4u>(uia, ut4);
  };
  v2f nl2e = {-1.44269504f, -1.44269504f};
  v4u hst[4];
  auto silu8 = [&](int gI) {
    const int j = gI >> 1, r = 8 * (gI & 1);
    v2f gv[4], pv[4], ev[4];
#pragma unroll
    for (int e = 0; e < 4; ++e) { gv[e] = (v2f){accg[j][r + 2 * e], accg[j][r + 2 * e + 1]}; ev[e] = gv[e] * nl2e; }
    SB0();
#pragma unroll
    for (int e = 0; e < 4; ++e) ev[e] = (v2f){__builtin_amdgcn_exp2f(ev[e].x), __builtin_amdgcn_exp2f(ev[e].y)};
    SB0();
#pragma unroll
    for (int e = 0; e < 4; ++e) { ev[e] = ev[e] + 1.f; pv[e] = gv[e] * (v2f){accu[j][r + 2 * e], accu[j][r + 2 * e + 1]}; }
    SB0();
#pragma unroll
    for (int e = 0; e < 4; ++e) ev[e] = (v2f){__builtin_amdgcn_rcpf(ev[e].x), __builtin_amdgcn_rcpf(ev[e].y)};
    SB0();
#pragma unroll
    for (int e = 0; e < 4; ++e) pv[e] = pv[e] * ev[e];
    SB0();
#pragma unroll
    for (int e = 0; e < 4; ++e) hst[gI][e] = pk_bf(pv[e].x, pv[e].y);
    st16<16>(hst[gI], rws, (j ? hadr2 : hadr) + (r >> 3) * 512);
    if (gI > 0) asm volatile("" :: "v"(hst[gI - 1]));
    SB0();
  };
  {
    v8bf fg[2], fu[2], fx0[2], fx1[2];
    auto rd = [&](int buf, int st, int ks) {
      const u32 wb = wa[st % 3] + ks * 4096, xb = xa[ks] + (st & 1) * 16384;
      fg[buf] = as_frag(lds_tr(wb), lds_tr(wb + 1024));
      fx0[buf] = as_frag(lds_ld<v4u>(xb));
      fu[buf] = as_frag(lds_tr(wb + 16384), lds_tr(wb + 16384 + 1024));
      fx1[buf] = as_frag(lds_ld<v4u>(xb + 4096));
    };
    v4f ks4[4];
    u32 kp01, kp23;
    auto kgen = [&](int n) {
      if (n == 22) {
#pragma unroll
        for (int k = 0; k < 4; ++k) ks4[k] = lds_ld<v4f>(LDS_SCR + (w & 4) * 4096 + (w & 3) * 1024 + vl16 + k * 4096);
      }
      if (n == 24) {
        const v4f s = add4s(add4s(ks4[0], ks4[1]), add4s(ks4[2], ks4[3]));
        const bool keep = !(w & 2) || l < 32;
        kp01 = opaque(keep ? pk_bf(s[0], s[1]) : 0); kp23 = opaque(keep ? pk_bf(s[2], s[3]) : 0);
      }
      if (n == 25) lds_st<v2u>(LDS_TAB + w * 512 + (vl16 >> 1), (v2u){kp01, kp23});
    };
    u32 xv[11], uta, s_ut;
    auto expand = [&](int step) {
      if (step == 0) {
        xv[0] = lds_ld<u16>(add_half<0>(LDS_TAB, tb0.x)); xv[1] = lds_ld<u16>(add_half<1>(LDS_TAB, tb0.x));
        xv[2] = lds_ld<u16>(add_half<0>(LDS_TAB, tb0.y)); xv[3] = lds_ld<u16>(add_half<1>(LDS_TAB, tb0.y));
      } else if (step == 1) {
        xv[4] = lds_ld<u16>(add_half<0>(LDS_TAB, tb0.z)); xv[5] = lds_ld<u16>(add_half<1>(LDS_TAB, tb0.z));
        xv[6] = lds_ld<u16>(add_half<0>(LDS_TAB, tb0.w)); xv[7] = lds_ld<u16>(add_half<1>(LDS_TAB, tb0.w));
      } else if (step == 2) {
        xv[8] = lds_ld<u16>(add_half<0>(LDS_TAB, tb1.x)); xv[9] = lds_ld<u16>(add_half<1>(LDS_TAB, tb1.x));
        xv[10] = lds_ld<u16>(add_half<0>(LDS_TAB, tb1.y));
      } else if (step < 16) {
        const int o = step - 14;
#pragma unroll
        for (int j = 0; j < 5; ++j) xe[o][j] = opaque(xv[2 * j + o] | (xv[2 * j + o + 1] << 16));
      }
    };
    v2u utl, uth;
    const u32 wfo = WS_FLAG_W + 16 * (l < 56 ? l : 0);
    const gu32* ufp = (const gu32*)(ws + (l < 16 ? WS_FLAG_U + ((cb >> 4) * 16 + l) * 4 : WS_FLAG_G + m2 * 4));
    v4u wflag;
    u32 uflag;
    auto side = [&](auto S, auto KS, auto H) {
      constexpr int s = decltype(S)::value, ks = decltype(KS)::value, h = decltype(H)::value;
      constexpr int st = 4 * s + ks;
      if constexpr (h == 0 || h == 2) {
        constexpr int i = h >> 1;
        if constexpr (ks == 3) { if constexpr (s < 5) PIECE(s + 2, i); }
        else if constexpr (s == 0) { if constexpr (ks < 2) PIECE(2, 2 + 2 * ks + i); }
        else if constexpr (ks < 2 && s + 2 < 7) PIECE(s + 2, 2 + 2 * ks + i);
        if constexpr (st >= 20) ldw(2 * (st - 20) + i);
        if constexpr (st == 3 && h == 2) st16<16>((v4u){utl.x, utl.y, uth.x, uth.y}, rws, s_ut);
      } else {
        constexpr int v = 2 * st + (h >> 1);
        if constexpr (v == 1) kgen(22);
        if constexpr (v == 2) s_ut = opaque(WS_UT + (((2 * mt + (wf >> 1)) * 28 + 2 * usl + wt) * 2 + (wf & 1)) * 1024 + (l & 15) * 64 + G * 16);
        if constexpr (v == 4) kgen(24);
        if constexpr (v == 11) kgen(25);
        if constexpr (v >= 16 && v < 19) expand(v - 16);
        if constexpr (v == 21 || v == 22) expand(v - 7);
        if constexpr (v == 24) {
          xkb = opaque(((w >> 2) ? LDS_KIMG1 : LDS_KIMG0) + (tid & 255) * 16);
          xz = opaque((tb1.y >> 16) << 4);
          uia = opaque((tb1.z & 0xffff) << 4);
        }
        if constexpr (v == 26) uta = opaque((tid >> 4) * (56 * 1024) + ((tid >> 3) & 1) * 1024 + (tid & 7) * 16);
        if constexpr (v == 30) {
          hadr = opaque(WS_H + ((mt * 8 + 4 * wt + ((l & 31) >> 4)) * 56 + ft * 4 + wf) * 1024 + (l & 15) * 16 + (l >> 5) * 256);
          hadr2 = opaque(hadr + 2 * 56 * 1024);
          asm volatile("" : "+v"(nl2e));
        }
        if constexpr (v == 32) {
          ka = opaque(((w >> 2) ? LDS_KIMG1 : LDS_KIMG0) + (l & 3) * KCOPY + 2 * (2028 + 8 * G - 4 * q) - 512 * 7 - 128 * (w & 3) - 64);
          ka8 = opaque(ka + 8);
        }
        if constexpr (v == 33) ub = opaque(LDS_UIMG + (w >> 2) * UCH + 2 * (240 + 16 * (l & 15) + 8 * G) - 128 * (w & 3) - 64);
        if constexpr (v == 34) yb = opaque(LDS_Y + ((w & 3) * 2 + (w >> 2)) * 8192 + (16 * (l & 15) + 4 * (G ^ ((l >> 1) & 3))) * 4);
        if constexpr (v == 35) { yo = opaque(LDS_Y + (tid >> 2) * 64 + (((tid & 3) ^ ((tid >> 3) & 3)) * 16)); uo = opaque(LDS_UIMG + 512 + tid * 8); }
      }
    };
    auto mm = [&](int buf, auto S, auto KS) {
      accg[0] = __builtin_amdgcn_mfma_f32_32x32x16_bf16(fg[buf], fx0[buf], accg[0], 0, 0, 0);
      SB0(); side(S, KS, ic<0>{}); SB0();
      accu[0] = __builtin_amdgcn_mfma_f32_32x32x16_bf16(fu[buf], fx0[buf], accu[0], 0, 0, 0);
      SB0(); side(S, KS, ic<1>{}); SB0();
      accg[1] = __builtin_amdgcn_mfma_f32_32x32x16_bf16(fg[buf], fx1[buf], accg[1], 0, 0, 0);
      SB0(); side(S, KS, ic<2>{}); SB0();
      accu[1] = __builtin_amdgcn_mfma_f32_32x32x16_bf16(fu[buf], fx1[buf], accu[1], 0, 0, 0);
      SB0(); side(S, KS, ic<3>{});
      asm volatile("" :: "v"(fg[buf]), "v"(fu[buf]), "v"(fx0[buf]), "v"(fx1[buf]));
      SB0();
    };
    auto stage = [&](auto S) {
      constexpr int s = decltype(S)::value;
      PRIO(2); rd(1, s, 1); SB0(); mm(0, S, ic<0>{});
      PRIO(1); rd(0, s, 2); SB0(); mm(1, S, ic<1>{});
      PRIO(0); rd(1, s, 3); SB0(); mm(0, S, ic<2>{});
      PRIO(3);
      if constexpr (s < 6) {
        WAITL0();
        if constexpr (s < 2) WAITV(5);
        else if constexpr (s < 4) WAITV(4);
        else if constexpr (s == 4) {
          asm volatile("s_waitcnt vmcnt(4)" : "+v"(wflag) :: "memory");
          if (w == 0 && !__all(wflag.x == tag && wflag.y == tag && wflag.z == tag && wflag.w == tag)) {
#pragma unroll 1
            for (int sp = 0; sp < SPIN_CAP; ++sp) {
              asm volatile("" ::: "memory");
              const v4u f = __builtin_amdgcn_raw_buffer_load_b128(rws, wfo, 0, 16);
              if (__all(f.x == tag && f.y == tag && f.z == tag && f.w == tag)) break;
            }
          }
        } else {
          asm volatile("s_waitcnt vmcnt(%1)" : "+v"(uflag) : "i"(6) : "memory");
          if (w == 0) wait_flags(ufp, tag, uflag, 2);
        }
        BAR();
        rd(0, s + 1, 0);
        if constexpr (s == 1) { if (w == 0) flag_store(rws, WS_FLAG_W + cb * 4, tag_v); }
        if constexpr (s == 2) { if (w == 0) flag_store(rws, WS_FLAG_U + (usl * 16 + mt) * 4, tag_v); }
        if constexpr (s == 3)
          asm volatile("s_cmp_lg_u32 %3, 0\n\ts_cbranch_scc1 1f\n\tbuffer_load_dwordx4 %0, %1, %2, 0 offen sc1\n1:"
                       : "=v"(wflag) : "v"(wfo), "s"(rws), "s"(w) : "memory", "scc");
        if constexpr (s == 4)
          asm volatile("s_cmp_lg_u32 %2, 0\n\ts_cbranch_scc1 1f\n\tglobal_load_dword %0, %1, off sc1\n1:"
                       : "=v"(uflag) : "v"(ufp), "s"(w) : "memory", "scc");
        if constexpr (s >= 4) __builtin_amdgcn_fence(__ATOMIC_ACQUIRE, "wavefront");
        if constexpr (s == 5) {
          ut4 = __builtin_amdgcn_raw_buffer_load_b128(rws, uta, WS_UT + (cb >> 3) * 2048 + (cb & 7) * 128, 16);
          gate4 = __builtin_amdgcn_raw_buffer_load_b128(rws, (tid >> 5) * 32, WS_GATE + m2 * 512 + (nt & 3) * 4, 16);
        }
        STAMP(s < 5 ? 22 + s : 20);
        if constexpr (s == 5) WSC(0);
        SB0();
      }
      mm(1, S, ic<3>{});
    };
    {
      const u32 a = LDS_WST + lw + (16 * (G >> 1) + 4 * (G & 1) + q) * 32 + p * 8;
      const v2u lo = lds_tr(a), hi = lds_tr(a + 256);
      st16<16>((v4u){lo.x, lo.y, hi.x, hi.y}, rws, WS_WT + (((ct >> 2) * 56 + (cb >> 2)) * 4 + (ct & 3)) * 1024 + vl16);
    }
    WAITL0(); BAR();
    STAMP(3);
    rd(0, 0, 0);
    utl = lds_tr(a_ut); uth = lds_tr((a_ut ^ 32) + 512);
    SB0();
    stage(ic<0>{}); stage(ic<1>{}); stage(ic<2>{}); stage(ic<3>{}); stage(ic<4>{}); stage(ic<5>{}); stage(ic<6>{});
  }
  STAMP(4);
  WSC(1);
  const gu32* hfp = (const gu32*)(ws + WS_FLAG_H) + (m2 * 56 + 7 * kr + (l < 7 ? l : 0)) * HFS;
  u32 hflag;
  v4u hf[28];
  u32 hso[4];
#pragma unroll
  for (int tt = 0; tt < 4; ++tt) hso[tt] = WS_H + (m2 * 4 + ((tt + nt) & 3)) * 56 * 1024;
  auto ldh = [&](int n) {
    const int i = n % 7;
    hf[n] = __builtin_amdgcn_raw_buffer_load_b128(rws, vl16, hso[n / 7] + koff[i], 16);
    SB0();
  };
  constexpr int AHEAD = 9;
  {
    v4f acc[8];
    v8bf A[8][2], B[8][2];
    auto lda_ = [&](int pp) {
#pragma unroll
      for (int qq = 0; qq < 2; ++qq)
        A[pp][qq] = as_frag(lds_ld<v2u>(ka + 64 - 64 * qq + 512 * (7 - pp)), lds_ld<v2u>(ka8 + 64 - 64 * qq + 512 * (7 - pp)));
    };
#pragma unroll
    for (int gI = 0; gI < 4; ++gI) {
      silu8(gI);
      if (gI == 3) img_writes();
      SB0();
    }
    WAITL0(); BAR();
    lda_(0);
#pragma unroll
    for (int e = 7; e >= 0; --e) B[e][0] = as_frag(lds_ld<v4u>(ub + 64 + 512 * e));
#pragma unroll
    for (int e = 7; e >= 0; --e) B[e][1] = as_frag(lds_ld<v4u>(ub + 512 * e));
    lda_(1);
    asm volatile("" :: "v"(hst[3]));
    SB0();
    PRIO(2);
    asm volatile("" ::: "memory");
    STAMP(5);
    WST(0);
    WSC(2);
#pragma unroll
    for (int pp = 0; pp < 8; ++pp) {
#pragma unroll
      for (int qq = 0; qq < 2; ++qq) {
#pragma unroll
        for (int c = 7; c >= pp; --c) {
          acc[c] = __builtin_amdgcn_mfma_f32_16x16x32_bf16(A[pp][qq], B[c - pp][qq], (qq == 0 && pp == 0) ? (v4f){0.f, 0.f, 0.f, 0.f} : acc[c], 0, 0, 0);
          if (c == 5 && pp < 3) { SB0(); ldw(16 + 4 * pp + 2 * qq); SB0(); }
        }
        SB0();
        if (qq == 0) { if (pp < 6) lda_(pp + 2); if (pp > 0) lds_st<v4f>(yb + (pp - 1) * 1024, acc[pp - 1]); }
        if (pp < 3) ldw(17 + 4 * pp + 2 * qq);
        const int hs = 2 * pp + qq;
        if (hs == HF_P) {
          WSC(3);
          asm volatile("s_waitcnt vmcnt(%0)" :: "i"(2 * HF_P + 2) : "memory");
          flag_store(rws, WS_FLAG_H + ((2 * mt + wt) * 56 + 4 * ft + wf) * (HFS * 4), tag_v);
          STAMP(6);
          WST(1);
          WSC(4);
        }
        if (hs == HL_P - 2) hflag = __hip_atomic_load(hfp, RLX_AGENT);
        if (hs == HL_P) {
          WSC(5);
          STAMP(7);
          WST(2);
          wait_flags(hfp, tag, hflag, 3);
          __builtin_amdgcn_fence(__ATOMIC_ACQUIRE, "wavefront");
          STAMP(8);
          WST(3);
        }
        if (hs >= HL_P && 2 * (hs - HL_P) < AHEAD - 1) { ldh(2 * (hs - HL_P)); ldh(2 * (hs - HL_P) + 1); }
        if (qq == 1 && pp == 1) PRIO(1);
        if (qq == 1 && pp == 3) PRIO(0);
        SB0();
      }
    }
    lds_st<v4f>(yb + 7 * 1024, acc[7]);
  }
  WSC(6);

  {
    v4u a_out;
    v4f yv[8], ys[2];
    v2u uv[2];
    auto a_step = [&](int k) {
      if (k < 2) {
        uv[k] = lds_ld<v2u>(uo + k * UCH);
#pragma unroll
        for (int z = 0; z < 4; ++z) yv[4 * k + z] = lds_ld<v4f>(yo + k * 8192 + z * 16384);
      } else if (k < 4) {
        ys[k - 2] = add4s(add4s(yv[4 * k - 8], yv[4 * k - 7]), add4s(yv[4 * k - 6], yv[4 * k - 5]));
      } else {
        const int e = k - 4;
        const float k0 = bf_lo(dsk2), k1 = bf_hi(dsk2);
        a_out[2 * e] = opaque(pk_bf(__builtin_fmaf(bf_lo(uv[0][e]), k0, ys[0][2 * e]), __builtin_fmaf(bf_lo(uv[1][e]), k1, ys[1][2 * e])));
        a_out[2 * e + 1] = opaque(pk_bf(__builtin_fmaf(bf_hi(uv[0][e]), k0, ys[0][2 * e + 1]), __builtin_fmaf(bf_hi(uv[1][e]), k1, ys[1][2 * e + 1])));
      }
    };
    auto sta = [&]() {
      const u32 ao = vl16 + WS_A2 + cb * 16384 + w * 2048;
      st16<16>((v4u){a_out.x, tag, a_out.y, tag}, rws, ao);
      st16<16>((v4u){a_out.z, tag, a_out.w, tag}, rws, ao + 1024);
      SB0();
    };
    STAMP(27);
    WST(4);
    STAMP(12);
    WSC(7);
    v4u a4[2];
    u32 a_vo, pw, prd, asw, asr, oad;
    const u32 a_so = WS_A2 + 32 * nt * 16384 + (m2 >> 2) * 2048 + (m2 & 3) * 256;
    auto lda = [&]() {
      a4[0] = __builtin_amdgcn_raw_buffer_load_b128(rws, a_vo, a_so, 16);
      a4[1] = __builtin_amdgcn_raw_buffer_load_b128(rws, a_vo + 1024, a_so, 16);
    };
    v4f acc[2][4];
    v2f pr[8], bs[4][2];
    u32 av[4], oso[4], prd2;
    const rsrc_t ro = mk_rsrc(g.out, L * D * 2);
    auto pread = [&](int tt) {
#pragma unroll
      for (int k = 0; k < 8; ++k) pr[k] = lds_ld<v2f>((tt == 2 ? prd2 : prd + (tt == 1) * PBUF) + k * PWAVE);
    };
    auto psum = [&](int tt, int h) { bs[tt][h] = add2x2s(pr[4 * h], pr[4 * h + 1], pr[4 * h + 2], pr[4 * h + 3]); };
    auto mix = [&](int tt) {
      __builtin_amdgcn_raw_buffer_store_b32(mix2(__uint_as_float(gate4[tt]), av[tt], bs[tt][0], bs[tt][1]), ro, oad, oso[tt], 0);
    };
#pragma unroll
    for (int tt = 0; tt < 3; ++tt) {
#pragma unroll
      for (int i = 0; i < 7; ++i) {
        const int n = 7 * tt + i;
        if (i == 0) PRIO(3);
        if (i == 2) PRIO(2);
        if (i == 4) PRIO(1);
        if (i == 6) PRIO(0);
        if (n + AHEAD - 1 < 28) ldh(n + AHEAD - 1);
        if (n == 12) sta();
        if (n == LDA_N) lda();
        SB0();
#pragma unroll
        for (int c = 0; c < 4; ++c)
          acc[tt & 1][c] = __builtin_amdgcn_mfma_f32_16x16x32_bf16(as_frag(wfr[4 * i + c]), as_frag(hf[n]),
                                                                   i == 0 ? (v4f){0.f, 0.f, 0.f, 0.f} : acc[tt & 1][c], 0, 0, 0);
        SB0();
        if (n == 0) { STAMP(29); WST(5); }
        if (n == 0) pw = opaque(LDS_P + w * PWAVE + (l & 15) * PROW + G * 16);
        if (n == 1) { prd = opaque(LDS_P + (tid >> 5) * PROW + (tid & 31) * 8); prd2 = opaque(prd - LDS_P); }
        if (n == 3) a_vo = opaque((tid >> 4) * 16384 + (tid & 15) * 16);
        if (n == 4) asw = opaque(LDS_AST + (tid >> 4) * 272 + ((tid - 4 * nt) & 15) * 16);
        if (n == 5) asr = opaque(LDS_AST + (tid & 31) * 272 + (tid >> 5) * 4);
        if (n == 11) oad = opaque((tid >> 5) * (D * 2) + (tid & 31) * 4);
        if (n == 12) {
#pragma unroll
          for (int k = 0; k < 4; ++k) {
            oso[k] = (64 * m2 + 16 * ((k + nt) & 3)) * (D * 2) + 128 * nt;
            asm volatile("" : "+s"(oso[k]));
          }
        }
        if (tt > 0 && i < 4) lds_st<v4f>(pw + (tt - 1) * PBUF + i * 64, acc[(tt - 1) & 1][i]);
        if (tt > 1 && i == 0) pread(tt - 2);
        if (tt > 1 && (i == 2 || i == 3)) psum(tt - 2, i - 2);
        if (n == 7) { a_step(0); a_step(1); }
        if (n >= 9 && n < 11) { a_step(n - 7); }
        if (n == 11) { a_step(4); a_step(5); }
        if (i == 6) { WAITL0(); BAR(); }
        if (n > 0) asm volatile("" :: "v"(hf[n - 1]));
        SB0();
      }
      if (tt == 0) STAMP(9);
      if (tt == 1) STAMP(16);
      if (tt == 2) STAMP(17);
    }
    const u32 pw2 = pw - LDS_P;
#pragma unroll
    for (int c = 0; c < 4; ++c) {
      if (c == 0) PRIO(3);
      if (c == 1) PRIO(2);
      if (c == 2) PRIO(1);
      if (c == 3) PRIO(0);
#pragma unroll
      for (int i = 0; i < 7; ++i) {
        acc[1][c] = __builtin_amdgcn_mfma_f32_16x16x32_bf16(as_frag(wfr[4 * i + c]), as_frag(hf[21 + i]),
                                                            i == 0 ? (v4f){0.f, 0.f, 0.f, 0.f} : acc[1][c], 0, 0, 0);
        SB0();
        if (c == 0 && i == 0) pread(1);
        if (i == 2) lds_st<v4f>(pw2 + c * 64, acc[0][c]);
        if (i == 4 && c > 0) lds_st<v4f>(pw + (c - 1) * 64, acc[1][c - 1]);
        if (c == 1 && i == 0) psum(1, 0);
        if (c == 1 && i == 5) psum(1, 1);
        if (c == 3 && i == 0) {
          if (!__all(((a4[0].y ^ tag) | (a4[0].w ^ tag) | (a4[1].y ^ tag) | (a4[1].w ^ tag)) == 0)) {
#pragma unroll 1
            for (int sp = 0; sp < SPIN_CAP; ++sp) {
              asm volatile("" ::: "memory");
              lda();
              if (__all(a4[0].y == tag && a4[0].w == tag && a4[1].y == tag && a4[1].w == tag)) break;
            }
          }
          lds_st<v2u>(asw, (v2u){a4[0].x, a4[0].z}); lds_st<v2u>(asw + 8, (v2u){a4[1].x, a4[1].z});
        }
        SB0();
      }
    }
    STAMP(18);
    lds_st<v4f>(pw + 3 * 64, acc[1][3]);
    WAITL0(); BAR();
    STAMP(10);
#pragma unroll
    for (int e = 0; e < 4; ++e) av[e] = lds_ld<u32>(asr + e * 64);
    pread(2);
    v2f pq[8];
#pragma unroll
    for (int k = 0; k < 8; ++k) pq[k] = lds_ld<v2f>(prd + k * PWAVE);
    mix(0); mix(1);
    psum(2, 0); psum(2, 1);
    mix(2);
    bs[3][0] = add2x2s(pq[0], pq[1], pq[2], pq[3]); bs[3][1] = add2x2s(pq[4], pq[5], pq[6], pq[7]);
    mix(3);
  }
}

extern "C" void kernel_launch(void* const* d_in, const int* in_sizes, int n_in, void* d_out, int out_size, void* d_ws,
                              size_t ws_size, hipStream_t stream) {
  Args a;
  for (int i = 0; i < 8; ++i) a.par[i] = (const u16*)d_in[1 + i];
  a.u = (const u16*)d_in[0];
  a.wg = (const u16*)d_in[10]; a.wu = (const u16*)d_in[11]; a.wd = (const u16*)d_in[12];
  a.gw = (const u16*)d_in[13];
  a.out = (u16*)d_out;
  a.ws = (char*)d_ws;
  (void)hipFuncSetAttribute((const void*)gated_mix_kernel, hipFuncAttributeMaxDynamicSharedMemorySize, LDS_TOTAL);
  gated_mix_kernel<<<NMAIN + NHELP, 512, LDS_TOTAL, stream>>>(a);
}
```

```cpp
#include <hip/hip_runtime.h>

typedef unsigned short u16;
typedef unsigned u32;
typedef unsigned long long u64;
typedef u32 v4u __attribute__((ext_vector_type(4)));
typedef u32 v2u __attribute__((ext_vector_type(2)));
typedef float v2f __attribute__((ext_vector_type(2)));
typedef float v4f __attribute__((ext_vector_type(4)));
typedef float v16f __attribute__((ext_vector_type(16)));
typedef __bf16 v8bf __attribute__((ext_vector_type(8)));
typedef __bf16 v2bf __attribute__((ext_vector_type(2)));
typedef short v4s __attribute__((ext_vector_type(4)));
#define LAS __attribute__((address_space(3)))
#define GAS __attribute__((address_space(1)))
typedef GAS u32 gu32;
typedef __amdgpu_buffer_rsrc_t rsrc_t;

#define AFFINE 0

constexpr int L = 2048, D = 448, NS = 256, F = 1792;
constexpr int NMAIN = 224, NHELP = 32;

constexpr u32 WS_EPOCH = 0;
constexpr u32 WS_FLAG_W = 1024;
constexpr u32 WS_FLAG_G = WS_FLAG_W + 1024;
constexpr u32 WS_FLAG_U = WS_FLAG_G + 1024;
constexpr u32 WS_GATE = 245760;
#define HF_P 0
#define LDA_N 20
#define AST_N 19
#define HL_P 9
#define CHK_N 99
#define AHD 9
#define HL_D 2
#define HFS 32
constexpr u32 WS_FLAG_H = 16384;
constexpr u32 WS_UT = 262144;
constexpr u32 WS_A2 = WS_UT + D * L * 2;
constexpr u32 WS_H = WS_A2 + D * L * 4;
constexpr u32 WS_WT = WS_H + L * F * 2;
constexpr u32 WS_END = WS_WT + F * D * 2;
constexpr u32 WS_STAMP = 16u << 20;
constexpr u32 TAG_STEP = 0x9E3779B9u;

constexpr u32 LDS_X = 0;
constexpr u32 LDS_W = 32768;
constexpr u32 LDS_SCR = 131072;
constexpr u32 LDS_TOTAL = 163840;
constexpr u32 LDS_PAR = LDS_W + 2 * 32768 + 16384;
constexpr u32 LDS_WST = LDS_W + 2 * 32768;
constexpr u32 KCOPY = 4160, KCH = 4 * KCOPY;
constexpr u32 LDS_KIMG0 = LDS_W + 32768, LDS_KIMG1 = LDS_SCR + 4096;
constexpr u32 UCH = 512 + 4096;
constexpr u32 LDS_UIMG = LDS_KIMG0 + KCH;
constexpr u32 LDS_TAB = LDS_SCR;
constexpr u32 LDS_Y = 0;
constexpr u32 PROW = 272, PWAVE = 16 * PROW, PBUF = 8 * PWAVE;
constexpr u32 LDS_P = 91648;
constexpr u32 LDS_AST = 65536;

struct ThreadTab { u32 v[512][8]; };
constexpr int round_bf16(int s) {
  const int sh = s >= 1024 ? 3 : s >= 512 ? 2 : s >= 256 ? 1 : 0;
  return sh ? ((s + (1 << (sh - 1)) - 1 + ((s >> sh) & 1)) >> sh) << sh : s;
}
constexpr int tab_pos(int s) {
  if (s < 0) return 2 * 512 + 32 * 8;
  const int v = round_bf16(s);
  int x = 0, row = 0, j = 0;
  if (v < 256) { j = v >> 3; row = v & 7; }
  else if (v < 512) { j = (v - 256) >> 3; row = 8 + ((v & 7) >> 1); }
  else if (v < 1024) { j = (v & 255) >> 3; row = 12 + 2 * ((v - 512) >> 8) + ((v & 7) >> 2); }
  else { x = 1; j = (v & 255) >> 3; row = (v - 1024) >> 8; }
  return (2 * x + (j >> 4)) * 512 + ((j & 15) + 16 * (row >> 2)) * 8 + (row & 3) * 2;
}
constexpr ThreadTab make_tab() {
  ThreadTab t{};
  for (int tid = 0; tid < 512; ++tid) {
    const int l = tid & 63, w = tid >> 6, wt = w & 1, wf = w >> 1, G = l >> 4, q = (l & 15) >> 2, p = l & 3;
    u32 e[16] = {};
    for (int j = 0; j < 11; ++j) e[j] = (tid >> 8) * 2048 + tab_pos(2047 - 8 * (tid & 255) - j);
    e[11] = ((tid & 127) < 16 ? ((tid & 8) ? LDS_KIMG1 : LDS_KIMG0) + ((tid >> 1) & 3) * KCOPY + 4096 + (tid & 1) * 16
                              : LDS_UIMG + ((tid >> 5) & 1) * UCH + (tid & 31) * 16) >> 4;
    e[12] = (LDS_UIMG + ((tid >> 2) & 1) * UCH + 512 + (tid >> 3) * 64 + (tid & 3) * 16) >> 4;
    e[13] = (4 * wf + G) * 1024 + q * 128 + ((2 * wt + (p >> 1)) ^ (4 * (G & 1) + (q >> 1))) * 16 + 8 * (p & 1);
    e[14] = LDS_X + (8 * wt + ((l & 31) >> 3)) * 1024 + (l & 7) * 128 + (((l >> 5) ^ ((l >> 1) & 7)) * 16);
    e[15] = LDS_W + (8 * (G >> 1) + q) * 256 + (((4 * wf + 2 * (G & 1) + (p >> 1)) ^ (4 * q)) * 16) + 8 * (p & 1);
    for (int k = 0; k < 8; ++k) t.v[tid][k] = e[2 * k] | (e[2 * k + 1] << 16);
  }
  return t;
}
static __device__ const ThreadTab thread_tab = make_tab();

#define SB0() __builtin_amdgcn_sched_barrier(0)
#define WAITV(n) asm volatile("s_waitcnt vmcnt(" #n ")" ::: "memory")
#define WAITL0() asm volatile("s_waitcnt lgkmcnt(0)" ::: "memory")
#define BAR() __builtin_amdgcn_s_barrier()
#define PRIO(n) __builtin_amdgcn_s_setprio(n)
#define RLX_AGENT __ATOMIC_RELAXED, __HIP_MEMORY_SCOPE_AGENT
constexpr int CODE_KB = 16;
constexpr int SPIN_CAP = 1 << 18;

#define STAMP(i) do { } while (0)
#define WST(i) do { } while (0)
#define WSC(i) do { } while (0)

extern __shared__ __attribute__((aligned(16))) char smem[];

__device__ __forceinline__ float bf_lo(u32 x) { return __uint_as_float(x << 16); }
__device__ __forceinline__ float bf_hi(u32 x) { return __uint_as_float(x & 0xffff0000u); }
__device__ __forceinline__ u32 pk_bf(float lo, float hi) {
  v2f v = {lo, hi};
  return __builtin_bit_cast(u32, __builtin_convertvector(v, v2bf));
}
__device__ __forceinline__ float dot2bf(u32 a, u32 b, float c) {
  return __builtin_amdgcn_fdot2_f32_bf16(__builtin_bit_cast(v2bf, a), __builtin_bit_cast(v2bf, b), c, false);
}
__device__ __forceinline__ float rbf(float x) { return bf_lo(pk_bf(x, x)); }
template <class T> __device__ __forceinline__ T lds_ld(u32 off) { return *(const LAS T*)((LAS char*)smem + off); }
template <class T> __device__ __forceinline__ void lds_st(u32 off, T v) { *(LAS T*)((LAS char*)smem + off) = v; }
__device__ __forceinline__ v2u lds_tr(u32 off) {
  return __builtin_bit_cast(v2u, __builtin_amdgcn_ds_read_tr16_b64_v4i16((LAS v4s*)((LAS char*)smem + off)));
}
__device__ __forceinline__ v8bf as_frag(v4u x) { return __builtin_bit_cast(v8bf, x); }
__device__ __forceinline__ v8bf as_frag(v2u a, v2u b) { v4u x = {a.x, a.y, b.x, b.y}; return __builtin_bit_cast(v8bf, x); }
__device__ __forceinline__ rsrc_t mk_rsrc(const void* p, u32 bytes) {
  return __builtin_amdgcn_make_buffer_rsrc((void*)p, (short)0, (int)bytes, 0x00020000);
}
template <int AUX> __device__ __forceinline__ void st16(v4u v, rsrc_t r, u32 voff) {
  __builtin_amdgcn_raw_buffer_store_b128(v, r, voff, 0, AUX);
}
__device__ __forceinline__ void dma16(rsrc_t r, u32 voff, u32 soff, u32 lds) {
  asm volatile("s_mov_b32 m0, %0\n\ts_nop 0\n\tbuffer_load_dwordx4 %1, %2, %3 offen lds"
               :: "s"(lds), "v"(voff), "s"(r), "s"(soff) : "memory");
}
template <int LO, int SO> __device__ __forceinline__ void dma16i(rsrc_t r, u32 voff, u32 soff, u32 lds) {
  u32 t;
  asm volatile("s_add_u32 m0, %1, %5\n\ts_add_u32 %0, %4, %6\n\tbuffer_load_dwordx4 %2, %3, %0 offen lds"
               : "=&s"(t) : "s"(lds), "v"(voff), "s"(r), "s"(soff), "i"(LO), "i"(SO) : "memory", "scc");
}
__device__ __forceinline__ void dma4_sc1(rsrc_t r, u32 voff, u32 soff, u32 lds) {
  asm volatile("s_mov_b32 m0, %0\n\ts_nop 0\n\tbuffer_load_dword %1, %2, %3 offen sc1 lds"
               :: "s"(lds), "v"(voff), "s"(r), "s"(soff) : "memory");
}
typedef v2f cpx;
#define CM_A(x, y, ax, bx, by) "v_mul_f32 " x ", " ax ", " bx "\n\tv_mul_f32 " y ", " ax ", " by "\n\t"
#define CM_B(x, y, ay, bx, nb) "v_fmac_f32 " x ", " ay ", " nb "\n\tv_fmac_f32 " y ", " ay ", " bx "\n\t"
__device__ __forceinline__ void cmul4(cpx& r0, cpx& r1, cpx& r2, cpx& r3, cpx a0, cpx a1, cpx a2, cpx a3, cpx b, float nb) {
  float x0, y0, x1, y1, x2, y2, x3, y3;
  asm volatile(CM_A("%0", "%1", "%8", "%16", "%17") CM_A("%2", "%3", "%10", "%16", "%17") CM_A("%4", "%5", "%12", "%16", "%17")
               CM_A("%6", "%7", "%14", "%16", "%17") CM_B("%0", "%1", "%9", "%16", "%18") CM_B("%2", "%3", "%11", "%16", "%18")
               CM_B("%4", "%5", "%13", "%16", "%18") CM_B("%6", "%7", "%15", "%16", "%18")
               : "=&v"(x0), "=&v"(y0), "=&v"(x1), "=&v"(y1), "=&v"(x2), "=&v"(y2), "=&v"(x3), "=&v"(y3)
               : "v"(a0.x), "v"(a0.y), "v"(a1.x), "v"(a1.y), "v"(a2.x), "v"(a2.y), "v"(a3.x), "v"(a3.y), "v"(b.x), "v"(b.y), "v"(nb));
  r0 = (cpx){x0, y0}; r1 = (cpx){x1, y1}; r2 = (cpx){x2, y2}; r3 = (cpx){x3, y3};
}
__device__ __forceinline__ void cmul2(cpx& r0, cpx& r1, cpx a0, cpx b0, float n0, cpx a1, cpx b1, float n1) {
  float x0, y0, x1, y1;
  asm volatile(CM_A("%0", "%1", "%4", "%6", "%7") CM_A("%2", "%3", "%9", "%11", "%12")
               CM_B("%0", "%1", "%5", "%6", "%8") CM_B("%2", "%3", "%10", "%11", "%13")
               : "=&v"(x0), "=&v"(y0), "=&v"(x1), "=&v"(y1)
               : "v"(a0.x), "v"(a0.y), "v"(b0.x), "v"(b0.y), "v"(n0), "v"(a1.x), "v"(a1.y), "v"(b1.x), "v"(b1.y), "v"(n1));
  r0 = (cpx){x0, y0}; r1 = (cpx){x1, y1};
}
__device__ __forceinline__ v2f add2x2s(v2f a, v2f b, v2f c, v2f d) {
  float x, y, z, t;
  asm("v_add_f32 %0, %4, %6\n\tv_add_f32 %1, %5, %7\n\tv_add_f32 %2, %8, %10\n\tv_add_f32 %3, %9, %11\n\t"
      "v_add_f32 %0, %0, %2\n\tv_add_f32 %1, %1, %3"
      : "=&v"(x), "=&v"(y), "=&v"(z), "=&v"(t)
      : "v"(a.x), "v"(a.y), "v"(b.x), "v"(b.y), "v"(c.x), "v"(c.y), "v"(d.x), "v"(d.y));
  return (v2f){x, y};
}
__device__ __forceinline__ u32 mix2(float g, u32 a, v2f b0, v2f b1) {
  float x, y, lo, hi;
  u32 o;
  asm("v_add_f32 %1, %7, %9\n\tv_add_f32 %2, %8, %10\n\tv_lshlrev_b32 %3, 16, %6\n\tv_and_b32 %4, 0xffff0000, %6\n\t"
      "v_sub_f32 %3, %3, %1\n\tv_sub_f32 %4, %4, %2\n\tv_fmac_f32 %1, %5, %3\n\tv_fmac_f32 %2, %5, %4\n\t"
      "s_nop 0\n\tv_cvt_pk_bf16_f32 %0, %1, %2"
      : "=&v"(o), "=&v"(x), "=&v"(y), "=&v"(lo), "=&v"(hi)
      : "v"(g), "v"(a), "v"(b0.x), "v"(b0.y), "v"(b1.x), "v"(b1.y));
  return o;
}
__device__ __forceinline__ v4f add4s(v4f a, v4f b) {
  float x, y, z, t;
  asm("v_add_f32 %0, %4, %8\n\tv_add_f32 %1, %5, %9\n\tv_add_f32 %2, %6, %10\n\tv_add_f32 %3, %7, %11"
      : "=&v"(x), "=&v"(y), "=&v"(z), "=&v"(t)
      : "v"(a.x), "v"(a.y), "v"(a.z), "v"(a.w), "v"(b.x), "v"(b.y), "v"(b.z), "v"(b.w));
  return (v4f){x, y, z, t};
}
constexpr int frow(int r) {
  return r < 7 ? LDS_W + 32768 + r * 288 : r < 13 ? LDS_W + 49152 + 224 + (r - 7) * 288 : LDS_W + 65536 + 1024 + 160 + (r - 13) * 288;
}
constexpr int FM0 = 49152;
constexpr float RND_UP = 1.0027f;
template <int R> __device__ __forceinline__ void rows4(v4u& keep, u32 m0v, cpx a, cpx b, cpx c, cpx d) {
  u32 t0, t1, t2, t3;
  asm volatile("s_mov_b32 m0, %4\n\tv_perm_b32 %0, %6, %5, %21\n\tv_perm_b32 %1, %8, %7, %21\n\tv_perm_b32 %2, %10, %9, %21\n\t"
               "v_perm_b32 %3, %12, %11, %21\n\tds_write_addtid_b32 %0 offset:%17\n\tds_write_addtid_b32 %1 offset:%18\n\t"
               "ds_write_addtid_b32 %2 offset:%19\n\tds_write_addtid_b32 %3 offset:%20"
               : "=&v"(t0), "=&v"(t1), "=&v"(t2), "=&v"(t3)
               : "s"(m0v), "v"(a.x), "v"(a.y), "v"(b.x), "v"(b.y), "v"(c.x), "v"(c.y), "v"(d.x), "v"(d.y),
                 "v"(keep.x), "v"(keep.y), "v"(keep.z), "v"(keep.w),
                 "i"(frow(R) - FM0), "i"(frow(R + 1) - FM0), "i"(frow(R + 2) - FM0), "i"(frow(R + 3) - FM0), "s"(0x07060302u)
               : "memory");
  keep = (v4u){t0, t1, t2, t3};
}
template <int H> __device__ __forceinline__ u32 add_half(u32 base, u32 x) {
  u32 r;
  if constexpr (H == 0) asm volatile("v_add_u32_sdwa %0, %1, %2 dst_sel:DWORD dst_unused:UNUSED_PAD src0_sel:DWORD src1_sel:WORD_0" : "=v"(r) : "s"(base), "v"(x));
  else asm volatile("v_add_u32_sdwa %0, %1, %2 dst_sel:DWORD dst_unused:UNUSED_PAD src0_sel:DWORD src1_sel:WORD_1" : "=v"(r) : "s"(base), "v"(x));
  return r;
}
__device__ __forceinline__ void flag_store(rsrc_t r, u32 soff, u32 tag_v) {
  asm volatile("s_mov_b64 exec, 1\n\tbuffer_store_dword %0, off, %1, %2 sc1\n\ts_mov_b64 exec, -1" :: "v"(tag_v), "s"(r), "s"(soff) : "memory");
}
__device__ __forceinline__ u32 sopaque(u32 x) { asm volatile("" : "+s"(x)); return x; }
__device__ __forceinline__ u32 opaque(u32 x) { asm volatile("" : "+v"(x)); return x; }
__device__ __forceinline__ cpx cpow_l(float a2, float bf, float l) {
  float m = __builtin_amdgcn_exp2f(a2 * l), ph = __builtin_amdgcn_fractf(bf * l);
  return (cpx){m * __builtin_amdgcn_cosf(ph), m * __builtin_amdgcn_sinf(ph)};
}
__device__ __forceinline__ void wait_flags(const gu32* p, u32 tag, u32 first, int code = 0) {
  if (__all(first == tag)) return;
  u32 v;
#pragma unroll 1
  for (int s = 0; s < SPIN_CAP; ++s) {
    v = __hip_atomic_load(p, RLX_AGENT);
    if (__all(v == tag)) break;
    __builtin_amdgcn_s_sleep(1);
  }
}

template <int V> struct ic { static constexpr int value = V; };
struct Args {
  const u16* par[8];
  const u16 *u, *wg, *wu, *wd, *gw;
  u16* out;
  char* ws;
};

__global__ __launch_bounds__(512, 2) void gated_mix_kernel(Args) {
  const int b = blockIdx.x, xcd = b & 7, bi = b >> 3;
  const int tid = threadIdx.x, l = tid & 63;
  const int w = __builtin_amdgcn_readfirstlane(tid >> 6);
  typedef u64 v4q __attribute__((ext_vector_type(4)));
  typedef u64 v2q __attribute__((ext_vector_type(2)));
  v4q ka0;
  v2q ka1;
  u64 ka2, par_w;
  asm volatile("s_load_dwordx8 %0, %4, 0x40\n\ts_load_dwordx4 %1, %4, 0x60\n\ts_load_dwordx2 %2, %4, 0x70\n\ts_load_dwordx2 %3, %4, %5"
               : "=&s"(ka0), "=&s"(ka1), "=&s"(ka2), "=&s"(par_w) : "s"(__builtin_amdgcn_kernarg_segment_ptr()), "s"(w * 8) : "memory");
  asm volatile("s_cmp_ge_u32 %0, %3\n\ts_cbranch_scc1 1f\n\ts_getpc_b64 s[4:5]\n\ts_mov_b32 m0, %2\n\ts_and_b32 s4, s4, 0xffffff00\n\t"
               "global_load_lds_dwordx4 %1, s[4:5]\n1:"
               :: "s"(bi * 8 + w), "v"((bi * 8 + w) * 1024 + l * 16), "s"(LDS_SCR + w * 4096), "i"(CODE_KB) : "s4", "s5", "scc", "memory");
  const int cb = xcd * 28 + bi;
  const int mt = (xcd & 3) * 4 + (bi & 3), ft = (xcd >> 2) * 7 + (bi >> 2);
  const int rot = ((bi >> 2) + 2 * (bi & 3) + (xcd & 3)) % 7;
  const int m2 = 4 * xcd + (bi & 3), nt = bi >> 2;
  const int wt = w & 1, wf = w >> 1;

  const u32 vl16 = opaque(l * 16);
  const u32 vx0 = opaque((l >> 3) * 896 + (((l & 7) ^ (l >> 4)) * 16)), vx1 = opaque((vx0 ^ 64) + 8 * 896);
  const u32 vw = opaque((l >> 4) * (F * 2) + (((l & 15) ^ (4 * (l >> 4))) * 16));
  const u32 sx = sopaque((mt * 128 + 16 * w) * 896 + rot * 128), sw = sopaque(8 * w * (F * 2) + ft * 256 + rot * (64 * F * 2)), lw = sopaque(w * 2048);
  const u32 par_o = sopaque(w < 6 ? cb * 1024 : (cb * 4) & ~15), par_n = sopaque(w < 6 ? D * NS * 2 : D * 2), par_l = sopaque(LDS_PAR + w * 1024);
  asm volatile("s_waitcnt lgkmcnt(0)" : "+s"(ka0), "+s"(ka1), "+s"(ka2), "+s"(par_w) :: "memory");
  struct { const u16 *u, *wg, *wu, *wd, *gw; u16* out; char* ws; } g = {
      (const u16*)ka0.x, (const u16*)ka0.y, (const u16*)ka0.z, (const u16*)ka0.w, (const u16*)ka1.x, (u16*)ka1.y, (char*)ka2};
  char* ws = g.ws;
  const rsrc_t rws = mk_rsrc(ws, WS_END);
  const rsrc_t rx = mk_rsrc(g.u, L * D * 2);
  STAMP(0);

  if (bi >= 28) {
    const int hb = b - NMAIN, t0 = 64 * hb;
    constexpr u32 HX = 0, HG = 64 * 896;
    const rsrc_t rgw = mk_rsrc(g.gw, D * 2);
#pragma unroll
    for (int j = 0; j < 7; ++j) dma16(rx, l * 16, (t0 + 8 * w) * 896 + j * 1024, HX + 8 * w * 896 + j * 1024);
    if (w == 0) { dma4_sc1(rws, 0, WS_EPOCH, HG + 1024); dma16(rgw, l * 16, 0, HG); }
    WAITV(0); BAR();
    STAMP(1);
    const u32 tag = __builtin_amdgcn_readfirstlane(lds_ld<u32>(HG + 1024)) + TAG_STEP;
    {
      const int r = 8 * w + (l >> 3);
      const u32 xa = opaque(HX + r * 896 + (l & 7) * 16), ga = opaque(HG + (l & 7) * 16);
      float s0 = 0.f, s1 = 0.f;
#pragma unroll
      for (int j = 0; j < 7; ++j) {
        const v4u xv = lds_ld<v4u>(xa + 128 * j), gv = lds_ld<v4u>(ga + 128 * j);
#pragma unroll
        for (int e = 0; e < 4; e += 2) {
          s0 = dot2bf(xv[e], gv[e], s0);
          s1 = dot2bf(xv[e + 1], gv[e + 1], s1);
        }
      }
      float s = s0 + s1;
      s += __shfl_xor(s, 1); s += __shfl_xor(s, 2); s += __shfl_xor(s, 4);
      s = rbf(s);
      float gt = rbf(__builtin_amdgcn_rcpf(1.f + __builtin_amdgcn_exp2f(-1.44269504f * s)));
      if ((l & 7) < 2) __hip_atomic_store((gu32*)(ws + WS_GATE) + hb * 128 + (r & 15) * 8 + (r >> 4) + 4 * (l & 1), __float_as_uint(gt), RLX_AGENT);
    }
    WAITV(0); BAR();
    if (tid == 0) __hip_atomic_store((gu32*)(ws + WS_FLAG_G) + hb, tag, RLX_AGENT);
    STAMP(2);
    if (hb == NHELP - 1) {
      if (w < 4) wait_flags((const gu32*)(ws + (tid < NMAIN ? WS_FLAG_W : WS_FLAG_G - NMAIN * 4)) + tid, tag, ~tag, 1);
      BAR();
      if (tid == 0) __hip_atomic_store((gu32*)(ws + WS_EPOCH), tag, RLX_AGENT);
    }
    return;
  }

  {
    const rsrc_t rp = mk_rsrc((const void*)par_w, par_n);
    dma16(rp, vl16, par_o, par_l);
    if (w == 0) dma4_sc1(rws, 0, WS_EPOCH, LDS_PAR + 8192);
  }
  const rsrc_t rwg = mk_rsrc(g.wg, D * F * 2), rwu = mk_rsrc(g.wu, D * F * 2);
  auto piece = [&](auto S, auto I) {
    constexpr int s = decltype(S)::value, i = decltype(I)::value;
    const bool wrap = s + rot >= 7;
    if constexpr (i < 2) dma16i<LDS_X + (s & 1) * 16384 + i * 1024, s * 128>(rx, i ? vx1 : vx0, wrap ? sx - 7 * 128 : sx, lw);
    else dma16i<LDS_W + (s % 3) * 32768 + (i >= 4) * 16384 + (i & 1) * 1024, (64 * s + 4 * (i & 1)) * F * 2>(i < 4 ? rwg : rwu, vw, wrap ? sw - 7 * 64 * F * 2 : sw, lw);
  };
#define PIECE(s, i) piece(ic<(s)>{}, ic<(i)>{})
  PIECE(0, 0); PIECE(0, 1); PIECE(0, 2); PIECE(0, 3);
  const int ct = 7 * (cb & 3) + (w < 7 ? w : 6);
  const int G = l >> 4, q = (l & 15) >> 2, p = l & 3;
  const int usl = 2 * rot + (ft >= 7);
  const u32 srd = opaque(lw + ((l & 15) < 7 ? frow(0) : (l & 15) < 13 ? frow(7) - 7 * 288 : frow(13) - 13 * 288) + (l & 15) * 288 + (l >> 4) * 16);
  const u32 po = opaque(LDS_PAR + (w >> 2) * 512 + (w & 3) * 128 + l * 2);
  const u32 pld = opaque(LDS_PAR + 6144 + ((cb * 4) & 15) + 2 * (w >> 2)), pdk = opaque(LDS_PAR + 7168 + ((cb * 4) & 15));
  const u32 wst_v = opaque((l >> 1) * 896 + (l & 1) * 16), tab_v = opaque(tid * 32);
  const u32 wst_a = opaque(LDS_WST + lw + (16 * (G >> 1) + 4 * (G & 1) + q) * 32 + p * 8);
  const u32 wt_o = opaque(WS_WT + (((ct >> 2) * 56 + (cb >> 2)) * 4 + (ct & 3)) * 1024 + vl16);
  const u32 kpw = opaque(LDS_SCR + w * 4096 + vl16), kpr = opaque(LDS_SCR + (w & 4) * 4096 + (w & 3) * 1024 + vl16);
  const u32 s_ut = opaque(WS_UT + (((2 * mt + (wf >> 1)) * 28 + 2 * usl + wt) * 2 + (wf & 1)) * 1024 + (l & 15) * 64 + G * 16);
  const u32 wfo = opaque(WS_FLAG_W + 16 * (l < 56 ? l : 0));
  const u32 ufo = opaque(l < 16 ? WS_FLAG_U + ((cb >> 4) * 16 + l) * 4 : WS_FLAG_G + m2 * 4);
  WAITV(4); BAR();
  PRIO(3);
  STAMP(1);

  auto frags = [&](v8bf (&f)[4]) {
#pragma unroll
    for (int k = 0; k < 4; ++k) f[k] = as_frag(lds_ld<v4u>(srd + k * 64));
  };
  u32 tag, dsk2;
  v4u tb0, tb1;
  {
    cpx c0, w1, w2, w256, w512;
    const u32 fm0 = lw + FM0;
    v8bf J[2][4], Hf[4], Hg[4];
    v4u keep = {0, 0, 0, 0};
    const float ld = bf_lo(lds_ld<u16>(pld));
    const u32 q0 = lds_ld<u16>(po), q1 = lds_ld<u16>(po + 1024), q2 = lds_ld<u16>(po + 2048), q3 = lds_ld<u16>(po + 3072);
    const u32 q4 = lds_ld<u16>(po + 4096), q5 = lds_ld<u16>(po + 5120);
    const u32 ep = lds_ld<u32>(pdk + 1024), dk = lds_ld<u32>(pdk);
    PIECE(0, 4); PIECE(0, 5);
    const float dt = rbf(__builtin_amdgcn_exp2f(ld * 1.44269504f));
    const float a2 = (dt * bf_lo(q0)) * 1.44269504f, bf = (dt * bf_lo(q1)) * 0.15915494f;
    const cpx w8 = cpow_l(a2, bf, 8.f);
    w1 = cpow_l(a2, -bf, 1.f); w256 = cpow_l(a2, -bf, 256.f);
    const cpx cc = {bf_lo(q4), -bf_lo(q5)}, bb = {(dt * RND_UP) * bf_lo(q2), -(dt * RND_UP) * bf_lo(q3)};
    tag = __builtin_amdgcn_readfirstlane(ep) + TAG_STEP; dsk2 = __builtin_amdgcn_readfirstlane(dk);
    cpx w16, w24, w32, j1, j2, j3;
    cmul2(w16, c0, w8, w8, -w8.y, cc, bb, -bb.y);
    cmul2(w24, w32, w16, w8, -w8.y, w16, w16, -w16.y);
    cpx j0 = {RND_UP, 0.f};
    j1 = w8 * RND_UP; j2 = w16 * RND_UP; j3 = w24 * RND_UP;
    const float n32 = -w32.y, n1 = -w1.y, n256 = -w256.y;
#define JSTEP(k) rows4<((k) & 3) * 4>(keep, fm0, j0, j1, j2, j3)
#define JNEXT() cmul4(j0, j1, j2, j3, j0, j1, j2, j3, w32, n32)
    JSTEP(0); JNEXT();
    JSTEP(1); JNEXT();
    PRIO(2);
    dma16(mk_rsrc(g.wd, F * D * 2), wst_v, 32 * (cb >> 2) * 896 + ct * 32, LDS_WST + lw);
    JSTEP(2); JNEXT();
    JSTEP(3); frags(J[0]); JNEXT();
    JSTEP(4); JNEXT();
    JSTEP(5); JNEXT();
    JSTEP(6); JNEXT();
    cmul2(w2, w512, w1, w1, n1, w256, w256, n256);
    PRIO(1);
    asm volatile("global_load_dwordx4 %0, %2, %3\n\tglobal_load_dwordx4 %1, %2, %3 offset:16"
                 : "=&v"(tb0), "=&v"(tb1) : "v"(tab_v), "s"(&thread_tab.v[0][0]) : "memory");
    JSTEP(7); frags(J[1]);
    cpx c1, c2, c3, c4, c5, c6, c7, w4, d0, d2, d4, d6, e0, e4, f0, f4, g0, g1, g2, g3, g4;
    const float n2 = -w2.y, n512 = -w512.y;
    cmul2(c1, c2, c0, w1, n1, c0, w2, n2);
    cmul2(c3, w4, c2, w1, n1, w2, w2, n2);
    cmul4(c4, c5, c6, c7, c0, c1, c2, c3, w4, -w4.y);
    rows4<0>(keep, fm0, c0, c1, c2, c3);
    cmul4(d0, d2, d4, d6, c0, c2, c4, c6, w256, n256);
    rows4<4>(keep, fm0, c4, c5, c6, c7);
    cmul4(e0, e4, f0, f4, c0, c4, d0, d4, w512, n512);
    rows4<8>(keep, fm0, d0, d2, d4, d6);
    cmul2(g0, g1, e0, w512, n512, f0, w512, n512);
    PRIO(0);
    rows4<12>(keep, fm0, e0, e4, f0, f4);
    frags(Hf);
    cmul2(g2, g3, g0, w512, n512, g1, w512, n512);
    g4 = (cpx){g2.x * w512.x + g2.y * n512, g2.x * w512.y + g2.y * w512.x};
    rows4<0>(keep, fm0, g0, g1, g2, g3);
    asm volatile("ds_write_addtid_b32 %0 offset:%1" :: "v"(__builtin_amdgcn_perm(__float_as_uint(g4.y), __float_as_uint(g4.x), 0x07060302u)),
                 "i"(frow(4) - FM0) : "memory");
    frags(Hg);
    SB0();
    v4f kc[4];
    auto kmm = [&](int k) {
#pragma unroll
      for (int t = 0; t < 4; ++t)
        kc[t] = __builtin_amdgcn_mfma_f32_16x16x32_bf16((t >> 1) ? Hg[k] : Hf[k], J[t & 1][k], k == 0 ? (v4f){0.f, 0.f, 0.f, 0.f} : kc[t], 0, 0, 0);
      SB0();
    };
    WAITL0();
    kmm(0); PIECE(1, 0); kmm(1); PIECE(1, 1); kmm(2); PIECE(1, 2); kmm(3); PIECE(1, 3);
#pragma unroll
    for (int t = 0; t < 4; ++t) lds_st<v4f>(kpw + t * 1024, kc[t]);
    SB0();
    PIECE(1, 4); PIECE(1, 5);
  }
  STAMP(13);
  asm volatile("s_waitcnt vmcnt(6)" : "+v"(tb0), "+v"(tb1) :: "memory");
  const u32 a_ut = (tb1.z >> 16) ^ ((usl & 1) << 6);
  u32 xa[4], wa[3];
  xa[0] = opaque(tb1.w & 0xffff);
#pragma unroll
  for (int s = 1; s < 4; ++s) xa[s] = opaque(xa[0] ^ (32 * s));
  wa[0] = opaque(tb1.w >> 16);
  wa[1] = opaque(wa[0] + 32768); wa[2] = opaque(wa[0] + 65536);
  const u32 tag_v = opaque(tag);
  STAMP(2);

  v16f accg[2] = {}, accu[2] = {};
  v4u ut4, gate4;
  v4u wfr[28];
  const int kr = w, krot = nt & 4;
  u32 koff[7];
#pragma unroll
  for (int i = 0; i < 7; ++i) koff[i] = (kr * 7 + (i + krot >= 7 ? i + krot - 7 : i + krot)) * 1024;
  const u32 wd_so = WS_WT + nt * 56 * 4096;
  auto ldw = [&](int i) { wfr[i] = __builtin_amdgcn_raw_buffer_load_b128(rws, vl16 + (i & 3) * 1024, wd_so + 4 * koff[i >> 2], 16); };
  u32 hadr, hadr2, ka, ka8, ub, yb, yo, uo, xe[2][5], xkb, xz, uia;
  auto img_writes = [&]() {
#pragma unroll
    for (int o = 0; o < 2; ++o) {
      lds_st<v4u>(xkb + (3 - o) * KCOPY, (v4u){xe[o][0], xe[o][1], xe[o][2], xe[o][3]});
      lds_st<v4u>(xkb + (1 - o) * KCOPY, (v4u){xe[o][1], xe[o][2], xe[o][3], xe[o][4]});
    }
    lds_st<v4u>(xz, (v4u){0, 0, 0, 0});
    lds_st<v4u>(uia, ut4);
  };
  v2f nl2e = {-1.44269504f, -1.44269504f};
  v4u hst[4];
  auto silu8 = [&](int gI) {
    const int j = gI >> 1, r = 8 * (gI & 1);
    v2f gv[4], pv[4], ev[4];
#pragma unroll
    for (int e = 0; e < 4; ++e) { gv[e] = (v2f){accg[j][r + 2 * e], accg[j][r + 2 * e + 1]}; ev[e] = gv[e] * nl2e; }
    SB0();
#pragma unroll
    for (int e = 0; e < 4; ++e) ev[e] = (v2f){__builtin_amdgcn_exp2f(ev[e].x), __builtin_amdgcn_exp2f(ev[e].y)};
    SB0();
#pragma unroll
    for (int e = 0; e < 4; ++e) { ev[e] = ev[e] + 1.f; pv[e] = gv[e] * (v2f){accu[j][r + 2 * e], accu[j][r + 2 * e + 1]}; }
    SB0();
#pragma unroll
    for (int e = 0; e < 4; ++e) ev[e] = (v2f){__builtin_amdgcn_rcpf(ev[e].x), __builtin_amdgcn_rcpf(ev[e].y)};
    SB0();
#pragma unroll
    for (int e = 0; e < 4; ++e) pv[e] = pv[e] * ev[e];
    SB0();
#pragma unroll
    for (int e = 0; e < 4; ++e) hst[gI][e] = pk_bf(pv[e].x, pv[e].y);
    st16<16>(hst[gI], rws, (j ? hadr2 : hadr) + (r >> 3) * 512);
    if (gI > 0) asm volatile("" :: "v"(hst[gI - 1]));
    SB0();
  };
  {
    v8bf fg[2], fu[2], fx0[2], fx1[2];
    auto rd = [&](int buf, int st, int ks) {
      const u32 wb = wa[st % 3] + ks * 4096, xb = xa[ks] + (st & 1) * 16384;
      fg[buf] = as_frag(lds_tr(wb), lds_tr(wb + 1024));
      fx0[buf] = as_frag(lds_ld<v4u>(xb));
      fu[buf] = as_frag(lds_tr(wb + 16384), lds_tr(wb + 16384 + 1024));
      fx1[buf] = as_frag(lds_ld<v4u>(xb + 4096));
    };
    v4f ks4[4];
    u32 kp01, kp23;
    auto kgen = [&](int n) {
      if (n == 22) {
#pragma unroll
        for (int k = 0; k < 4; ++k) ks4[k] = lds_ld<v4f>(kpr + k * 4096);
      }
      if (n == 24) {
        const v4f s = add4s(add4s(ks4[0], ks4[1]), add4s(ks4[2], ks4[3]));
        const bool keep = !(w & 2) || l < 32;
        kp01 = opaque(keep ? pk_bf(s[0], s[1]) : 0); kp23 = opaque(keep ? pk_bf(s[2], s[3]) : 0);
      }
      if (n == 25) lds_st<v2u>(LDS_TAB + w * 512 + (vl16 >> 1), (v2u){kp01, kp23});
    };
    u32 xv[11], uta;
    auto expand = [&](int step) {
      if (step == 0) {
        xv[0] = lds_ld<u16>(add_half<0>(LDS_TAB, tb0.x)); xv[1] = lds_ld<u16>(add_half<1>(LDS_TAB, tb0.x));
        xv[2] = lds_ld<u16>(add_half<0>(LDS_TAB, tb0.y)); xv[3] = lds_ld<u16>(add_half<1>(LDS_TAB, tb0.y));
      } else if (step == 1) {
        xv[4] = lds_ld<u16>(add_half<0>(LDS_TAB, tb0.z)); xv[5] = lds_ld<u16>(add_half<1>(LDS_TAB, tb0.z));
        xv[6] = lds_ld<u16>(add_half<0>(LDS_TAB, tb0.w)); xv[7] = lds_ld<u16>(add_half<1>(LDS_TAB, tb0.w));
      } else if (step == 2) {
        xv[8] = lds_ld<u16>(add_half<0>(LDS_TAB, tb1.x)); xv[9] = lds_ld<u16>(add_half<1>(LDS_TAB, tb1.x));
        xv[10] = lds_ld<u16>(add_half<0>(LDS_TAB, tb1.y));
      } else if (step < 16) {
        const int o = step - 14;
#pragma unroll
        for (int j = 0; j < 5; ++j) xe[o][j] = opaque(xv[2 * j + o] | (xv[2 * j + o + 1] << 16));
      }
    };
    v2u utl, uth;
    v4u wflag;
    u32 uflag;
    auto side = [&](auto S, auto KS, auto H) {
      constexpr int s = decltype(S)::value, ks = decltype(KS)::value, h = decltype(H)::value;
      constexpr int st = 4 * s + ks;
      if constexpr (h == 0 || h == 2) {
        constexpr int i = h >> 1;
        if constexpr (ks == 3) { if constexpr (s < 5) PIECE(s + 2, i); }
        else if constexpr (s == 0) { if constexpr (ks < 2) PIECE(2, 2 + 2 * ks + i); }
        else if constexpr (ks < 2 && s + 2 < 7) PIECE(s + 2, 2 + 2 * ks + i);
        if constexpr (st >= 20) ldw(2 * (st - 20) + i);
        if constexpr (st == 3 && h == 2) st16<16>((v4u){utl.x, utl.y, uth.x, uth.y}, rws, s_ut);
      } else {
        constexpr int v = 2 * st + (h >> 1);
        if constexpr (v == 1) kgen(22);
        if constexpr (v == 4) kgen(24);
        if constexpr (v == 11) kgen(25);
        if constexpr (v >= 16 && v < 19) expand(v - 16);
        if constexpr (v == 21 || v == 22) expand(v - 7);
        if constexpr (v == 24) {
          xkb = opaque(((w >> 2) ? LDS_KIMG1 : LDS_KIMG0) + (tid & 255) * 16);
          xz = opaque((tb1.y >> 16) << 4);
          uia = opaque((tb1.z & 0xffff) << 4);
        }
        if constexpr (v == 26) uta = opaque((tid >> 4) * (56 * 1024) + ((tid >> 3) & 1) * 1024 + (tid & 7) * 16);
        if constexpr (v == 30) {
          hadr = opaque(WS_H + ((mt * 8 + 4 * wt + ((l & 31) >> 4)) * 56 + ft * 4 + wf) * 1024 + (l & 15) * 16 + (l >> 5) * 256);
          hadr2 = opaque(hadr + 2 * 56 * 1024);
          asm volatile("" : "+v"(nl2e));
        }
        if constexpr (v == 32) {
          ka = opaque(((w >> 2) ? LDS_KIMG1 : LDS_KIMG0) + (l & 3) * KCOPY + 2 * (2028 + 8 * G - 4 * q) - 512 * 7 - 128 * (w & 3) - 64);
          ka8 = opaque(ka + 8);
        }
        if constexpr (v == 33) ub = opaque(LDS_UIMG + (w >> 2) * UCH + 2 * (240 + 16 * (l & 15) + 8 * G) - 128 * (w & 3) - 64);
        if constexpr (v == 34) yb = opaque(LDS_Y + ((w & 3) * 2 + (w >> 2)) * 8192 + (16 * (l & 15) + 4 * (G ^ ((l >> 1) & 3))) * 4);
        if constexpr (v == 35) { yo = opaque(LDS_Y + (tid >> 2) * 64 + (((tid & 3) ^ ((tid >> 3) & 3)) * 16)); uo = opaque(LDS_UIMG + 512 + tid * 8); }
      }
    };
    auto mm = [&](int buf, auto S, auto KS) {
      accg[0] = __builtin_amdgcn_mfma_f32_32x32x16_bf16(fg[buf], fx0[buf], accg[0], 0, 0, 0);
      SB0(); side(S, KS, ic<0>{}); SB0();
      accu[0] = __builtin_amdgcn_mfma_f32_32x32x16_bf16(fu[buf], fx0[buf], accu[0], 0, 0, 0);
      SB0(); side(S, KS, ic<1>{}); SB0();
      accg[1] = __builtin_amdgcn_mfma_f32_32x32x16_bf16(fg[buf], fx1[buf], accg[1], 0, 0, 0);
      SB0(); side(S, KS, ic<2>{}); SB0();
      accu[1] = __builtin_amdgcn_mfma_f32_32x32x16_bf16(fu[buf], fx1[buf], accu[1], 0, 0, 0);
      SB0(); side(S, KS, ic<3>{});
      asm volatile("" :: "v"(fg[buf]), "v"(fu[buf]), "v"(fx0[buf]), "v"(fx1[buf]));
      SB0();
    };
    auto stage = [&](auto S) {
      constexpr int s = decltype(S)::value;
      PRIO(2); rd(1, s, 1); SB0(); mm(0, S, ic<0>{});
      PRIO(1); rd(0, s, 2); SB0(); mm(1, S, ic<1>{});
      PRIO(0); rd(1, s, 3); SB0(); mm(0, S, ic<2>{});
      PRIO(3);
      if constexpr (s < 6) {
        WAITL0();
        if constexpr (s < 2) WAITV(5);
        else if constexpr (s < 4) WAITV(4);
        else if constexpr (s == 4) {
          asm volatile("s_waitcnt vmcnt(4)" : "+v"(wflag) :: "memory");
          if (w == 0 && !__all(wflag.x == tag && wflag.y == tag && wflag.z == tag && wflag.w == tag)) {
#pragma unroll 1
            for (int sp = 0; sp < SPIN_CAP; ++sp) {
              asm volatile("" ::: "memory");
              const v4u f = __builtin_amdgcn_raw_buffer_load_b128(rws, wfo, 0, 16);
              if (__all(f.x == tag && f.y == tag && f.z == tag && f.w == tag)) break;
            }
          }
        } else {
          asm volatile("s_waitcnt vmcnt(%1)" : "+v"(uflag) : "i"(6) : "memory");
          if (w == 0) wait_flags((const gu32*)(ws + ufo), tag, uflag, 2);
        }
        BAR();
        rd(0, s + 1, 0);
        if constexpr (s == 1) { if (w == 0) flag_store(rws, WS_FLAG_W + cb * 4, tag_v); }
        if constexpr (s == 2) { if (w == 0) flag_store(rws, WS_FLAG_U + (usl * 16 + mt) * 4, tag_v); }
        if constexpr (s == 3)
          asm volatile("s_cmp_lg_u32 %3, 0\n\ts_cbranch_scc1 1f\n\tbuffer_load_dwordx4 %0, %1, %2, 0 offen sc1\n1:"
                       : "=v"(wflag) : "v"(wfo), "s"(rws), "s"(w) : "memory", "scc");
        if constexpr (s == 4)
          asm volatile("s_cmp_lg_u32 %3, 0\n\ts_cbranch_scc1 1f\n\tbuffer_load_dword %0, %1, %2, 0 offen sc1\n1:"
                       : "=v"(uflag) : "v"(ufo), "s"(rws), "s"(w) : "memory", "scc");
        if constexpr (s >= 4) __builtin_amdgcn_fence(__ATOMIC_ACQUIRE, "wavefront");
        if constexpr (s == 5) {
          ut4 = __builtin_amdgcn_raw_buffer_load_b128(rws, uta, WS_UT + (cb >> 3) * 2048 + (cb & 7) * 128, 16);
          gate4 = __builtin_amdgcn_raw_buffer_load_b128(rws, (tid >> 5) * 32, WS_GATE + m2 * 512 + (nt & 3) * 4, 16);
        }
        STAMP(s < 5 ? 22 + s : 20);
        if constexpr (s == 5) WSC(0);
        SB0();
      }
      mm(1, S, ic<3>{});
    };
    {
      const v2u lo = lds_tr(wst_a), hi = lds_tr(wst_a + 256);
      st16<16>((v4u){lo.x, lo.y, hi.x, hi.y}, rws, wt_o);
    }
    WAITL0(); BAR();
    STAMP(3);
    rd(0, 0, 0);
    utl = lds_tr(a_ut); uth = lds_tr((a_ut ^ 32) + 512);
    SB0();
    stage(ic<0>{}); stage(ic<1>{}); stage(ic<2>{}); stage(ic<3>{}); stage(ic<4>{}); stage(ic<5>{}); stage(ic<6>{});
  }
  STAMP(4);
  WSC(1);
  const gu32* hfp = (const gu32*)(ws + WS_FLAG_H) + (m2 * 56 + 7 * kr + (l < 7 ? l : 0)) * HFS;
  u32 hflag;
  v4u hf[28];
  u32 hso[4];
#pragma unroll
  for (int tt = 0; tt < 4; ++tt) hso[tt] = WS_H + (m2 * 4 + ((tt + nt) & 3)) * 56 * 1024;
  auto ldh = [&](int n) {
    const int i = n % 7;
    hf[n] = __builtin_amdgcn_raw_buffer_load_b128(rws, vl16, hso[n / 7] + koff[i], 16);
    SB0();
  };
  constexpr int AHEAD = AHD;
  {
    v4f acc[8];
    v8bf A[8][2], B[8][2];
    auto lda_ = [&](int pp) {
#pragma unroll
      for (int qq = 0; qq < 2; ++qq)
        A[pp][qq] = as_frag(lds_ld<v2u>(ka + 64 - 64 * qq + 512 * (7 - pp)), lds_ld<v2u>(ka8 + 64 - 64 * qq + 512 * (7 - pp)));
    };
#pragma unroll
    for (int gI = 0; gI < 4; ++gI) {
      silu8(gI);
      if (gI == 3) img_writes();
      SB0();
    }
    WAITL0(); BAR();
    lda_(0);
#pragma unroll
    for (int e = 7; e >= 0; --e) B[e][0] = as_frag(lds_ld<v4u>(ub + 64 + 512 * e));
#pragma unroll
    for (int e = 7; e >= 0; --e) B[e][1] = as_frag(lds_ld<v4u>(ub + 512 * e));
    lda_(1);
    asm volatile("" :: "v"(hst[3]));
    WAITV(0);
    flag_store(rws, WS_FLAG_H + ((2 * mt + wt) * 56 + 4 * ft + wf) * (HFS * 4), tag_v);
    WST(1);
    SB0();
    PRIO(2);
    asm volatile("" ::: "memory");
    STAMP(5);
    WST(0);
    WSC(2);
#pragma unroll
    for (int pp = 0; pp < 8; ++pp) {
#pragma unroll
      for (int qq = 0; qq < 2; ++qq) {
#pragma unroll
        for (int c = 7; c >= pp; --c) {
          acc[c] = __builtin_amdgcn_mfma_f32_16x16x32_bf16(A[pp][qq], B[c - pp][qq], (qq == 0 && pp == 0) ? (v4f){0.f, 0.f, 0.f, 0.f} : acc[c], 0, 0, 0);
          if (c == 5 && pp < 3) { SB0(); ldw(16 + 4 * pp + 2 * qq); SB0(); }
        }
        SB0();
        if (qq == 0) { if (pp < 6) lda_(pp + 2); if (pp > 0) lds_st<v4f>(yb + (pp - 1) * 1024, acc[pp - 1]); }
        if (pp < 3) ldw(17 + 4 * pp + 2 * qq);
        const int hs = 2 * pp + qq;
        if (hs == HL_P - HL_D) hflag = __hip_atomic_load(hfp, RLX_AGENT);
        if (hs == HL_P) {
          WSC(5);
          STAMP(7);
          WST(2);
          wait_flags(hfp, tag, hflag, 3);
          __builtin_amdgcn_fence(__ATOMIC_ACQUIRE, "wavefront");
          STAMP(8);
          WST(3);
        }
        if (hs >= HL_P && 2 * (hs - HL_P) < 8) { ldh(2 * (hs - HL_P)); ldh(2 * (hs - HL_P) + 1); }
        if (qq == 1 && pp == 1) PRIO(1);
        if (qq == 1 && pp == 3) PRIO(0);
        SB0();
      }
    }
    lds_st<v4f>(yb + 7 * 1024, acc[7]);
  }
  WSC(6);

  {
    v4u a_out;
    v4f yv[8], ys[2];
    v2u uv[2];
    auto a_step = [&](int k) {
      if (k < 2) {
        uv[k] = lds_ld<v2u>(uo + k * UCH);
#pragma unroll
        for (int z = 0; z < 4; ++z) yv[4 * k + z] = lds_ld<v4f>(yo + k * 8192 + z * 16384);
      } else if (k < 4) {
        ys[k - 2] = add4s(add4s(yv[4 * k - 8], yv[4 * k - 7]), add4s(yv[4 * k - 6], yv[4 * k - 5]));
      } else {
        const int e = k - 4;
        const float k0 = bf_lo(dsk2), k1 = bf_hi(dsk2);
        a_out[2 * e] = opaque(pk_bf(__builtin_fmaf(bf_lo(uv[0][e]), k0, ys[0][2 * e]), __builtin_fmaf(bf_lo(uv[1][e]), k1, ys[1][2 * e])));
        a_out[2 * e + 1] = opaque(pk_bf(__builtin_fmaf(bf_hi(uv[0][e]), k0, ys[0][2 * e + 1]), __builtin_fmaf(bf_hi(uv[1][e]), k1, ys[1][2 * e + 1])));
      }
    };
    auto sta = [&]() {
      const u32 ao = vl16 + WS_A2 + cb * 16384 + w * 2048;
      st16<16>((v4u){a_out.x, tag, a_out.y, tag}, rws, ao);
      st16<16>((v4u){a_out.z, tag, a_out.w, tag}, rws, ao + 1024);
      SB0();
    };
#pragma unroll
    for (int n = 8; n < AHEAD - 1; ++n) ldh(n);
    STAMP(27);
    WST(4);
    WAITL0(); BAR();
    a_step(0); a_step(1);
    SB0();
    STAMP(12);
    WSC(7);
    v4u a4[2];
    u32 a_vo, pw, prd, asw, asr, oad;
    const u32 a_so = WS_A2 + 32 * nt * 16384 + (m2 >> 2) * 2048 + (m2 & 3) * 256;
    auto lda = [&]() {
      a4[0] = __builtin_amdgcn_raw_buffer_load_b128(rws, a_vo, a_so, 16);
      a4[1] = __builtin_amdgcn_raw_buffer_load_b128(rws, a_vo + 1024, a_so, 16);
    };
    auto check_a = [&]() {
          if (!__all(((a4[0].y ^ tag) | (a4[0].w ^ tag) | (a4[1].y ^ tag) | (a4[1].w ^ tag)) == 0)) {
#pragma unroll 1
            for (int sp = 0; sp < SPIN_CAP; ++sp) {
              asm volatile("" ::: "memory");
              lda();
              if (__all(a4[0].y == tag && a4[0].w == tag && a4[1].y == tag && a4[1].w == tag)) break;
            }
          }
          lds_st<v2u>(asw, (v2u){a4[0].x, a4[0].z}); lds_st<v2u>(asw + 8, (v2u){a4[1].x, a4[1].z});
        };
    v4f acc[2][4];
    v2f pr[8], bs[4][2];
    u32 av[4], oso[4], prd2;
    const rsrc_t ro = mk_rsrc(g.out, L * D * 2);
    auto pread = [&](int tt) {
#pragma unroll
      for (int k = 0; k < 8; ++k) pr[k] = lds_ld<v2f>((tt == 2 ? prd2 : prd + (tt == 1) * PBUF) + k * PWAVE);
    };
    auto psum = [&](int tt, int h) { bs[tt][h] = add2x2s(pr[4 * h], pr[4 * h + 1], pr[4 * h + 2], pr[4 * h + 3]); };
    auto fetch_a = [&]() {
#pragma unroll
      for (int e = 0; e < 4; ++e) av[e] = lds_ld<u32>(asr + e * 64);
    };
    auto mix = [&](int tt) {
      __builtin_amdgcn_raw_buffer_store_b32(mix2(__uint_as_float(gate4[tt]), av[tt], bs[tt][0], bs[tt][1]), ro, oad, oso[tt], 16);
    };
#pragma unroll
    for (int tt = 0; tt < 3; ++tt) {
#pragma unroll
      for (int i = 0; i < 7; ++i) {
        const int n = 7 * tt + i;
        if (i == 0) PRIO(3);
        if (i == 2) PRIO(2);
        if (i == 4) PRIO(1);
        if (i == 6) PRIO(0);
        if (n + AHEAD - 1 < 28) ldh(n + AHEAD - 1);
          if (n == 4) sta();
        if (n == LDA_N) lda();
        SB0();
#pragma unroll
        for (int c = 0; c < 4; ++c)
          acc[tt & 1][c] = __builtin_amdgcn_mfma_f32_16x16x32_bf16(as_frag(wfr[4 * i + c]), as_frag(hf[n]),
                                                                   i == 0 ? (v4f){0.f, 0.f, 0.f, 0.f} : acc[tt & 1][c], 0, 0, 0);
        SB0();
        if (n == 0) { STAMP(29); WST(5); }
        if (n == 5) pw = opaque(LDS_P + w * PWAVE + (l & 15) * PROW + G * 16);
        if (n == 6) { prd = opaque(LDS_P + (tid >> 5) * PROW + (tid & 31) * 8); prd2 = opaque(prd - LDS_P); }
        if (n == 8) a_vo = opaque((tid >> 4) * 16384 + (tid & 15) * 16);
        if (n == 9) asw = opaque(LDS_AST + (tid >> 4) * 272 + ((tid - 4 * nt) & 15) * 16);
        if (n == 10) asr = opaque(LDS_AST + (tid & 31) * 272 + (tid >> 5) * 4);
        if (n == 11) oad = opaque((tid >> 5) * (D * 2) + (tid & 31) * 4);
        if (n == 12) {
#pragma unroll
          for (int k = 0; k < 4; ++k) {
            oso[k] = (64 * m2 + 16 * ((k + nt) & 3)) * (D * 2) + 128 * nt;
            asm volatile("" : "+s"(oso[k]));
          }
        }
        if (tt > 0 && i < 4) lds_st<v4f>(pw + (tt - 1) * PBUF + i * 64, acc[(tt - 1) & 1][i]);
        if (tt > 1 && i == 0) pread(tt - 2);
        if (tt > 1 && (i == 2 || i == 3)) psum(tt - 2, i - 2);
          if (n < 4) a_step(n + 2);
        if (n == CHK_N) check_a();
        if (i == 6 && tt > 0) { WAITL0(); BAR(); }
        if (n > 0) asm volatile("" :: "v"(hf[n - 1]));
        SB0();
      }
      if (tt == 0) STAMP(9);
      if (tt == 1) STAMP(16);
      if (tt == 2) STAMP(17);
    }
    const u32 pw2 = pw - LDS_P;
#pragma unroll
    for (int c = 0; c < 4; ++c) {
      if (c == 0) PRIO(3);
      if (c == 1) PRIO(2);
      if (c == 2) PRIO(1);
      if (c == 3) PRIO(0);
#pragma unroll
      for (int i = 0; i < 7; ++i) {
        acc[1][c] = __builtin_amdgcn_mfma_f32_16x16x32_bf16(as_frag(wfr[4 * i + c]), as_frag(hf[21 + i]),
                                                            i == 0 ? (v4f){0.f, 0.f, 0.f, 0.f} : acc[1][c], 0, 0, 0);
        SB0();
        if (c == 0 && i == 0) pread(1);
        if (i == 2) lds_st<v4f>(pw2 + c * 64, acc[0][c]);
        if (i == 4 && c > 0) lds_st<v4f>(pw + (c - 1) * 64, acc[1][c - 1]);
        if (c == 1 && i == 0) psum(1, 0);
        if (c == 1 && i == 5) psum(1, 1);
        if (CHK_N < 21) {
          if (c == 0 && i == 3) fetch_a();
          if (c == 1 && i == 2) mix(0);
          if (c == 2 && i == 2) mix(1);
        } else if (c == 3 && i == 0) check_a();
        SB0();
      }
    }
    STAMP(18);
    lds_st<v4f>(pw + 3 * 64, acc[1][3]);
    WAITL0(); BAR();
    STAMP(10);
    if (CHK_N >= 21) fetch_a();
    pread(2);
    v2f pq[8];
#pragma unroll
    for (int k = 0; k < 8; ++k) pq[k] = lds_ld<v2f>(prd + k * PWAVE);
    if (CHK_N >= 21) { mix(0); mix(1); }
    psum(2, 0); psum(2, 1);
    mix(2);
    bs[3][0] = add2x2s(pq[0], pq[1], pq[2], pq[3]); bs[3][1] = add2x2s(pq[4], pq[5], pq[6], pq[7]);
    mix(3);
  }
}

extern "C" void kernel_launch(void* const* d_in, const int* in_sizes, int n_in, void* d_out, int out_size, void* d_ws,
                              size_t ws_size, hipStream_t stream) {
  Args a;
  for (int i = 0; i < 8; ++i) a.par[i] = (const u16*)d_in[1 + i];
  a.u = (const u16*)d_in[0];
  a.wg = (const u16*)d_in[10]; a.wu = (const u16*)d_in[11]; a.wd = (const u16*)d_in[12];
  a.gw = (const u16*)d_in[13];
  a.out = (u16*)d_out;
  a.ws = (char*)d_ws;
  (void)hipFuncSetAttribute((const void*)gated_mix_kernel, hipFuncAttributeMaxDynamicSharedMemorySize, LDS_TOTAL);
  gated_mix_kernel<<<NMAIN + NHELP, 512, LDS_TOTAL, stream>>>(a);
}
```

```cpp
#include <hip/hip_runtime.h>

typedef unsigned short u16;
typedef unsigned u32;
typedef unsigned long long u64;
typedef u32 v4u __attribute__((ext_vector_type(4)));
typedef u32 v2u __attribute__((ext_vector_type(2)));
typedef float v2f __attribute__((ext_vector_type(2)));
typedef float v4f __attribute__((ext_vector_type(4)));
typedef float v16f __attribute__((ext_vector_type(16)));
typedef __bf16 v8bf __attribute__((ext_vector_type(8)));
typedef __bf16 v2bf __attribute__((ext_vector_type(2)));
typedef short v4s __attribute__((ext_vector_type(4)));
#define LAS __attribute__((address_space(3)))
#define GAS __attribute__((address_space(1)))
typedef GAS u32 gu32;
typedef __amdgpu_buffer_rsrc_t rsrc_t;

#define AFFINE 0

constexpr int L = 2048, D = 448, NS = 256, F = 1792;
constexpr int NMAIN = 224, NHELP = 32;

constexpr u32 WS_EPOCH = 0;
constexpr u32 WS_FLAG_W = 1024;
constexpr u32 WS_FLAG_G = WS_FLAG_W + 1024;
constexpr u32 WS_FLAG_U = WS_FLAG_G + 1024;
constexpr u32 WS_GATE = 245760;
#define HF_P 0
#define LDA_N 20
#define AST_N 19
#define HL_P 9
#define CHK_N 99
#define AHD 9
#define HL_D 2
#define PAR_FIRST 1
#define HDELAY 64
#define HFS 32
constexpr u32 WS_FLAG_H = 16384;
constexpr u32 WS_UT = 262144;
constexpr u32 WS_A2 = WS_UT + D * L * 2;
constexpr u32 WS_H = WS_A2 + D * L * 4;
constexpr u32 WS_WT = WS_H + L * F * 2;
constexpr u32 WS_END = WS_WT + F * D * 2;
constexpr u32 WS_STAMP = 16u << 20;
constexpr u32 TAG_STEP = 0x9E3779B9u;

constexpr u32 LDS_X = 0;
constexpr u32 LDS_W = 32768;
constexpr u32 LDS_SCR = 131072;
constexpr u32 LDS_TOTAL = 163840;
constexpr u32 LDS_PAR = LDS_W + 2 * 32768 + 16384;
constexpr u32 LDS_WST = LDS_W + 2 * 32768;
constexpr u32 KCOPY = 4160, KCH = 4 * KCOPY;
constexpr u32 LDS_KIMG0 = LDS_W + 32768, LDS_KIMG1 = LDS_SCR + 4096;
constexpr u32 UCH = 512 + 4096;
constexpr u32 LDS_UIMG = LDS_KIMG0 + KCH;
constexpr u32 LDS_TAB = 91648;
constexpr u32 LDS_Y = 0;
constexpr u32 PROW = 272, PWAVE = 16 * PROW, PBUF = 8 * PWAVE;
constexpr u32 LDS_P = 91648;
constexpr u32 LDS_AST = 65536;

struct ThreadTab { u32 v[512][8]; };
constexpr int round_bf16(int s) {
  const int sh = s >= 1024 ? 3 : s >= 512 ? 2 : s >= 256 ? 1 : 0;
  return sh ? ((s + (1 << (sh - 1)) - 1 + ((s >> sh) & 1)) >> sh) << sh : s;
}
constexpr int tab_pos(int s) {
  if (s < 0) return 2 * 512 + 32 * 8;
  const int v = round_bf16(s);
  int x = 0, row = 0, j = 0;
  if (v < 256) { j = v >> 3; row = v & 7; }
  else if (v < 512) { j = (v - 256) >> 3; row = 8 + ((v & 7) >> 1); }
  else if (v < 1024) { j = (v & 255) >> 3; row = 12 + 2 * ((v - 512) >> 8) + ((v & 7) >> 2); }
  else { x = 1; j = (v & 255) >> 3; row = (v - 1024) >> 8; }
  return (2 * x + (j >> 4)) * 512 + ((j & 15) + 16 * (row >> 2)) * 8 + (row & 3) * 2;
}
constexpr ThreadTab make_tab() {
  ThreadTab t{};
  for (int tid = 0; tid < 512; ++tid) {
    const int l = tid & 63, w = tid >> 6, wt = w & 1, wf = w >> 1, G = l >> 4, q = (l & 15) >> 2, p = l & 3;
    u32 e[16] = {};
    for (int j = 0; j < 11; ++j) e[j] = (tid >> 8) * 2048 + tab_pos(2047 - 8 * (tid & 255) - j);
    e[11] = ((tid & 127) < 16 ? ((tid & 8) ? LDS_KIMG1 : LDS_KIMG0) + ((tid >> 1) & 3) * KCOPY + 4096 + (tid & 1) * 16
                              : LDS_UIMG + ((tid >> 5) & 1) * UCH + (tid & 31) * 16) >> 4;
    e[12] = (LDS_UIMG + ((tid >> 2) & 1) * UCH + 512 + (tid >> 3) * 64 + (tid & 3) * 16) >> 4;
    e[13] = (4 * wf + G) * 1024 + q * 128 + ((2 * wt + (p >> 1)) ^ (4 * (G & 1) + (q >> 1))) * 16 + 8 * (p & 1);
    e[14] = LDS_X + (8 * wt + ((l & 31) >> 3)) * 1024 + (l & 7) * 128 + (((l >> 5) ^ ((l >> 1) & 7)) * 16);
    e[15] = LDS_W + (8 * (G >> 1) + q) * 256 + (((4 * wf + 2 * (G & 1) + (p >> 1)) ^ (4 * q)) * 16) + 8 * (p & 1);
    for (int k = 0; k < 8; ++k) t.v[tid][k] = e[2 * k] | (e[2 * k + 1] << 16);
  }
  return t;
}
static __device__ const ThreadTab thread_tab = make_tab();

#define SB0() __builtin_amdgcn_sched_barrier(0)
#define WAITV(n) asm volatile("s_waitcnt vmcnt(" #n ")" ::: "memory")
#define WAITL0() asm volatile("s_waitcnt lgkmcnt(0)" ::: "memory")
#define BAR() __builtin_amdgcn_s_barrier()
#define PRIO(n) __builtin_amdgcn_s_setprio(n)
#define RLX_AGENT __ATOMIC_RELAXED, __HIP_MEMORY_SCOPE_AGENT
constexpr int CODE_KB = 16;
constexpr int SPIN_CAP = 1 << 18;

#define STAMP(i) do { } while (0)
#define WST(i) do { } while (0)
#define WSC(i) do { } while (0)

extern __shared__ __attribute__((aligned(16))) char smem[];

__device__ __forceinline__ float bf_lo(u32 x) { return __uint_as_float(x << 16); }
__device__ __forceinline__ float bf_hi(u32 x) { return __uint_as_float(x & 0xffff0000u); }
__device__ __forceinline__ u32 pk_bf(float lo, float hi) {
  v2f v = {lo, hi};
  return __builtin_bit_cast(u32, __builtin_convertvector(v, v2bf));
}
__device__ __forceinline__ float dot2bf(u32 a, u32 b, float c) {
  return __builtin_amdgcn_fdot2_f32_bf16(__builtin_bit_cast(v2bf, a), __builtin_bit_cast(v2bf, b), c, false);
}
__device__ __forceinline__ float rbf(float x) { return bf_lo(pk_bf(x, x)); }
template <class T> __device__ __forceinline__ T lds_ld(u32 off) { return *(const LAS T*)((LAS char*)smem + off); }
template <class T> __device__ __forceinline__ void lds_st(u32 off, T v) { *(LAS T*)((LAS char*)smem + off) = v; }
__device__ __forceinline__ v2u lds_tr(u32 off) {
  return __builtin_bit_cast(v2u, __builtin_amdgcn_ds_read_tr16_b64_v4i16((LAS v4s*)((LAS char*)smem + off)));
}
__device__ __forceinline__ v8bf as_frag(v4u x) { return __builtin_bit_cast(v8bf, x); }
__device__ __forceinline__ v8bf as_frag(v2u a, v2u b) { v4u x = {a.x, a.y, b.x, b.y}; return __builtin_bit_cast(v8bf, x); }
__device__ __forceinline__ rsrc_t mk_rsrc(const void* p, u32 bytes) {
  return __builtin_amdgcn_make_buffer_rsrc((void*)p, (short)0, (int)bytes, 0x00020000);
}
template <int AUX> __device__ __forceinline__ void st16(v4u v, rsrc_t r, u32 voff) {
  __builtin_amdgcn_raw_buffer_store_b128(v, r, voff, 0, AUX);
}
__device__ __forceinline__ void dma16(rsrc_t r, u32 voff, u32 soff, u32 lds) {
  asm volatile("s_mov_b32 m0, %0\n\ts_nop 0\n\tbuffer_load_dwordx4 %1, %2, %3 offen lds"
               :: "s"(lds), "v"(voff), "s"(r), "s"(soff) : "memory");
}
template <int LO, int SO> __device__ __forceinline__ void dma16i(rsrc_t r, u32 voff, u32 soff, u32 lds) {
  u32 t;
  asm volatile("s_add_u32 m0, %1, %5\n\ts_add_u32 %0, %4, %6\n\tbuffer_load_dwordx4 %2, %3, %0 offen lds"
               : "=&s"(t) : "s"(lds), "v"(voff), "s"(r), "s"(soff), "i"(LO), "i"(SO) : "memory", "scc");
}
__device__ __forceinline__ void dma4_sc1(rsrc_t r, u32 voff, u32 soff, u32 lds) {
  asm volatile("s_mov_b32 m0, %0\n\ts_nop 0\n\tbuffer_load_dword %1, %2, %3 offen sc1 lds"
               :: "s"(lds), "v"(voff), "s"(r), "s"(soff) : "memory");
}
typedef v2f cpx;
#define CM_A(x, y, ax, bx, by) "v_mul_f32 " x ", " ax ", " bx "\n\tv_mul_f32 " y ", " ax ", " by "\n\t"
#define CM_B(x, y, ay, bx, nb) "v_fmac_f32 " x ", " ay ", " nb "\n\tv_fmac_f32 " y ", " ay ", " bx "\n\t"
__device__ __forceinline__ void cmul4(cpx& r0, cpx& r1, cpx& r2, cpx& r3, cpx a0, cpx a1, cpx a2, cpx a3, cpx b, float nb) {
  float x0, y0, x1, y1, x2, y2, x3, y3;
  asm volatile(CM_A("%0", "%1", "%8", "%16", "%17") CM_A("%2", "%3", "%10", "%16", "%17") CM_A("%4", "%5", "%12", "%16", "%17")
               CM_A("%6", "%7", "%14", "%16", "%17") CM_B("%0", "%1", "%9", "%16", "%18") CM_B("%2", "%3", "%11", "%16", "%18")
               CM_B("%4", "%5", "%13", "%16", "%18") CM_B("%6", "%7", "%15", "%16", "%18")
               : "=&v"(x0), "=&v"(y0), "=&v"(x1), "=&v"(y1), "=&v"(x2), "=&v"(y2), "=&v"(x3), "=&v"(y3)
               : "v"(a0.x), "v"(a0.y), "v"(a1.x), "v"(a1.y), "v"(a2.x), "v"(a2.y), "v"(a3.x), "v"(a3.y), "v"(b.x), "v"(b.y), "v"(nb));
  r0 = (cpx){x0, y0}; r1 = (cpx){x1, y1}; r2 = (cpx){x2, y2}; r3 = (cpx){x3, y3};
}
__device__ __forceinline__ void cmul2(cpx& r0, cpx& r1, cpx a0, cpx b0, float n0, cpx a1, cpx b1, float n1) {
  float x0, y0, x1, y1;
  asm volatile(CM_A("%0", "%1", "%4", "%6", "%7") CM_A("%2", "%3", "%9", "%11", "%12")
               CM_B("%0", "%1", "%5", "%6", "%8") CM_B("%2", "%3", "%10", "%11", "%13")
               : "=&v"(x0), "=&v"(y0), "=&v"(x1), "=&v"(y1)
               : "v"(a0.x), "v"(a0.y), "v"(b0.x), "v"(b0.y), "v"(n0), "v"(a1.x), "v"(a1.y), "v"(b1.x), "v"(b1.y), "v"(n1));
  r0 = (cpx){x0, y0}; r1 = (cpx){x1, y1};
}
__device__ __forceinline__ v2f add2x2s(v2f a, v2f b, v2f c, v2f d) {
  float x, y, z, t;
  asm("v_add_f32 %0, %4, %6\n\tv_add_f32 %1, %5, %7\n\tv_add_f32 %2, %8, %10\n\tv_add_f32 %3, %9, %11\n\t"
      "v_add_f32 %0, %0, %2\n\tv_add_f32 %1, %1, %3"
      : "=&v"(x), "=&v"(y), "=&v"(z), "=&v"(t)
      : "v"(a.x), "v"(a.y), "v"(b.x), "v"(b.y), "v"(c.x), "v"(c.y), "v"(d.x), "v"(d.y));
  return (v2f){x, y};
}
__device__ __forceinline__ u32 mix2(float g, u32 a, v2f b0, v2f b1) {
  float x, y, lo, hi;
  u32 o;
  asm("v_add_f32 %1, %7, %9\n\tv_add_f32 %2, %8, %10\n\tv_lshlrev_b32 %3, 16, %6\n\tv_and_b32 %4, 0xffff0000, %6\n\t"
      "v_sub_f32 %3, %3, %1\n\tv_sub_f32 %4, %4, %2\n\tv_fmac_f32 %1, %5, %3\n\tv_fmac_f32 %2, %5, %4\n\t"
      "s_nop 0\n\tv_cvt_pk_bf16_f32 %0, %1, %2"
      : "=&v"(o), "=&v"(x), "=&v"(y), "=&v"(lo), "=&v"(hi)
      : "v"(g), "v"(a), "v"(b0.x), "v"(b0.y), "v"(b1.x), "v"(b1.y));
  return o;
}
__device__ __forceinline__ v4f add4s(v4f a, v4f b) {
  float x, y, z, t;
  asm("v_add_f32 %0, %4, %8\n\tv_add_f32 %1, %5, %9\n\tv_add_f32 %2, %6, %10\n\tv_add_f32 %3, %7, %11"
      : "=&v"(x), "=&v"(y), "=&v"(z), "=&v"(t)
      : "v"(a.x), "v"(a.y), "v"(a.z), "v"(a.w), "v"(b.x), "v"(b.y), "v"(b.z), "v"(b.w));
  return (v4f){x, y, z, t};
}
constexpr int frow(int r) {
  return r < 7 ? LDS_W + 32768 + r * 288 : r < 13 ? LDS_W + 49152 + 224 + (r - 7) * 288 : LDS_W + 65536 + 1024 + 160 + (r - 13) * 288;
}
constexpr int FM0 = 49152;
constexpr float RND_UP = 1.0027f;
template <int R> __device__ __forceinline__ void rows4(v4u& keep, u32 m0v, cpx a, cpx b, cpx c, cpx d) {
  u32 t0, t1, t2, t3;
  asm volatile("s_mov_b32 m0, %4\n\tv_perm_b32 %0, %6, %5, %21\n\tv_perm_b32 %1, %8, %7, %21\n\tv_perm_b32 %2, %10, %9, %21\n\t"
               "v_perm_b32 %3, %12, %11, %21\n\tds_write_addtid_b32 %0 offset:%17\n\tds_write_addtid_b32 %1 offset:%18\n\t"
               "ds_write_addtid_b32 %2 offset:%19\n\tds_write_addtid_b32 %3 offset:%20"
               : "=&v"(t0), "=&v"(t1), "=&v"(t2), "=&v"(t3)
               : "s"(m0v), "v"(a.x), "v"(a.y), "v"(b.x), "v"(b.y), "v"(c.x), "v"(c.y), "v"(d.x), "v"(d.y),
                 "v"(keep.x), "v"(keep.y), "v"(keep.z), "v"(keep.w),
                 "i"(frow(R) - FM0), "i"(frow(R + 1) - FM0), "i"(frow(R + 2) - FM0), "i"(frow(R + 3) - FM0), "s"(0x07060302u)
               : "memory");
  keep = (v4u){t0, t1, t2, t3};
}
template <int H> __device__ __forceinline__ u32 add_half(u32 base, u32 x) {
  u32 r;
  if constexpr (H == 0) asm volatile("v_add_u32_sdwa %0, %1, %2 dst_sel:DWORD dst_unused:UNUSED_PAD src0_sel:DWORD src1_sel:WORD_0" : "=v"(r) : "s"(base), "v"(x));
  else asm volatile("v_add_u32_sdwa %0, %1, %2 dst_sel:DWORD dst_unused:UNUSED_PAD src0_sel:DWORD src1_sel:WORD_1" : "=v"(r) : "s"(base), "v"(x));
  return r;
}
__device__ __forceinline__ void flag_store(rsrc_t r, u32 soff, u32 tag_v) {
  asm volatile("s_mov_b64 exec, 1\n\tbuffer_store_dword %0, off, %1, %2 sc1\n\ts_mov_b64 exec, -1" :: "v"(tag_v), "s"(r), "s"(soff) : "memory");
}
__device__ __forceinline__ u32 sopaque(u32 x) { asm volatile("" : "+s"(x)); return x; }
__device__ __forceinline__ u32 opaque(u32 x) { asm volatile("" : "+v"(x)); return x; }
__device__ __forceinline__ cpx cpow_l(float a2, float bf, float l) {
  float m = __builtin_amdgcn_exp2f(a2 * l), ph = __builtin_amdgcn_fractf(bf * l);
  return (cpx){m * __builtin_amdgcn_cosf(ph), m * __builtin_amdgcn_sinf(ph)};
}
__device__ __forceinline__ void wait_flags(const gu32* p, u32 tag, u32 first, int code = 0) {
  if (__all(first == tag)) return;
  u32 v;
#pragma unroll 1
  for (int s = 0; s < SPIN_CAP; ++s) {
    v = __hip_atomic_load(p, RLX_AGENT);
    if (__all(v == tag)) break;
    __builtin_amdgcn_s_sleep(1);
  }
}

template <int V> struct ic { static constexpr int value = V; };
struct Args {
  const u16* par[8];
  const u16 *u, *wg, *wu, *wd, *gw;
  u16* out;
  char* ws;
};

__global__ __launch_bounds__(512, 2) void gated_mix_kernel(Args) {
  const int b = blockIdx.x, xcd = b & 7, bi = b >> 3;
  const int tid = threadIdx.x, l = tid & 63;
  const int w = __builtin_amdgcn_readfirstlane(tid >> 6);
  typedef u64 v4q __attribute__((ext_vector_type(4)));
  typedef u64 v2q __attribute__((ext_vector_type(2)));
  v4q ka0;
  v2q ka1;
  u64 ka2, par_w;
  asm volatile("s_load_dwordx8 %0, %4, 0x40\n\ts_load_dwordx4 %1, %4, 0x60\n\ts_load_dwordx2 %2, %4, 0x70\n\ts_load_dwordx2 %3, %4, %5"
               : "=&s"(ka0), "=&s"(ka1), "=&s"(ka2), "=&s"(par_w) : "s"(__builtin_amdgcn_kernarg_segment_ptr()), "s"(w * 8) : "memory");
  asm volatile("s_cmp_ge_u32 %0, %3\n\ts_cbranch_scc1 1f\n\ts_getpc_b64 s[4:5]\n\ts_mov_b32 m0, %2\n\ts_and_b32 s4, s4, 0xffffff00\n\t"
               "global_load_lds_dwordx4 %1, s[4:5]\n1:"
               :: "s"(bi * 8 + w), "v"((bi * 8 + w) * 1024 + l * 16), "s"(LDS_SCR + w * 4096), "i"(CODE_KB) : "s4", "s5", "scc", "memory");
  const int cb = xcd * 28 + bi;
  const int mt = (xcd & 3) * 4 + (bi & 3), ft = (xcd >> 2) * 7 + (bi >> 2);
  const int rot = ((bi >> 2) + 2 * (bi & 3) + (xcd & 3)) % 7;
  const int m2 = 4 * xcd + (bi & 3), nt = bi >> 2;
  const int wt = w & 1, wf = w >> 1;

  const u32 vl16 = opaque(l * 16);
  const u32 vx0 = opaque((l >> 3) * 896 + (((l & 7) ^ (l >> 4)) * 16)), vx1 = opaque((vx0 ^ 64) + 8 * 896);
  const u32 vw = opaque((l >> 4) * (F * 2) + (((l & 15) ^ (4 * (l >> 4))) * 16));
  const u32 sx = sopaque((mt * 128 + 16 * w) * 896 + rot * 128), sw = sopaque(8 * w * (F * 2) + ft * 256 + rot * (64 * F * 2)), lw = sopaque(w * 2048);
  const u32 par_o = sopaque(w < 6 ? cb * 1024 : (cb * 4) & ~15), par_n = sopaque(w < 6 ? D * NS * 2 : D * 2), par_l = sopaque(LDS_PAR + w * 1024);
  asm volatile("s_waitcnt lgkmcnt(0)" : "+s"(ka0), "+s"(ka1), "+s"(ka2), "+s"(par_w) :: "memory");
  struct { const u16 *u, *wg, *wu, *wd, *gw; u16* out; char* ws; } g = {
      (const u16*)ka0.x, (const u16*)ka0.y, (const u16*)ka0.z, (const u16*)ka0.w, (const u16*)ka1.x, (u16*)ka1.y, (char*)ka2};
  char* ws = g.ws;
  const rsrc_t rws = mk_rsrc(ws, WS_END);
  const rsrc_t rx = mk_rsrc(g.u, L * D * 2);
  STAMP(0);

  if (bi >= 28) {
    const int hb = b - NMAIN, t0 = 64 * hb;
    constexpr u32 HX = 0, HG = 64 * 896;
    const rsrc_t rgw = mk_rsrc(g.gw, D * 2);
    if (HDELAY) __builtin_amdgcn_s_sleep(HDELAY);
#pragma unroll
    for (int j = 0; j < 7; ++j) dma16(rx, l * 16, (t0 + 8 * w) * 896 + j * 1024, HX + 8 * w * 896 + j * 1024);
    if (w == 0) { dma4_sc1(rws, 0, WS_EPOCH, HG + 1024); dma16(rgw, l * 16, 0, HG); }
    WAITV(0); BAR();
    STAMP(1);
    const u32 tag = __builtin_amdgcn_readfirstlane(lds_ld<u32>(HG + 1024)) + TAG_STEP;
    {
      const int r = 8 * w + (l >> 3);
      const u32 xa = opaque(HX + r * 896 + (l & 7) * 16), ga = opaque(HG + (l & 7) * 16);
      float s0 = 0.f, s1 = 0.f;
#pragma unroll
      for (int j = 0; j < 7; ++j) {
        const v4u xv = lds_ld<v4u>(xa + 128 * j), gv = lds_ld<v4u>(ga + 128 * j);
#pragma unroll
        for (int e = 0; e < 4; e += 2) {
          s0 = dot2bf(xv[e], gv[e], s0);
          s1 = dot2bf(xv[e + 1], gv[e + 1], s1);
        }
      }
      float s = s0 + s1;
      s += __shfl_xor(s, 1); s += __shfl_xor(s, 2); s += __shfl_xor(s, 4);
      s = rbf(s);
      float gt = rbf(__builtin_amdgcn_rcpf(1.f + __builtin_amdgcn_exp2f(-1.44269504f * s)));
      if ((l & 7) < 2) __hip_atomic_store((gu32*)(ws + WS_GATE) + hb * 128 + (r & 15) * 8 + (r >> 4) + 4 * (l & 1), __float_as_uint(gt), RLX_AGENT);
    }
    WAITV(0); BAR();
    if (tid == 0) __hip_atomic_store((gu32*)(ws + WS_FLAG_G) + hb, tag, RLX_AGENT);
    STAMP(2);
    if (hb == NHELP - 1) {
      if (w < 4) wait_flags((const gu32*)(ws + (tid < NMAIN ? WS_FLAG_W : WS_FLAG_G - NMAIN * 4)) + tid, tag, ~tag, 1);
      BAR();
      if (tid == 0) __hip_atomic_store((gu32*)(ws + WS_EPOCH), tag, RLX_AGENT);
    }
    return;
  }

  {
    const rsrc_t rp = mk_rsrc((const void*)par_w, par_n);
    dma16(rp, vl16, par_o, par_l);
    if (w == 0) dma4_sc1(rws, 0, WS_EPOCH, LDS_PAR + 8192);
  }
  const rsrc_t rwg = mk_rsrc(g.wg, D * F * 2), rwu = mk_rsrc(g.wu, D * F * 2);
  auto piece = [&](auto S, auto I) {
    constexpr int s = decltype(S)::value, i = decltype(I)::value;
    const bool wrap = s + rot >= 7;
    if constexpr (i < 2) dma16i<LDS_X + (s & 1) * 16384 + i * 1024, s * 128>(rx, i ? vx1 : vx0, wrap ? sx - 7 * 128 : sx, lw);
    else dma16i<LDS_W + (s % 3) * 32768 + (i >= 4) * 16384 + (i & 1) * 1024, (64 * s + 4 * (i & 1)) * F * 2>(i < 4 ? rwg : rwu, vw, wrap ? sw - 7 * 64 * F * 2 : sw, lw);
  };
#define PIECE(s, i) piece(ic<(s)>{}, ic<(i)>{})
  const int ct = 7 * (cb & 3) + (w < 7 ? w : 6);
  const int G = l >> 4, q = (l & 15) >> 2, p = l & 3;
  const int usl = 2 * rot + (ft >= 7);
  const u32 srd = opaque(lw + ((l & 15) < 7 ? frow(0) : (l & 15) < 13 ? frow(7) - 7 * 288 : frow(13) - 13 * 288) + (l & 15) * 288 + (l >> 4) * 16);
  const u32 po = opaque(LDS_PAR + (w >> 2) * 512 + (w & 3) * 128 + l * 2);
  const u32 pld = opaque(LDS_PAR + 6144 + ((cb * 4) & 15) + 2 * (w >> 2)), pdk = opaque(LDS_PAR + 7168 + ((cb * 4) & 15));
  const u32 wst_v = opaque((l >> 1) * 896 + (l & 1) * 16), tab_v = opaque(tid * 32);
  const u32 wst_a = opaque(LDS_WST + lw + (16 * (G >> 1) + 4 * (G & 1) + q) * 32 + p * 8);
  const u32 wt_o = opaque(WS_WT + (((ct >> 2) * 56 + (cb >> 2)) * 4 + (ct & 3)) * 1024 + vl16);
  const u32 kpw = opaque(LDS_SCR + w * 4096 + vl16), kpr = opaque(LDS_SCR + (w & 4) * 4096 + (w & 3) * 1024 + vl16);
  const u32 s_ut = opaque(WS_UT + (((2 * mt + (wf >> 1)) * 28 + 2 * usl + wt) * 2 + (wf & 1)) * 1024 + (l & 15) * 64 + G * 16);
  const u32 wfo = opaque(WS_FLAG_W + 16 * (l < 56 ? l : 0));
  const u32 ufo = opaque(l < 16 ? WS_FLAG_U + ((cb >> 4) * 16 + l) * 4 : WS_FLAG_G + m2 * 4);
  PIECE(0, 0); PIECE(0, 1); PIECE(0, 2); PIECE(0, 3);
  WAITV(4); BAR();
  PRIO(3);
  STAMP(1);

  auto frags = [&](v8bf (&f)[4]) {
#pragma unroll
    for (int k = 0; k < 4; ++k) f[k] = as_frag(lds_ld<v4u>(srd + k * 64));
  };
  u32 tag, dsk2;
  v4u tb0, tb1;
  {
    cpx c0, w1, w2, w256, w512;
    const u32 fm0 = lw + FM0;
    v8bf J[2][4], Hf[4], Hg[4];
    v4u keep = {0, 0, 0, 0};
    const float ld = bf_lo(lds_ld<u16>(pld));
    const u32 q0 = lds_ld<u16>(po), q1 = lds_ld<u16>(po + 1024), q2 = lds_ld<u16>(po + 2048), q3 = lds_ld<u16>(po + 3072);
    const u32 q4 = lds_ld<u16>(po + 4096), q5 = lds_ld<u16>(po + 5120);
    const u32 ep = lds_ld<u32>(pdk + 1024), dk = lds_ld<u32>(pdk);
    PIECE(0, 4); PIECE(0, 5);
    const float dt = rbf(__builtin_amdgcn_exp2f(ld * 1.44269504f));
    const float a2 = (dt * bf_lo(q0)) * 1.44269504f, bf = (dt * bf_lo(q1)) * 0.15915494f;
    const cpx w8 = cpow_l(a2, bf, 8.f);
    w1 = cpow_l(a2, -bf, 1.f); w256 = cpow_l(a2, -bf, 256.f);
    const cpx cc = {bf_lo(q4), -bf_lo(q5)}, bb = {(dt * RND_UP) * bf_lo(q2), -(dt * RND_UP) * bf_lo(q3)};
    tag = __builtin_amdgcn_readfirstlane(ep) + TAG_STEP; dsk2 = __builtin_amdgcn_readfirstlane(dk);
    cpx w16, w24, w32, j1, j2, j3;
    cmul2(w16, c0, w8, w8, -w8.y, cc, bb, -bb.y);
    cmul2(w24, w32, w16, w8, -w8.y, w16, w16, -w16.y);
    cpx j0 = {RND_UP, 0.f};
    j1 = w8 * RND_UP; j2 = w16 * RND_UP; j3 = w24 * RND_UP;
    const float n32 = -w32.y, n1 = -w1.y, n256 = -w256.y;
#define JSTEP(k) rows4<((k) & 3) * 4>(keep, fm0, j0, j1, j2, j3)
#define JNEXT() cmul4(j0, j1, j2, j3, j0, j1, j2, j3, w32, n32)
    JSTEP(0); JNEXT();
    JSTEP(1); JNEXT();
    PRIO(2);
    dma16(mk_rsrc(g.wd, F * D * 2), wst_v, 32 * (cb >> 2) * 896 + ct * 32, LDS_WST + lw);
    JSTEP(2); JNEXT();
    JSTEP(3); frags(J[0]); JNEXT();
    JSTEP(4); JNEXT();
    JSTEP(5); JNEXT();
    JSTEP(6); JNEXT();
    cmul2(w2, w512, w1, w1, n1, w256, w256, n256);
    PRIO(1);
    asm volatile("global_load_dwordx4 %0, %2, %3\n\tglobal_load_dwordx4 %1, %2, %3 offset:16"
                 : "=&v"(tb0), "=&v"(tb1) : "v"(tab_v), "s"(&thread_tab.v[0][0]) : "memory");
    JSTEP(7); frags(J[1]);
    cpx c1, c2, c3, c4, c5, c6, c7, w4, d0, d2, d4, d6, e0, e4, f0, f4, g0, g1, g2, g3, g4;
    const float n2 = -w2.y, n512 = -w512.y;
    cmul2(c1, c2, c0, w1, n1, c0, w2, n2);
    cmul2(c3, w4, c2, w1, n1, w2, w2, n2);
    cmul4(c4, c5, c6, c7, c0, c1, c2, c3, w4, -w4.y);
    rows4<0>(keep, fm0, c0, c1, c2, c3);
    cmul4(d0, d2, d4, d6, c0, c2, c4, c6, w256, n256);
    rows4<4>(keep, fm0, c4, c5, c6, c7);
    cmul4(e0, e4, f0, f4, c0, c4, d0, d4, w512, n512);
    rows4<8>(keep, fm0, d0, d2, d4, d6);
    cmul2(g0, g1, e0, w512, n512, f0, w512, n512);
    PRIO(0);
    rows4<12>(keep, fm0, e0, e4, f0, f4);
    frags(Hf);
    cmul2(g2, g3, g0, w512, n512, g1, w512, n512);
    g4 = (cpx){g2.x * w512.x + g2.y * n512, g2.x * w512.y + g2.y * w512.x};
    rows4<0>(keep, fm0, g0, g1, g2, g3);
    asm volatile("ds_write_addtid_b32 %0 offset:%1" :: "v"(__builtin_amdgcn_perm(__float_as_uint(g4.y), __float_as_uint(g4.x), 0x07060302u)),
                 "i"(frow(4) - FM0) : "memory");
    frags(Hg);
    SB0();
    v4f kc[4];
    auto kmm = [&](int k) {
#pragma unroll
      for (int t = 0; t < 4; ++t)
        kc[t] = __builtin_amdgcn_mfma_f32_16x16x32_bf16((t >> 1) ? Hg[k] : Hf[k], J[t & 1][k], k == 0 ? (v4f){0.f, 0.f, 0.f, 0.f} : kc[t], 0, 0, 0);
      SB0();
    };
    WAITL0();
    kmm(0); PIECE(1, 0); kmm(1); PIECE(1, 1); kmm(2); PIECE(1, 2); kmm(3); PIECE(1, 3);
#pragma unroll
    for (int t = 0; t < 4; ++t) lds_st<v4f>(kpw + t * 1024, kc[t]);
    SB0();
    PIECE(1, 4); PIECE(1, 5);
  }
  STAMP(13);
  asm volatile("s_waitcnt vmcnt(6)" : "+v"(tb0), "+v"(tb1) :: "memory");
  const u32 a_ut = (tb1.z >> 16) ^ ((usl & 1) << 6);
  u32 xa[4], wa[3];
  xa[0] = opaque(tb1.w & 0xffff);
#pragma unroll
  for (int s = 1; s < 4; ++s) xa[s] = opaque(xa[0] ^ (32 * s));
  wa[0] = opaque(tb1.w >> 16);
  wa[1] = opaque(wa[0] + 32768); wa[2] = opaque(wa[0] + 65536);
  const u32 tag_v = opaque(tag);
  STAMP(2);

  v16f accg[2] = {}, accu[2] = {};
  v4u ut4, gate4;
  v4u wfr[28];
  const int kr = w, krot = nt & 4;
  u32 koff[7];
#pragma unroll
  for (int i = 0; i < 7; ++i) koff[i] = (kr * 7 + (i + krot >= 7 ? i + krot - 7 : i + krot)) * 1024;
  const u32 wd_so = WS_WT + nt * 56 * 4096;
  auto ldw = [&](int i) { wfr[i] = __builtin_amdgcn_raw_buffer_load_b128(rws, vl16 + (i & 3) * 1024, wd_so + 4 * koff[i >> 2], 16); };
  u32 hfo, hadr, hadr2, ka, ka8, ub, yb, yo, uo, xe[2][5], xkb, xz, uia;
  auto img_writes = [&]() {
#pragma unroll
    for (int o = 0; o < 2; ++o) {
      lds_st<v4u>(xkb + (3 - o) * KCOPY, (v4u){xe[o][0], xe[o][1], xe[o][2], xe[o][3]});
      lds_st<v4u>(xkb + (1 - o) * KCOPY, (v4u){xe[o][1], xe[o][2], xe[o][3], xe[o][4]});
    }
    lds_st<v4u>(xz, (v4u){0, 0, 0, 0});
    lds_st<v4u>(uia, ut4);
  };
  v2f nl2e = {-1.44269504f, -1.44269504f};
  v4u hst[4];
  auto silu8 = [&](int gI) {
    const int j = gI >> 1, r = 8 * (gI & 1);
    v2f gv[4], pv[4], ev[4];
#pragma unroll
    for (int e = 0; e < 4; ++e) { gv[e] = (v2f){accg[j][r + 2 * e], accg[j][r + 2 * e + 1]}; ev[e] = gv[e] * nl2e; }
    SB0();
#pragma unroll
    for (int e = 0; e < 4; ++e) ev[e] = (v2f){__builtin_amdgcn_exp2f(ev[e].x), __builtin_amdgcn_exp2f(ev[e].y)};
    SB0();
#pragma unroll
    for (int e = 0; e < 4; ++e) { ev[e] = ev[e] + 1.f; pv[e] = gv[e] * (v2f){accu[j][r + 2 * e], accu[j][r + 2 * e + 1]}; }
    SB0();
#pragma unroll
    for (int e = 0; e < 4; ++e) ev[e] = (v2f){__builtin_amdgcn_rcpf(ev[e].x), __builtin_amdgcn_rcpf(ev[e].y)};
    SB0();
#pragma unroll
    for (int e = 0; e < 4; ++e) pv[e] = pv[e] * ev[e];
    SB0();
#pragma unroll
    for (int e = 0; e < 4; ++e) hst[gI][e] = pk_bf(pv[e].x, pv[e].y);
    st16<16>(hst[gI], rws, (j ? hadr2 : hadr) + (r >> 3) * 512);
    if (gI > 0) asm volatile("" :: "v"(hst[gI - 1]));
    SB0();
  };
  {
    v8bf fg[2], fu[2], fx0[2], fx1[2];
    auto rd = [&](int buf, int st, int ks) {
      const u32 wb = wa[st % 3] + ks * 4096, xb = xa[ks] + (st & 1) * 16384;
      fg[buf] = as_frag(lds_tr(wb), lds_tr(wb + 1024));
      fx0[buf] = as_frag(lds_ld<v4u>(xb));
      fu[buf] = as_frag(lds_tr(wb + 16384), lds_tr(wb + 16384 + 1024));
      fx1[buf] = as_frag(lds_ld<v4u>(xb + 4096));
    };
    v4f ks4[4];
    u32 kp01, kp23;
    auto kgen = [&](int n) {
      if (n == 22) {
#pragma unroll
        for (int k = 0; k < 4; ++k) ks4[k] = lds_ld<v4f>(kpr + k * 4096);
      }
      if (n == 24) {
        const v4f s = add4s(add4s(ks4[0], ks4[1]), add4s(ks4[2], ks4[3]));
        const bool keep = !(w & 2) || l < 32;
        kp01 = opaque(keep ? pk_bf(s[0], s[1]) : 0); kp23 = opaque(keep ? pk_bf(s[2], s[3]) : 0);
      }
      if (n == 25) lds_st<v2u>(LDS_TAB + w * 512 + (vl16 >> 1), (v2u){kp01, kp23});
    };
    u32 xv[11], uta;
    auto expand = [&](int step) {
      if (step == 0) {
        xv[0] = lds_ld<u16>(add_half<0>(LDS_TAB, tb0.x)); xv[1] = lds_ld<u16>(add_half<1>(LDS_TAB, tb0.x));
        xv[2] = lds_ld<u16>(add_half<0>(LDS_TAB, tb0.y)); xv[3] = lds_ld<u16>(add_half<1>(LDS_TAB, tb0.y));
      } else if (step == 1) {
        xv[4] = lds_ld<u16>(add_half<0>(LDS_TAB, tb0.z)); xv[5] = lds_ld<u16>(add_half<1>(LDS_TAB, tb0.z));
        xv[6] = lds_ld<u16>(add_half<0>(LDS_TAB, tb0.w)); xv[7] = lds_ld<u16>(add_half<1>(LDS_TAB, tb0.w));
      } else if (step == 2) {
        xv[8] = lds_ld<u16>(add_half<0>(LDS_TAB, tb1.x)); xv[9] = lds_ld<u16>(add_half<1>(LDS_TAB, tb1.x));
        xv[10] = lds_ld<u16>(add_half<0>(LDS_TAB, tb1.y));
      } else if (step < 16) {
        const int o = step - 14;
#pragma unroll
        for (int j = 0; j < 5; ++j) xe[o][j] = opaque(xv[2 * j + o] | (xv[2 * j + o + 1] << 16));
      }
    };
    v2u utl, uth;
    v4u wflag;
    u32 uflag;
    auto side = [&](auto S, auto KS, auto H) {
      constexpr int s = decltype(S)::value, ks = decltype(KS)::value, h = decltype(H)::value;
      constexpr int st = 4 * s + ks;
      if constexpr (h == 0 || h == 2) {
        constexpr int i = h >> 1;
        if constexpr (ks == 3) { if constexpr (s < 5) PIECE(s + 2, i); }
        else if constexpr (s == 0) { if constexpr (ks < 2) PIECE(2, 2 + 2 * ks + i); }
        else if constexpr (ks < 2 && s + 2 < 7) PIECE(s + 2, 2 + 2 * ks + i);
        if constexpr (st >= 20) ldw(2 * (st - 20) + i);
        if constexpr (st == 3 && h == 2) st16<16>((v4u){utl.x, utl.y, uth.x, uth.y}, rws, s_ut);
      } else {
        constexpr int v = 2 * st + (h >> 1);
        if constexpr (v == 1) kgen(22);
        if constexpr (v == 4) kgen(24);
        if constexpr (v == 28) hfo = opaque(WS_FLAG_H + (m2 * 56 + 7 * kr + (l < 7 ? l : 0)) * (HFS * 4));
        if constexpr (v == 43) kgen(25);
        if constexpr (v >= 46 && v < 49) expand(v - 46);
        if constexpr (v == 52 || v == 53) expand(v - 38);
        if constexpr (v == 14) {
          xkb = opaque(((w >> 2) ? LDS_KIMG1 : LDS_KIMG0) + (tid & 255) * 16);
          xz = opaque((tb1.y >> 16) << 4);
          uia = opaque((tb1.z & 0xffff) << 4);
        }
        if constexpr (v == 16) uta = opaque((tid >> 4) * (56 * 1024) + ((tid >> 3) & 1) * 1024 + (tid & 7) * 16);
        if constexpr (v == 18) {
          hadr = opaque(WS_H + ((mt * 8 + 4 * wt + ((l & 31) >> 4)) * 56 + ft * 4 + wf) * 1024 + (l & 15) * 16 + (l >> 5) * 256);
          hadr2 = opaque(hadr + 2 * 56 * 1024);
          asm volatile("" : "+v"(nl2e));
        }
        if constexpr (v == 20) {
          ka = opaque(((w >> 2) ? LDS_KIMG1 : LDS_KIMG0) + (l & 3) * KCOPY + 2 * (2028 + 8 * G - 4 * q) - 512 * 7 - 128 * (w & 3) - 64);
          ka8 = opaque(ka + 8);
        }
        if constexpr (v == 22) ub = opaque(LDS_UIMG + (w >> 2) * UCH + 2 * (240 + 16 * (l & 15) + 8 * G) - 128 * (w & 3) - 64);
        if constexpr (v == 24) yb = opaque(LDS_Y + ((w & 3) * 2 + (w >> 2)) * 8192 + (16 * (l & 15) + 4 * (G ^ ((l >> 1) & 3))) * 4);
        if constexpr (v == 26) { yo = opaque(LDS_Y + (tid >> 2) * 64 + (((tid & 3) ^ ((tid >> 3) & 3)) * 16)); uo = opaque(LDS_UIMG + 512 + tid * 8); }
      }
    };
    auto mm = [&](int buf, auto S, auto KS) {
      accg[0] = __builtin_amdgcn_mfma_f32_32x32x16_bf16(fg[buf], fx0[buf], accg[0], 0, 0, 0);
      SB0(); side(S, KS, ic<0>{}); SB0();
      accu[0] = __builtin_amdgcn_mfma_f32_32x32x16_bf16(fu[buf], fx0[buf], accu[0], 0, 0, 0);
      SB0(); side(S, KS, ic<1>{}); SB0();
      accg[1] = __builtin_amdgcn_mfma_f32_32x32x16_bf16(fg[buf], fx1[buf], accg[1], 0, 0, 0);
      SB0(); side(S, KS, ic<2>{}); SB0();
      accu[1] = __builtin_amdgcn_mfma_f32_32x32x16_bf16(fu[buf], fx1[buf], accu[1], 0, 0, 0);
      SB0(); side(S, KS, ic<3>{});
      asm volatile("" :: "v"(fg[buf]), "v"(fu[buf]), "v"(fx0[buf]), "v"(fx1[buf]));
      SB0();
    };
    auto stage = [&](auto S) {
      constexpr int s = decltype(S)::value;
      PRIO(2); rd(1, s, 1); SB0(); mm(0, S, ic<0>{});
      PRIO(1); rd(0, s, 2); SB0(); mm(1, S, ic<1>{});
      PRIO(0); rd(1, s, 3); SB0(); mm(0, S, ic<2>{});
      PRIO(3);
      if constexpr (s < 6) {
        WAITL0();
        if constexpr (s < 2) WAITV(5);
        else if constexpr (s < 4) WAITV(4);
        else if constexpr (s == 4) {
          asm volatile("s_waitcnt vmcnt(4)" : "+v"(wflag) :: "memory");
          if (w == 0 && !__all(((wflag.x ^ tag_v) | (wflag.y ^ tag_v) | (wflag.z ^ tag_v) | (wflag.w ^ tag_v)) == 0)) {
#pragma unroll 1
            for (int sp = 0; sp < SPIN_CAP; ++sp) {
              asm volatile("" ::: "memory");
              const v4u f = __builtin_amdgcn_raw_buffer_load_b128(rws, wfo, 0, 16);
              if (__all(f.x == tag && f.y == tag && f.z == tag && f.w == tag)) break;
            }
          }
        } else {
          asm volatile("s_waitcnt vmcnt(%1)" : "+v"(uflag) : "i"(6) : "memory");
          if (w == 0) wait_flags((const gu32*)(ws + ufo), tag, uflag, 2);
        }
        BAR();
        rd(0, s + 1, 0);
        if constexpr (s == 1) { if (w == 0) flag_store(rws, WS_FLAG_W + cb * 4, tag_v); }
        if constexpr (s == 2) { if (w == 0) flag_store(rws, WS_FLAG_U + (usl * 16 + mt) * 4, tag_v); }
        if constexpr (s == 3)
          asm volatile("s_cmp_lg_u32 %3, 0\n\ts_cbranch_scc1 1f\n\tbuffer_load_dwordx4 %0, %1, %2, 0 offen sc1\n1:"
                       : "=v"(wflag) : "v"(wfo), "s"(rws), "s"(w) : "memory", "scc");
        if constexpr (s == 4)
          asm volatile("s_cmp_lg_u32 %3, 0\n\ts_cbranch_scc1 1f\n\tbuffer_load_dword %0, %1, %2, 0 offen sc1\n1:"
                       : "=v"(uflag) : "v"(ufo), "s"(rws), "s"(w) : "memory", "scc");
        if constexpr (s >= 4) __builtin_amdgcn_fence(__ATOMIC_ACQUIRE, "wavefront");
        if constexpr (s == 5) {
          ut4 = __builtin_amdgcn_raw_buffer_load_b128(rws, uta, WS_UT + (cb >> 3) * 2048 + (cb & 7) * 128, 16);
          gate4 = __builtin_amdgcn_raw_buffer_load_b128(rws, (tid >> 5) * 32, WS_GATE + m2 * 512 + (nt & 3) * 4, 16);
        }
        STAMP(s < 5 ? 22 + s : 20);
        if constexpr (s == 5) WSC(0);
        SB0();
      }
      mm(1, S, ic<3>{});
    };
    {
      const v2u lo = lds_tr(wst_a), hi = lds_tr(wst_a + 256);
      st16<16>((v4u){lo.x, lo.y, hi.x, hi.y}, rws, wt_o);
    }
    WAITL0(); BAR();
    STAMP(3);
    rd(0, 0, 0);
    utl = lds_tr(a_ut); uth = lds_tr((a_ut ^ 32) + 512);
    SB0();
    stage(ic<0>{}); stage(ic<1>{}); stage(ic<2>{}); stage(ic<3>{}); stage(ic<4>{}); stage(ic<5>{}); stage(ic<6>{});
  }
  STAMP(4);
  WSC(1);
  u32 hflag;
  v4u hf[28];
  u32 hso[4];
#pragma unroll
  for (int tt = 0; tt < 4; ++tt) hso[tt] = WS_H + (m2 * 4 + ((tt + nt) & 3)) * 56 * 1024;
  auto ldh = [&](int n) {
    const int i = n % 7;
    hf[n] = __builtin_amdgcn_raw_buffer_load_b128(rws, vl16, hso[n / 7] + koff[i], 16);
    SB0();
  };
  constexpr int AHEAD = AHD;
  {
    v4f acc[8];
    v8bf A[8][2], B[8][2];
    auto lda_ = [&](int pp) {
#pragma unroll
      for (int qq = 0; qq < 2; ++qq)
        A[pp][qq] = as_frag(lds_ld<v2u>(ka + 64 - 64 * qq + 512 * (7 - pp)), lds_ld<v2u>(ka8 + 64 - 64 * qq + 512 * (7 - pp)));
    };
#pragma unroll
    for (int gI = 0; gI < 4; ++gI) {
      silu8(gI);
      if (gI == 3) img_writes();
      SB0();
    }
    WAITL0(); BAR();
    lda_(0);
#pragma unroll
    for (int e = 7; e >= 0; --e) B[e][0] = as_frag(lds_ld<v4u>(ub + 64 + 512 * e));
#pragma unroll
    for (int e = 7; e >= 0; --e) B[e][1] = as_frag(lds_ld<v4u>(ub + 512 * e));
    lda_(1);
    asm volatile("" :: "v"(hst[3]));
    WAITV(0);
    flag_store(rws, WS_FLAG_H + ((2 * mt + wt) * 56 + 4 * ft + wf) * (HFS * 4), tag_v);
    WST(1);
    SB0();
    PRIO(2);
    asm volatile("" ::: "memory");
    STAMP(5);
    WST(0);
    WSC(2);
#pragma unroll
    for (int pp = 0; pp < 8; ++pp) {
#pragma unroll
      for (int qq = 0; qq < 2; ++qq) {
#pragma unroll
        for (int c = 7; c >= pp; --c) {
          acc[c] = __builtin_amdgcn_mfma_f32_16x16x32_bf16(A[pp][qq], B[c - pp][qq], (qq == 0 && pp == 0) ? (v4f){0.f, 0.f, 0.f, 0.f} : acc[c], 0, 0, 0);
          if (c == 5 && pp < 3) { SB0(); ldw(16 + 4 * pp + 2 * qq); SB0(); }
        }
        SB0();
        if (qq == 0) { if (pp < 6) lda_(pp + 2); if (pp > 0) lds_st<v4f>(yb + (pp - 1) * 1024, acc[pp - 1]); }
        if (pp < 3) ldw(17 + 4 * pp + 2 * qq);
        const int hs = 2 * pp + qq;
        if (hs == HL_P - HL_D) hflag = __builtin_amdgcn_raw_buffer_load_b32(rws, hfo, 0, 16);
        if (hs == HL_P) {
          WSC(5);
          STAMP(7);
          WST(2);
          wait_flags((const gu32*)(ws + hfo), tag, hflag, 3);
          __builtin_amdgcn_fence(__ATOMIC_ACQUIRE, "wavefront");
          STAMP(8);
          WST(3);
        }
        if (hs >= HL_P && 2 * (hs - HL_P) < 8) { ldh(2 * (hs - HL_P)); ldh(2 * (hs - HL_P) + 1); }
        if (qq == 1 && pp == 1) PRIO(1);
        if (qq == 1 && pp == 3) PRIO(0);
        SB0();
      }
    }
    lds_st<v4f>(yb + 7 * 1024, acc[7]);
  }
  WSC(6);

  {
    v4u a_out;
    v4f yv[8], ys[2];
    v2u uv[2];
    auto a_step = [&](int k) {
      if (k < 2) {
        uv[k] = lds_ld<v2u>(uo + k * UCH);
#pragma unroll
        for (int z = 0; z < 4; ++z) yv[4 * k + z] = lds_ld<v4f>(yo + k * 8192 + z * 16384);
      } else if (k < 4) {
        ys[k - 2] = add4s(add4s(yv[4 * k - 8], yv[4 * k - 7]), add4s(yv[4 * k - 6], yv[4 * k - 5]));
      } else {
        const int e = k - 4;
        const float k0 = bf_lo(dsk2), k1 = bf_hi(dsk2);
        a_out[2 * e] = opaque(pk_bf(__builtin_fmaf(bf_lo(uv[0][e]), k0, ys[0][2 * e]), __builtin_fmaf(bf_lo(uv[1][e]), k1, ys[1][2 * e])));
        a_out[2 * e + 1] = opaque(pk_bf(__builtin_fmaf(bf_hi(uv[0][e]), k0, ys[0][2 * e + 1]), __builtin_fmaf(bf_hi(uv[1][e]), k1, ys[1][2 * e + 1])));
      }
    };
    auto sta = [&]() {
      const u32 ao = vl16 + WS_A2 + cb * 16384 + w * 2048;
      st16<16>((v4u){a_out.x, tag, a_out.y, tag}, rws, ao);
      st16<16>((v4u){a_out.z, tag, a_out.w, tag}, rws, ao + 1024);
      SB0();
    };
#pragma unroll
    for (int n = 8; n < AHEAD - 1; ++n) ldh(n);
    STAMP(27);
    WST(4);
    WAITL0(); BAR();
    a_step(0); a_step(1);
    SB0();
    STAMP(12);
    WSC(7);
    v4u a4[2];
    u32 a_vo, pw, prd, asw, asr, oad;
    const u32 a_so = WS_A2 + 32 * nt * 16384 + (m2 >> 2) * 2048 + (m2 & 3) * 256;
    auto lda = [&]() {
      a4[0] = __builtin_amdgcn_raw_buffer_load_b128(rws, a_vo, a_so, 16);
      a4[1] = __builtin_amdgcn_raw_buffer_load_b128(rws, a_vo + 1024, a_so, 16);
    };
    auto check_a = [&]() {
          if (!__all(((a4[0].y ^ tag) | (a4[0].w ^ tag) | (a4[1].y ^ tag) | (a4[1].w ^ tag)) == 0)) {
#pragma unroll 1
            for (int sp = 0; sp < SPIN_CAP; ++sp) {
              asm volatile("" ::: "memory");
              lda();
              if (__all(a4[0].y == tag && a4[0].w == tag && a4[1].y == tag && a4[1].w == tag)) break;
            }
          }
          lds_st<v2u>(asw, (v2u){a4[0].x, a4[0].z}); lds_st<v2u>(asw + 8, (v2u){a4[1].x, a4[1].z});
        };
    v4f acc[2][4];
    v2f pr[8], bs[4][2];
    u32 av[4], oso[4], prd2;
    const rsrc_t ro = mk_rsrc(g.out, L * D * 2);
    auto pread = [&](int tt) {
#pragma unroll
      for (int k = 0; k < 8; ++k) pr[k] = lds_ld<v2f>((tt == 2 ? prd2 : prd + (tt == 1) * PBUF) + k * PWAVE);
    };
    auto psum = [&](int tt, int h) { bs[tt][h] = add2x2s(pr[4 * h], pr[4 * h + 1], pr[4 * h + 2], pr[4 * h + 3]); };
    auto fetch_a = [&]() {
#pragma unroll
      for (int e = 0; e < 4; ++e) av[e] = lds_ld<u32>(asr + e * 64);
    };
    auto mix = [&](int tt) {
      __builtin_amdgcn_raw_buffer_store_b32(mix2(__uint_as_float(gate4[tt]), av[tt], bs[tt][0], bs[tt][1]), ro, oad, oso[tt], 16);
    };
#pragma unroll
    for (int tt = 0; tt < 3; ++tt) {
#pragma unroll
      for (int i = 0; i < 7; ++i) {
        const int n = 7 * tt + i;
        if (i == 0) PRIO(3);
        if (i == 2) PRIO(2);
        if (i == 4) PRIO(1);
        if (i == 6) PRIO(0);
        if (n + AHEAD - 1 < 28) ldh(n + AHEAD - 1);
          if (n == 4) sta();
        if (n == LDA_N) lda();
        SB0();
#pragma unroll
        for (int c = 0; c < 4; ++c)
          acc[tt & 1][c] = __builtin_amdgcn_mfma_f32_16x16x32_bf16(as_frag(wfr[4 * i + c]), as_frag(hf[n]),
                                                                   i == 0 ? (v4f){0.f, 0.f, 0.f, 0.f} : acc[tt & 1][c], 0, 0, 0);
        SB0();
        if (n == 0) { STAMP(29); WST(5); }
        if (n == 5) pw = opaque(LDS_P + w * PWAVE + (l & 15) * PROW + G * 16);
        if (n == 6) { prd = opaque(LDS_P + (tid >> 5) * PROW + (tid & 31) * 8); prd2 = opaque(prd - LDS_P); }
        if (n == 8) a_vo = opaque((tid >> 4) * 16384 + (tid & 15) * 16);
        if (n == 9) asw = opaque(LDS_AST + (tid >> 4) * 272 + ((tid - 4 * nt) & 15) * 16);
        if (n == 10) asr = opaque(LDS_AST + (tid & 31) * 272 + (tid >> 5) * 4);
        if (n == 11) oad = opaque((tid >> 5) * (D * 2) + (tid & 31) * 4);
        if (n == 12) {
#pragma unroll
          for (int k = 0; k < 4; ++k) {
            oso[k] = (64 * m2 + 16 * ((k + nt) & 3)) * (D * 2) + 128 * nt;
            asm volatile("" : "+s"(oso[k]));
          }
        }
        if (tt > 0 && i < 4) lds_st<v4f>(pw + (tt - 1) * PBUF + i * 64, acc[(tt - 1) & 1][i]);
        if (tt > 1 && i == 0) pread(tt - 2);
        if (tt > 1 && (i == 2 || i == 3)) psum(tt - 2, i - 2);
          if (n < 4) a_step(n + 2);
        if (n == CHK_N) check_a();
        if (i == 6 && tt > 0) { WAITL0(); BAR(); }
        if (n > 0) asm volatile("" :: "v"(hf[n - 1]));
        SB0();
      }
      if (tt == 0) STAMP(9);
      if (tt == 1) STAMP(16);
      if (tt == 2) STAMP(17);
    }
    const u32 pw2 = pw - LDS_P;
#pragma unroll
    for (int c = 0; c < 4; ++c) {
      if (c == 0) PRIO(3);
      if (c == 1) PRIO(2);
      if (c == 2) PRIO(1);
      if (c == 3) PRIO(0);
#pragma unroll
      for (int i = 0; i < 7; ++i) {
        acc[1][c] = __builtin_amdgcn_mfma_f32_16x16x32_bf16(as_frag(wfr[4 * i + c]), as_frag(hf[21 + i]),
                                                            i == 0 ? (v4f){0.f, 0.f, 0.f, 0.f} : acc[1][c], 0, 0, 0);
        SB0();
        if (c == 0 && i == 0) pread(1);
        if (i == 2) lds_st<v4f>(pw2 + c * 64, acc[0][c]);
        if (i == 4 && c > 0) lds_st<v4f>(pw + (c - 1) * 64, acc[1][c - 1]);
        if (c == 1 && i == 0) psum(1, 0);
        if (c == 1 && i == 5) psum(1, 1);
        if (CHK_N < 21) {
          if (c == 0 && i == 3) fetch_a();
          if (c == 1 && i == 2) mix(0);
          if (c == 2 && i == 2) mix(1);
        } else if (c == 3 && i == 0) check_a();
        SB0();
      }
    }
    STAMP(18);
    lds_st<v4f>(pw + 3 * 64, acc[1][3]);
    WAITL0(); BAR();
    STAMP(10);
    if (CHK_N >= 21) fetch_a();
    pread(2);
    v2f pq[8];
#pragma unroll
    for (int k = 0; k < 8; ++k) pq[k] = lds_ld<v2f>(prd + k * PWAVE);
    if (CHK_N >= 21) { mix(0); mix(1); }
    psum(2, 0); psum(2, 1);
    mix(2);
    bs[3][0] = add2x2s(pq[0], pq[1], pq[2], pq[3]); bs[3][1] = add2x2s(pq[4], pq[5], pq[6], pq[7]);
    mix(3);
  }
}

extern "C" void kernel_launch(void* const* d_in, const int* in_sizes, int n_in, void* d_out, int out_size, void* d_ws,
                              size_t ws_size, hipStream_t stream) {
  Args a;
  for (int i = 0; i < 8; ++i) a.par[i] = (const u16*)d_in[1 + i];
  a.u = (const u16*)d_in[0];
  a.wg = (const u16*)d_in[10]; a.wu = (const u16*)d_in[11]; a.wd = (const u16*)d_in[12];
  a.gw = (const u16*)d_in[13];
  a.out = (u16*)d_out;
  a.ws = (char*)d_ws;
  (void)hipFuncSetAttribute((const void*)gated_mix_kernel, hipFuncAttributeMaxDynamicSharedMemorySize, LDS_TOTAL);
  gated_mix_kernel<<<NMAIN + NHELP, 512, LDS_TOTAL, stream>>>(a);
}
```

```cpp
#include <hip/hip_runtime.h>

typedef unsigned short u16;
typedef unsigned u32;
typedef unsigned long long u64;
typedef u32 v4u __attribute__((ext_vector_type(4)));
typedef u32 v2u __attribute__((ext_vector_type(2)));
typedef float v2f __attribute__((ext_vector_type(2)));
typedef float v4f __attribute__((ext_vector_type(4)));
typedef float v16f __attribute__((ext_vector_type(16)));
typedef __bf16 v8bf __attribute__((ext_vector_type(8)));
typedef __bf16 v2bf __attribute__((ext_vector_type(2)));
typedef short v4s __attribute__((ext_vector_type(4)));
#define LAS __attribute__((address_space(3)))
#define GAS __attribute__((address_space(1)))
typedef GAS u32 gu32;
typedef __amdgpu_buffer_rsrc_t rsrc_t;

#define AFFINE 0

constexpr int L = 2048, D = 448, NS = 256, F = 1792;
constexpr int NMAIN = 224, NHELP = 32;

constexpr u32 WS_EPOCH = 0;
constexpr u32 WS_FLAG_W = 1024;
constexpr u32 WS_FLAG_U = 2048;
constexpr u32 WS_GATE = 245760;
#define HF_P 0
#define LDA_N 20
#define AST_N 19
#define HL_P 8
#define CHK_N 99
#define AHD 9
#define HL_D 2
#define PAR_FIRST 1
#define HDELAY 16
#define ROTV 1
#define WROT 0
#define LW_G 16
#define LW_E 0
#define LW_L 0
#define WPF 1
#define WPD 40
#define HFS 32
constexpr u32 WS_FLAG_H = 16384;
constexpr u32 WS_UT = 262144;
constexpr u32 WS_A2 = WS_UT + D * L * 2;
constexpr u32 WS_H = WS_A2 + D * L * 4;
constexpr u32 WS_WT = WS_H + L * F * 2;
constexpr u32 WS_END = WS_WT + F * D * 2;
constexpr u32 WS_STAMP = 16u << 20;
constexpr u32 TAG_STEP = 0x9E3779B9u;

constexpr u32 LDS_X = 0;
constexpr u32 LDS_W = 32768;
constexpr u32 LDS_SCR = 131072;
constexpr u32 LDS_TOTAL = 163840;
constexpr u32 LDS_PAR = LDS_W + 2 * 32768 + 16384;
constexpr u32 LDS_WST = LDS_W + 2 * 32768;
constexpr u32 KCOPY = 4160, KCH = 4 * KCOPY;
constexpr u32 LDS_KIMG0 = LDS_W + 32768, LDS_KIMG1 = LDS_SCR + 4096;
constexpr u32 UCH = 512 + 4096;
constexpr u32 LDS_UIMG = LDS_KIMG0 + KCH;
constexpr u32 LDS_TAB = 91648;
constexpr u32 LDS_Y = 0;
constexpr u32 PROW = 272, PWAVE = 16 * PROW, PBUF = 8 * PWAVE;
constexpr u32 LDS_P = 91648;
constexpr u32 LDS_AST = 65536;

struct ThreadTab { u32 v[512][8]; };
constexpr int round_bf16(int s) {
  const int sh = s >= 1024 ? 3 : s >= 512 ? 2 : s >= 256 ? 1 : 0;
  return sh ? ((s + (1 << (sh - 1)) - 1 + ((s >> sh) & 1)) >> sh) << sh : s;
}
constexpr int tab_pos(int s) {
  if (s < 0) return 2 * 512 + 32 * 8;
  const int v = round_bf16(s);
  int x = 0, row = 0, j = 0;
  if (v < 256) { j = v >> 3; row = v & 7; }
  else if (v < 512) { j = (v - 256) >> 3; row = 8 + ((v & 7) >> 1); }
  else if (v < 1024) { j = (v & 255) >> 3; row = 12 + 2 * ((v - 512) >> 8) + ((v & 7) >> 2); }
  else { x = 1; j = (v & 255) >> 3; row = (v - 1024) >> 8; }
  return (2 * x + (j >> 4)) * 512 + ((j & 15) + 16 * (row >> 2)) * 8 + (row & 3) * 2;
}
constexpr ThreadTab make_tab() {
  ThreadTab t{};
  for (int tid = 0; tid < 512; ++tid) {
    const int l = tid & 63, w = tid >> 6, wt = w & 1, wf = w >> 1, G = l >> 4, q = (l & 15) >> 2, p = l & 3;
    u32 e[16] = {};
    for (int j = 0; j < 11; ++j) e[j] = (tid >> 8) * 2048 + tab_pos(2047 - 8 * (tid & 255) - j);
    e[11] = ((tid & 127) < 16 ? ((tid & 8) ? LDS_KIMG1 : LDS_KIMG0) + ((tid >> 1) & 3) * KCOPY + 4096 + (tid & 1) * 16
                              : LDS_UIMG + ((tid >> 5) & 1) * UCH + (tid & 31) * 16) >> 4;
    e[12] = (LDS_UIMG + ((tid >> 2) & 1) * UCH + 512 + (tid >> 3) * 64 + (tid & 3) * 16) >> 4;
    e[14] = LDS_X + (8 * wt + ((l & 31) >> 3)) * 1024 + (l & 7) * 128 + (((l >> 5) ^ ((l >> 1) & 7)) * 16);
    e[15] = LDS_W + (8 * (G >> 1) + q) * 256 + (((4 * wf + 2 * (G & 1) + (p >> 1)) ^ (4 * q)) * 16) + 8 * (p & 1);
    for (int k = 0; k < 8; ++k) t.v[tid][k] = e[2 * k] | (e[2 * k + 1] << 16);
  }
  return t;
}
static __device__ const ThreadTab thread_tab = make_tab();

#define SB0() __builtin_amdgcn_sched_barrier(0)
#define WAITV(n) asm volatile("s_waitcnt vmcnt(" #n ")" ::: "memory")
#define WAITL0() asm volatile("s_waitcnt lgkmcnt(0)" ::: "memory")
#define BAR() __builtin_amdgcn_s_barrier()
#define PRIO(n) __builtin_amdgcn_s_setprio(n)
#define RLX_AGENT __ATOMIC_RELAXED, __HIP_MEMORY_SCOPE_AGENT
constexpr int CODE_KB = 16;
constexpr int SPIN_CAP = 1 << 18;

#define STAMP(i) do { } while (0)
#define WST(i) do { } while (0)
#define WSC(i) do { } while (0)

extern __shared__ __attribute__((aligned(16))) char smem[];

__device__ __forceinline__ float bf_lo(u32 x) { return __uint_as_float(x << 16); }
__device__ __forceinline__ float bf_hi(u32 x) { return __uint_as_float(x & 0xffff0000u); }
__device__ __forceinline__ u32 pk_bf(float lo, float hi) {
  v2f v = {lo, hi};
  return __builtin_bit_cast(u32, __builtin_convertvector(v, v2bf));
}
__device__ __forceinline__ float dot2bf(u32 a, u32 b, float c) {
  return __builtin_amdgcn_fdot2_f32_bf16(__builtin_bit_cast(v2bf, a), __builtin_bit_cast(v2bf, b), c, false);
}
__device__ __forceinline__ float rbf(float x) { return bf_lo(pk_bf(x, x)); }
template <class T> __device__ __forceinline__ T lds_ld(u32 off) { return *(const LAS T*)((LAS char*)smem + off); }
template <class T> __device__ __forceinline__ void lds_st(u32 off, T v) { *(LAS T*)((LAS char*)smem + off) = v; }
__device__ __forceinline__ v2u lds_tr(u32 off) {
  return __builtin_bit_cast(v2u, __builtin_amdgcn_ds_read_tr16_b64_v4i16((LAS v4s*)((LAS char*)smem + off)));
}
__device__ __forceinline__ v8bf as_frag(v4u x) { return __builtin_bit_cast(v8bf, x); }
__device__ __forceinline__ v8bf as_frag(v2u a, v2u b) { v4u x = {a.x, a.y, b.x, b.y}; return __builtin_bit_cast(v8bf, x); }
__device__ __forceinline__ rsrc_t mk_rsrc(const void* p, u32 bytes) {
  return __builtin_amdgcn_make_buffer_rsrc((void*)p, (short)0, (int)bytes, 0x00020000);
}
template <int AUX> __device__ __forceinline__ void st16(v4u v, rsrc_t r, u32 voff) {
  __builtin_amdgcn_raw_buffer_store_b128(v, r, voff, 0, AUX);
}
__device__ __forceinline__ void dma16(rsrc_t r, u32 voff, u32 soff, u32 lds) {
  asm volatile("s_mov_b32 m0, %0\n\ts_nop 0\n\tbuffer_load_dwordx4 %1, %2, %3 offen lds"
               :: "s"(lds), "v"(voff), "s"(r), "s"(soff) : "memory");
}
template <int LO, int SO> __device__ __forceinline__ void dma16i(rsrc_t r, u32 voff, u32 soff, u32 lds) {
  u32 t;
  asm volatile("s_add_u32 m0, %1, %5\n\ts_add_u32 %0, %4, %6\n\tbuffer_load_dwordx4 %2, %3, %0 offen lds"
               : "=&s"(t) : "s"(lds), "v"(voff), "s"(r), "s"(soff), "i"(LO), "i"(SO) : "memory", "scc");
}
__device__ __forceinline__ void dma16_56(rsrc_t r, u32 voff, u32 soff, u32 lds) {
  asm volatile("s_mov_b32 m0, %0\n\ts_mov_b32 exec_hi, 0xffffff\n\tbuffer_load_dwordx4 %1, %2, %3 offen lds\n\ts_mov_b32 exec_hi, -1"
               :: "s"(lds), "v"(voff), "s"(r), "s"(soff) : "memory");
}
__device__ __forceinline__ void dma4_sc1(rsrc_t r, u32 voff, u32 soff, u32 lds) {
  asm volatile("s_mov_b32 m0, %0\n\ts_nop 0\n\tbuffer_load_dword %1, %2, %3 offen sc1 lds"
               :: "s"(lds), "v"(voff), "s"(r), "s"(soff) : "memory");
}
typedef v2f cpx;
#define CM_A(x, y, ax, bx, by) "v_mul_f32 " x ", " ax ", " bx "\n\tv_mul_f32 " y ", " ax ", " by "\n\t"
#define CM_B(x, y, ay, bx, nb) "v_fmac_f32 " x ", " ay ", " nb "\n\tv_fmac_f32 " y ", " ay ", " bx "\n\t"
__device__ __forceinline__ void cmul4(cpx& r0, cpx& r1, cpx& r2, cpx& r3, cpx a0, cpx a1, cpx a2, cpx a3, cpx b, float nb) {
  float x0, y0, x1, y1, x2, y2, x3, y3;
  asm volatile(CM_A("%0", "%1", "%8", "%16", "%17") CM_A("%2", "%3", "%10", "%16", "%17") CM_A("%4", "%5", "%12", "%16", "%17")
               CM_A("%6", "%7", "%14", "%16", "%17") CM_B("%0", "%1", "%9", "%16", "%18") CM_B("%2", "%3", "%11", "%16", "%18")
               CM_B("%4", "%5", "%13", "%16", "%18") CM_B("%6", "%7", "%15", "%16", "%18")
               : "=&v"(x0), "=&v"(y0), "=&v"(x1), "=&v"(y1), "=&v"(x2), "=&v"(y2), "=&v"(x3), "=&v"(y3)
               : "v"(a0.x), "v"(a0.y), "v"(a1.x), "v"(a1.y), "v"(a2.x), "v"(a2.y), "v"(a3.x), "v"(a3.y), "v"(b.x), "v"(b.y), "v"(nb));
  r0 = (cpx){x0, y0}; r1 = (cpx){x1, y1}; r2 = (cpx){x2, y2}; r3 = (cpx){x3, y3};
}
__device__ __forceinline__ void cmul2(cpx& r0, cpx& r1, cpx a0, cpx b0, float n0, cpx a1, cpx b1, float n1) {
  float x0, y0, x1, y1;
  asm volatile(CM_A("%0", "%1", "%4", "%6", "%7") CM_A("%2", "%3", "%9", "%11", "%12")
               CM_B("%0", "%1", "%5", "%6", "%8") CM_B("%2", "%3", "%10", "%11", "%13")
               : "=&v"(x0), "=&v"(y0), "=&v"(x1), "=&v"(y1)
               : "v"(a0.x), "v"(a0.y), "v"(b0.x), "v"(b0.y), "v"(n0), "v"(a1.x), "v"(a1.y), "v"(b1.x), "v"(b1.y), "v"(n1));
  r0 = (cpx){x0, y0}; r1 = (cpx){x1, y1};
}
__device__ __forceinline__ v2f add2x2s(v2f a, v2f b, v2f c, v2f d) {
  float x, y, z, t;
  asm("v_add_f32 %0, %4, %6\n\tv_add_f32 %1, %5, %7\n\tv_add_f32 %2, %8, %10\n\tv_add_f32 %3, %9, %11\n\t"
      "v_add_f32 %0, %0, %2\n\tv_add_f32 %1, %1, %3"
      : "=&v"(x), "=&v"(y), "=&v"(z), "=&v"(t)
      : "v"(a.x), "v"(a.y), "v"(b.x), "v"(b.y), "v"(c.x), "v"(c.y), "v"(d.x), "v"(d.y));
  return (v2f){x, y};
}
__device__ __forceinline__ u32 mix2(float g, u32 a, v2f b0, v2f b1) {
  float x, y, lo, hi;
  u32 o;
  asm("v_add_f32 %1, %7, %9\n\tv_add_f32 %2, %8, %10\n\tv_lshlrev_b32 %3, 16, %6\n\tv_and_b32 %4, 0xffff0000, %6\n\t"
      "v_sub_f32 %3, %3, %1\n\tv_sub_f32 %4, %4, %2\n\tv_fmac_f32 %1, %5, %3\n\tv_fmac_f32 %2, %5, %4\n\t"
      "s_nop 0\n\tv_cvt_pk_bf16_f32 %0, %1, %2"
      : "=&v"(o), "=&v"(x), "=&v"(y), "=&v"(lo), "=&v"(hi)
      : "v"(g), "v"(a), "v"(b0.x), "v"(b0.y), "v"(b1.x), "v"(b1.y));
  return o;
}
__device__ __forceinline__ v4f add4s(v4f a, v4f b) {
  float x, y, z, t;
  asm("v_add_f32 %0, %4, %8\n\tv_add_f32 %1, %5, %9\n\tv_add_f32 %2, %6, %10\n\tv_add_f32 %3, %7, %11"
      : "=&v"(x), "=&v"(y), "=&v"(z), "=&v"(t)
      : "v"(a.x), "v"(a.y), "v"(a.z), "v"(a.w), "v"(b.x), "v"(b.y), "v"(b.z), "v"(b.w));
  return (v4f){x, y, z, t};
}
constexpr int frow(int r) {
  return r < 7 ? LDS_W + 32768 + r * 288 : r < 13 ? LDS_W + 49152 + 224 + (r - 7) * 288 : LDS_W + 65536 + 1024 + 160 + (r - 13) * 288;
}
constexpr int FM0 = 49152;
constexpr float RND_UP = 1.0027f;
template <int R> __device__ __forceinline__ void rows4(v4u& keep, u32 m0v, cpx a, cpx b, cpx c, cpx d) {
  u32 t0, t1, t2, t3;
  asm volatile("s_mov_b32 m0, %4\n\tv_perm_b32 %0, %6, %5, %21\n\tv_perm_b32 %1, %8, %7, %21\n\tv_perm_b32 %2, %10, %9, %21\n\t"
               "v_perm_b32 %3, %12, %11, %21\n\tds_write_addtid_b32 %0 offset:%17\n\tds_write_addtid_b32 %1 offset:%18\n\t"
               "ds_write_addtid_b32 %2 offset:%19\n\tds_write_addtid_b32 %3 offset:%20"
               : "=&v"(t0), "=&v"(t1), "=&v"(t2), "=&v"(t3)
               : "s"(m0v), "v"(a.x), "v"(a.y), "v"(b.x), "v"(b.y), "v"(c.x), "v"(c.y), "v"(d.x), "v"(d.y),
                 "v"(keep.x), "v"(keep.y), "v"(keep.z), "v"(keep.w),
                 "i"(frow(R) - FM0), "i"(frow(R + 1) - FM0), "i"(frow(R + 2) - FM0), "i"(frow(R + 3) - FM0), "s"(0x07060302u)
               : "memory");
  keep = (v4u){t0, t1, t2, t3};
}
template <int H> __device__ __forceinline__ u32 add_half(u32 base, u32 x) {
  u32 r;
  if constexpr (H == 0) asm volatile("v_add_u32_sdwa %0, %1, %2 dst_sel:DWORD dst_unused:UNUSED_PAD src0_sel:DWORD src1_sel:WORD_0" : "=v"(r) : "s"(base), "v"(x));
  else asm volatile("v_add_u32_sdwa %0, %1, %2 dst_sel:DWORD dst_unused:UNUSED_PAD src0_sel:DWORD src1_sel:WORD_1" : "=v"(r) : "s"(base), "v"(x));
  return r;
}
__device__ __forceinline__ void flag_store(rsrc_t r, u32 soff, u32 tag_v) {
  asm volatile("s_mov_b64 exec, 1\n\tbuffer_store_dword %0, off, %1, %2 sc1\n\ts_mov_b64 exec, -1" :: "v"(tag_v), "s"(r), "s"(soff) : "memory");
}
__device__ __forceinline__ u32 sopaque(u32 x) { asm volatile("" : "+s"(x)); return x; }
__device__ __forceinline__ u32 opaque(u32 x) { asm volatile("" : "+v"(x)); return x; }
__device__ __forceinline__ cpx cpow_l(float a2, float bf, float l) {
  float m = __builtin_amdgcn_exp2f(a2 * l), ph = __builtin_amdgcn_fractf(bf * l);
  return (cpx){m * __builtin_amdgcn_cosf(ph), m * __builtin_amdgcn_sinf(ph)};
}
__device__ __forceinline__ void wait_flags(const gu32* p, u32 tag, u32 first, int code = 0, int nap = 1) {
  if (__all(first == tag)) return;
  u32 v;
#pragma unroll 1
  for (int s = 0; s < SPIN_CAP; ++s) {
    v = __hip_atomic_load(p, RLX_AGENT);
    if (__all(v == tag)) break;
    if (nap == 1) __builtin_amdgcn_s_sleep(1); else __builtin_amdgcn_s_sleep(8);
  }
}

template <int V> struct ic { static constexpr int value = V; };
struct Args {
  const u16* par[8];
  const u16 *u, *wg, *wu, *wd, *gw;
  u16* out;
  char* ws;
};

__global__ __launch_bounds__(512, 2) void gated_mix_kernel(Args) {
  u64 tstart; asm volatile("s_memrealtime %0\n\ts_waitcnt lgkmcnt(0)" : "=s"(tstart) :: "memory");
  const int b = blockIdx.x, xcd = b & 7, bi = b >> 3;
  const int tid = threadIdx.x, l = tid & 63;
  const int w = __builtin_amdgcn_readfirstlane(tid >> 6);
  typedef u64 v4q __attribute__((ext_vector_type(4)));
  typedef u64 v2q __attribute__((ext_vector_type(2)));
  v4q ka0;
  v2q ka1;
  u64 ka2, par_w;
  asm volatile("s_load_dwordx8 %0, %4, 0x40\n\ts_load_dwordx4 %1, %4, 0x60\n\ts_load_dwordx2 %2, %4, 0x70\n\ts_load_dwordx2 %3, %4, %5"
               : "=&s"(ka0), "=&s"(ka1), "=&s"(ka2), "=&s"(par_w) : "s"(__builtin_amdgcn_kernarg_segment_ptr()), "s"(w * 8) : "memory");
  asm volatile("s_cmp_ge_u32 %0, %3\n\ts_cbranch_scc1 1f\n\ts_getpc_b64 s[4:5]\n\ts_mov_b32 m0, %2\n\ts_and_b32 s4, s4, 0xffffff00\n\t"
               "global_load_lds_dwordx4 %1, s[4:5]\n1:"
               :: "s"(bi * 8 + w), "v"((bi * 8 + w) * 1024 + l * 16), "s"(LDS_SCR + w * 4096), "i"(CODE_KB) : "s4", "s5", "scc", "memory");
  const int cb = xcd * 28 + bi;
  const int mt = (xcd & 3) * 4 + (bi & 3), ft = (xcd >> 2) * 7 + (bi >> 2);
  const int rot = ROTV == 0 ? ((bi >> 2) + 2 * (bi & 3) + (xcd & 3)) % 7
                : ROTV == 1 ? ((xcd & 3) + (xcd >> 2) + 3 * (((bi >> 2) + bi) & 1)) % 7
                : ROTV == 2 ? ((xcd & 3) + (xcd >> 2) + 2 * (((bi >> 2) + (bi & 3)) % 3)) % 7
                            : (2 * (xcd & 3) + (xcd >> 2)) % 7;
  const int m2 = 4 * xcd + (bi & 3), nt = bi >> 2;
  const int wt = w & 1, wf = w >> 1;

  const u32 vl16 = opaque(l * 16);
  const u32 vx0 = opaque((l >> 3) * 896 + (((l & 7) ^ (l >> 4)) * 16)), vx1 = opaque((vx0 ^ 64) + 8 * 896);
  const u32 vw = opaque((l >> 4) * (F * 2) + (((l & 15) ^ (4 * (l >> 4))) * 16));
  const u32 sx = sopaque((mt * 128 + 16 * w) * 896 + rot * 128), sw = sopaque(8 * w * (F * 2) + ft * 256 + rot * (64 * F * 2)), lw = sopaque(w * 2048);
  const u32 par_o = sopaque(w < 6 ? cb * 1024 : (cb * 4) & ~15), par_n = sopaque(w < 6 ? D * NS * 2 : D * 2), par_l = sopaque(LDS_PAR + w * 1024);
  asm volatile("s_waitcnt lgkmcnt(0)" : "+s"(ka0), "+s"(ka1), "+s"(ka2), "+s"(par_w) :: "memory");
  struct { const u16 *u, *wg, *wu, *wd, *gw; u16* out; char* ws; } g = {
      (const u16*)ka0.x, (const u16*)ka0.y, (const u16*)ka0.z, (const u16*)ka0.w, (const u16*)ka1.x, (u16*)ka1.y, (char*)ka2};
  char* ws = g.ws;
  const rsrc_t rws = mk_rsrc(ws, WS_END);
  const rsrc_t rx = mk_rsrc(g.u, L * D * 2);
  STAMP(0);

  if (bi >= 28) {
    const int hb = b - NMAIN, t0 = 64 * hb;
    constexpr u32 HX = 0, HG = 64 * 896;
    const rsrc_t rgw = mk_rsrc(g.gw, D * 2);
    if (HDELAY) __builtin_amdgcn_s_sleep(HDELAY);
#pragma unroll
    for (int j = 0; j < 8; ++j) dma16_56(rx, vl16 ^ (((j & 2) + 4 * (w & 1)) * 16), (t0 + 8 * w + j) * 896, HX + (8 * w + j) * 896);
    if (w == 0) { dma4_sc1(rws, 0, WS_EPOCH, HG + 1024); dma16(rgw, vl16, 0, HG); }
    const int G = l >> 4, q = (l & 15) >> 2, p = l & 3;
    const u32 a_u = opaque(HX + (32 * (w & 1) + 8 * G + q) * 896 + (((w & 6) + (p >> 1)) ^ ((q & 2) + 4 * (G & 1))) * 16 + 8 * (p & 1));
    WAITV(0); BAR();
    STAMP(1);
    const u32 tag = __builtin_amdgcn_readfirstlane(lds_ld<u32>(HG + 1024)) + TAG_STEP;
    {
      const u32 o = WS_UT + (hb * 56 + w) * 1024 + (l & 15) * 64 + G * 16;
#pragma unroll
      for (int j = 0; j < 7; ++j) {
        v2u lo = lds_tr(a_u + 128 * j), hi = lds_tr(a_u + 128 * j + 4 * 896);
        st16<16>((v4u){lo.x, lo.y, hi.x, hi.y}, rws, o + j * 8192);
      }
    }
    {
      const int r = 8 * w + (l >> 3);
      const u32 xa = opaque(HX + r * 896 + ((l & 7) ^ (((l >> 3) & 2) + 4 * (w & 1))) * 16), ga = opaque(HG + (l & 7) * 16);
      float s0 = 0.f, s1 = 0.f;
#pragma unroll
      for (int j = 0; j < 7; ++j) {
        const v4u xv = lds_ld<v4u>(xa + 128 * j), gv = lds_ld<v4u>(ga + 128 * j);
#pragma unroll
        for (int e = 0; e < 4; e += 2) {
          s0 = dot2bf(xv[e], gv[e], s0);
          s1 = dot2bf(xv[e + 1], gv[e + 1], s1);
        }
      }
      float s = s0 + s1;
      s += __shfl_xor(s, 1); s += __shfl_xor(s, 2); s += __shfl_xor(s, 4);
      s = rbf(s);
      float gt = rbf(__builtin_amdgcn_rcpf(1.f + __builtin_amdgcn_exp2f(-1.44269504f * s)));
      if ((l & 7) < 2) __hip_atomic_store((gu32*)(ws + WS_GATE) + hb * 128 + (r & 15) * 8 + (r >> 4) + 4 * (l & 1), __float_as_uint(gt), RLX_AGENT);
    }
    WAITV(0); BAR();
    if (tid == 0) __hip_atomic_store((gu32*)(ws + WS_FLAG_U) + hb, tag, RLX_AGENT);
    STAMP(2);
    if (WPD) __builtin_amdgcn_s_sleep(WPD);
#pragma unroll 1
    for (int i = 0; i < 21 * WPF; ++i) {
      const int id = (hb >> 3) * 8 + w + 32 * (i % 21), unit = id >> 2, n7 = unit / 24, r = unit - 24 * n7, k8 = r / 3;
      int k = r - 3 * k8 + (n7 < 4 ? 4 : 1) - (i >= 21 ? 3 : 0);
      k = k < 0 ? k + 7 : k;
      dma16(rws, vl16, __builtin_amdgcn_readfirstlane(WS_WT + ((n7 * 56 + k8 * 7 + k) * 4 + (id & 3)) * 1024), w * 1024);
    }
    WAITV(0);
    if (hb == NHELP - 1) {
      if (w < 4) wait_flags((const gu32*)(ws + (tid < NMAIN ? WS_FLAG_W : WS_FLAG_U - NMAIN * 4)) + tid, tag, ~tag, 1, 8);
      BAR();
      if (tid == 0) __hip_atomic_store((gu32*)(ws + WS_EPOCH), tag, RLX_AGENT);
    }
    return;
  }

  {
    const rsrc_t rp = mk_rsrc((const void*)par_w, par_n);
    dma16(rp, vl16, par_o, par_l);
    if (w == 0) dma4_sc1(rws, 0, WS_EPOCH, LDS_PAR + 8192);
  }
  const rsrc_t rwg = mk_rsrc(g.wg, D * F * 2), rwu = mk_rsrc(g.wu, D * F * 2);
  auto piece = [&](auto S, auto I) {
    constexpr int s = decltype(S)::value, i = decltype(I)::value;
    const bool wrap = s + rot >= 7;
    if constexpr (i < 2) dma16i<LDS_X + (s & 1) * 16384 + i * 1024, s * 128>(rx, i ? vx1 : vx0, wrap ? sx - 7 * 128 : sx, lw);
    else dma16i<LDS_W + (s % 3) * 32768 + (i >= 4) * 16384 + (i & 1) * 1024, (64 * s + 4 * (i & 1)) * F * 2>(i < 4 ? rwg : rwu, vw, wrap ? sw - 7 * 64 * F * 2 : sw, lw);
  };
#define PIECE(s, i) piece(ic<(s)>{}, ic<(i)>{})
  const int ct = 7 * (cb & 3) + (w < 7 ? w : 6);
  const int G = l >> 4, q = (l & 15) >> 2, p = l & 3;
  const u32 srd = opaque(lw + ((l & 15) < 7 ? frow(0) : (l & 15) < 13 ? frow(7) - 7 * 288 : frow(13) - 13 * 288) + (l & 15) * 288 + (l >> 4) * 16);
  const u32 po = opaque(LDS_PAR + (w >> 2) * 512 + (w & 3) * 128 + l * 2);
  const u32 pld = opaque(LDS_PAR + 6144 + ((cb * 4) & 15) + 2 * (w >> 2)), pdk = opaque(LDS_PAR + 7168 + ((cb * 4) & 15));
  const u32 wst_v = opaque((l >> 1) * 896 + (l & 1) * 16), tab_v = opaque(tid * 32);
  const u32 wst_a = opaque(LDS_WST + lw + (16 * (G >> 1) + 4 * (G & 1) + q) * 32 + p * 8);
  const u32 wt_o = opaque(WS_WT + (((ct >> 2) * 56 + (cb >> 2)) * 4 + (ct & 3)) * 1024 + vl16);
  const u32 kpw = opaque(LDS_SCR + w * 4096 + vl16), kpr = opaque(LDS_SCR + (w & 4) * 4096 + (w & 3) * 1024 + vl16);
  const u32 wfo = opaque(WS_FLAG_W + 16 * (l < 56 ? l : 0));
  const u32 ufo = opaque(WS_FLAG_U + (l & 31) * 4);
  PIECE(0, 0); PIECE(0, 1); PIECE(0, 2); PIECE(0, 3);
  WAITV(4); BAR();
  PRIO(3);
  STAMP(1);

  auto frags = [&](v8bf (&f)[4]) {
#pragma unroll
    for (int k = 0; k < 4; ++k) f[k] = as_frag(lds_ld<v4u>(srd + k * 64));
  };
  u32 tag, dsk2;
  v4u tb0, tb1;
  {
    cpx c0, w1, w2, w256, w512;
    const u32 fm0 = lw + FM0;
    v8bf J[2][4], Hf[4], Hg[4];
    v4u keep = {0, 0, 0, 0};
    const float ld = bf_lo(lds_ld<u16>(pld));
    const u32 q0 = lds_ld<u16>(po), q1 = lds_ld<u16>(po + 1024), q2 = lds_ld<u16>(po + 2048), q3 = lds_ld<u16>(po + 3072);
    const u32 q4 = lds_ld<u16>(po + 4096), q5 = lds_ld<u16>(po + 5120);
    const u32 ep = lds_ld<u32>(pdk + 1024), dk = lds_ld<u32>(pdk);
    PIECE(0, 4); PIECE(0, 5);
    const float dt = rbf(__builtin_amdgcn_exp2f(ld * 1.44269504f));
    const float a2 = (dt * bf_lo(q0)) * 1.44269504f, bf = (dt * bf_lo(q1)) * 0.15915494f;
    const cpx w8 = cpow_l(a2, bf, 8.f);
    w1 = cpow_l(a2, -bf, 1.f); w256 = cpow_l(a2, -bf, 256.f);
    const cpx cc = {bf_lo(q4), -bf_lo(q5)}, bb = {(dt * RND_UP) * bf_lo(q2), -(dt * RND_UP) * bf_lo(q3)};
    tag = __builtin_amdgcn_readfirstlane(ep) + TAG_STEP; dsk2 = __builtin_amdgcn_readfirstlane(dk);
    cpx w16, w24, w32, j1, j2, j3;
    cmul2(w16, c0, w8, w8, -w8.y, cc, bb, -bb.y);
    cmul2(w24, w32, w16, w8, -w8.y, w16, w16, -w16.y);
    cpx j0 = {RND_UP, 0.f};
    j1 = w8 * RND_UP; j2 = w16 * RND_UP; j3 = w24 * RND_UP;
    const float n32 = -w32.y, n1 = -w1.y, n256 = -w256.y;
#define JSTEP(k) rows4<((k) & 3) * 4>(keep, fm0, j0, j1, j2, j3)
#define JNEXT() cmul4(j0, j1, j2, j3, j0, j1, j2, j3, w32, n32)
    JSTEP(0); JNEXT();
    JSTEP(1); JNEXT();
    PRIO(2);
    dma16(mk_rsrc(g.wd, F * D * 2), wst_v, 32 * (cb >> 2) * 896 + ct * 32, LDS_WST + lw);
    JSTEP(2); JNEXT();
    JSTEP(3); frags(J[0]); JNEXT();
    JSTEP(4); JNEXT();
    JSTEP(5); JNEXT();
    JSTEP(6); JNEXT();
    cmul2(w2, w512, w1, w1, n1, w256, w256, n256);
    PRIO(1);
    asm volatile("global_load_dwordx4 %0, %2, %3\n\tglobal_load_dwordx4 %1, %2, %3 offset:16"
                 : "=&v"(tb0), "=&v"(tb1) : "v"(tab_v), "s"(&thread_tab.v[0][0]) : "memory");
    JSTEP(7); frags(J[1]);
    cpx c1, c2, c3, c4, c5, c6, c7, w4, d0, d2, d4, d6, e0, e4, f0, f4, g0, g1, g2, g3, g4;
    const float n2 = -w2.y, n512 = -w512.y;
    cmul2(c1, c2, c0, w1, n1, c0, w2, n2);
    cmul2(c3, w4, c2, w1, n1, w2, w2, n2);
    cmul4(c4, c5, c6, c7, c0, c1, c2, c3, w4, -w4.y);
    rows4<0>(keep, fm0, c0, c1, c2, c3);
    cmul4(d0, d2, d4, d6, c0, c2, c4, c6, w256, n256);
    rows4<4>(keep, fm0, c4, c5, c6, c7);
    cmul4(e0, e4, f0, f4, c0, c4, d0, d4, w512, n512);
    rows4<8>(keep, fm0, d0, d2, d4, d6);
    cmul2(g0, g1, e0, w512, n512, f0, w512, n512);
    PRIO(0);
    rows4<12>(keep, fm0, e0, e4, f0, f4);
    frags(Hf);
    cmul2(g2, g3, g0, w512, n512, g1, w512, n512);
    g4 = (cpx){g2.x * w512.x + g2.y * n512, g2.x * w512.y + g2.y * w512.x};
    rows4<0>(keep, fm0, g0, g1, g2, g3);
    asm volatile("ds_write_addtid_b32 %0 offset:%1" :: "v"(__builtin_amdgcn_perm(__float_as_uint(g4.y), __float_as_uint(g4.x), 0x07060302u)),
                 "i"(frow(4) - FM0) : "memory");
    frags(Hg);
    SB0();
    v4f kc[4];
    auto kmm = [&](int k) {
#pragma unroll
      for (int t = 0; t < 4; ++t)
        kc[t] = __builtin_amdgcn_mfma_f32_16x16x32_bf16((t >> 1) ? Hg[k] : Hf[k], J[t & 1][k], k == 0 ? (v4f){0.f, 0.f, 0.f, 0.f} : kc[t], 0, 0, 0);
      SB0();
    };
    WAITL0();
    kmm(0); PIECE(1, 0); kmm(1); PIECE(1, 1); kmm(2); PIECE(1, 2); kmm(3); PIECE(1, 3);
#pragma unroll
    for (int t = 0; t < 4; ++t) lds_st<v4f>(kpw + t * 1024, kc[t]);
    SB0();
    PIECE(1, 4); PIECE(1, 5);
  }
  STAMP(13);
  asm volatile("s_waitcnt vmcnt(6)" : "+v"(tb0), "+v"(tb1) :: "memory");
  u32 xa[4], wa[3];
  xa[0] = opaque(tb1.w & 0xffff);
#pragma unroll
  for (int s = 1; s < 4; ++s) xa[s] = opaque(xa[0] ^ (32 * s));
  wa[0] = opaque(tb1.w >> 16);
  wa[1] = opaque(wa[0] + 32768); wa[2] = opaque(wa[0] + 65536);
  const u32 tag_v = opaque(tag);
  STAMP(2);

  v16f accg[2] = {}, accu[2] = {};
  v4u ut4, gate4;
  v4u wfr[28];
  const int kr = w, krot = WROT == 1 ? (4 * (nt >> 2) + 2 * (bi & 3)) % 7 : WROT == 2 ? (4 * (nt >> 2) + 3 * (bi & 1)) % 7 : nt & 4;
  u32 koff[7];
#pragma unroll
  for (int i = 0; i < 7; ++i) koff[i] = (kr * 7 + (i + krot >= 7 ? i + krot - 7 : i + krot)) * 1024;
  const u32 wd_so = WS_WT + nt * 56 * 4096;
  auto ldw = [&](int i) { wfr[i] = __builtin_amdgcn_raw_buffer_load_b128(rws, vl16 + (i & 3) * 1024, wd_so + 4 * koff[i >> 2], 16); };
  u32 hfo, hadr, hadr2, ka, ka8, ub, yb, yo, uo, xe[2][5], xkb, xz, uia;
  auto img_writes = [&]() {
#pragma unroll
    for (int o = 0; o < 2; ++o) {
      lds_st<v4u>(xkb + (3 - o) * KCOPY, (v4u){xe[o][0], xe[o][1], xe[o][2], xe[o][3]});
      lds_st<v4u>(xkb + (1 - o) * KCOPY, (v4u){xe[o][1], xe[o][2], xe[o][3], xe[o][4]});
    }
    lds_st<v4u>(xz, (v4u){0, 0, 0, 0});
    lds_st<v4u>(uia, ut4);
  };
  v2f nl2e = {-1.44269504f, -1.44269504f};
  v4u hst[4];
  auto silu8 = [&](int gI) {
    const int j = gI >> 1, r = 8 * (gI & 1);
    v2f gv[4], pv[4], ev[4];
#pragma unroll
    for (int e = 0; e < 4; ++e) { gv[e] = (v2f){accg[j][r + 2 * e], accg[j][r + 2 * e + 1]}; ev[e] = gv[e] * nl2e; }
    SB0();
#pragma unroll
    for (int e = 0; e < 4; ++e) ev[e] = (v2f){__builtin_amdgcn_exp2f(ev[e].x), __builtin_amdgcn_exp2f(ev[e].y)};
    SB0();
#pragma unroll
    for (int e = 0; e < 4; ++e) { ev[e] = ev[e] + 1.f; pv[e] = gv[e] * (v2f){accu[j][r + 2 * e], accu[j][r + 2 * e + 1]}; }
    SB0();
#pragma unroll
    for (int e = 0; e < 4; ++e) ev[e] = (v2f){__builtin_amdgcn_rcpf(ev[e].x), __builtin_amdgcn_rcpf(ev[e].y)};
    SB0();
#pragma unroll
    for (int e = 0; e < 4; ++e) pv[e] = pv[e] * ev[e];
    SB0();
#pragma unroll
    for (int e = 0; e < 4; ++e) hst[gI][e] = pk_bf(pv[e].x, pv[e].y);
    st16<16>(hst[gI], rws, (j ? hadr2 : hadr) + (r >> 3) * 512);
    if (gI > 0) asm volatile("" :: "v"(hst[gI - 1]));
    SB0();
  };
  {
    v8bf fg[2], fu[2], fx0[2], fx1[2];
    auto rd = [&](int buf, int st, int ks) {
      const u32 wb = wa[st % 3] + ks * 4096, xb = xa[ks] + (st & 1) * 16384;
      fg[buf] = as_frag(lds_tr(wb), lds_tr(wb + 1024));
      fx0[buf] = as_frag(lds_ld<v4u>(xb));
      fu[buf] = as_frag(lds_tr(wb + 16384), lds_tr(wb + 16384 + 1024));
      fx1[buf] = as_frag(lds_ld<v4u>(xb + 4096));
    };
    v4f ks4[4];
    u32 kp01, kp23;
    auto kgen = [&](int n) {
      if (n == 22) {
#pragma unroll
        for (int k = 0; k < 4; ++k) ks4[k] = lds_ld<v4f>(kpr + k * 4096);
      }
      if (n == 24) {
        const v4f s = add4s(add4s(ks4[0], ks4[1]), add4s(ks4[2], ks4[3]));
        const bool keep = !(w & 2) || l < 32;
        kp01 = opaque(keep ? pk_bf(s[0], s[1]) : 0); kp23 = opaque(keep ? pk_bf(s[2], s[3]) : 0);
      }
      if (n == 25) lds_st<v2u>(LDS_TAB + w * 512 + (vl16 >> 1), (v2u){kp01, kp23});
    };
    u32 xv[11], uta;
    auto expand = [&](int step) {
      if (step == 0) {
        xv[0] = lds_ld<u16>(add_half<0>(LDS_TAB, tb0.x)); xv[1] = lds_ld<u16>(add_half<1>(LDS_TAB, tb0.x));
        xv[2] = lds_ld<u16>(add_half<0>(LDS_TAB, tb0.y)); xv[3] = lds_ld<u16>(add_half<1>(LDS_TAB, tb0.y));
      } else if (step == 1) {
        xv[4] = lds_ld<u16>(add_half<0>(LDS_TAB, tb0.z)); xv[5] = lds_ld<u16>(add_half<1>(LDS_TAB, tb0.z));
        xv[6] = lds_ld<u16>(add_half<0>(LDS_TAB, tb0.w)); xv[7] = lds_ld<u16>(add_half<1>(LDS_TAB, tb0.w));
      } else if (step == 2) {
        xv[8] = lds_ld<u16>(add_half<0>(LDS_TAB, tb1.x)); xv[9] = lds_ld<u16>(add_half<1>(LDS_TAB, tb1.x));
        xv[10] = lds_ld<u16>(add_half<0>(LDS_TAB, tb1.y));
      } else if (step < 16) {
        const int o = step - 14;
#pragma unroll
        for (int j = 0; j < 5; ++j) xe[o][j] = opaque(xv[2 * j + o] | (xv[2 * j + o + 1] << 16));
      }
    };
    v4u wflag;
    u32 uflag;
    auto side = [&](auto S, auto KS, auto H) {
      constexpr int s = decltype(S)::value, ks = decltype(KS)::value, h = decltype(H)::value;
      constexpr int st = 4 * s + ks;
      if constexpr (h == 0 || h == 2) {
        constexpr int i = h >> 1;
        if constexpr (ks == 3) { if constexpr (s < 5) PIECE(s + 2, i); }
        else if constexpr (s == 0) { if constexpr (ks < 2) PIECE(2, 2 + 2 * ks + i); }
        else if constexpr (ks < 2 && s + 2 < 7) PIECE(s + 2, 2 + 2 * ks + i);
        if constexpr (st >= 20) { if constexpr (LW_G == 16) ldw(2 * (st - 20) + i); else if constexpr (LW_G == 12) { if constexpr (i == 0 || (st & 1)) ldw(3 * ((st - 20) >> 1) + (st & 1) + i); } else if constexpr (i == 0) ldw(st - 20); }
      } else {
        constexpr int v = 2 * st + (h >> 1);
        if constexpr (v == 1) kgen(22);
        if constexpr (v == 4) kgen(24);
        if constexpr (v == 28) hfo = opaque(WS_FLAG_H + (m2 * 56 + 7 * kr + (l < 7 ? l : 0)) * (HFS * 4));
        if constexpr (v == 43) kgen(25);
        if constexpr (v >= 46 && v < 49) expand(v - 46);
        if constexpr (v == 52 || v == 53) expand(v - 38);
        if constexpr (v == 14) {
          xkb = opaque(((w >> 2) ? LDS_KIMG1 : LDS_KIMG0) + (tid & 255) * 16);
          xz = opaque((tb1.y >> 16) << 4);
          uia = opaque((tb1.z & 0xffff) << 4);
        }
        if constexpr (v == 16) uta = opaque((tid >> 4) * (56 * 1024) + ((tid >> 3) & 1) * 1024 + (tid & 7) * 16);
        if constexpr (v == 18) {
          hadr = opaque(WS_H + ((mt * 8 + 4 * wt + ((l & 31) >> 4)) * 56 + ft * 4 + wf) * 1024 + (l & 15) * 16 + (l >> 5) * 256);
          hadr2 = opaque(hadr + 2 * 56 * 1024);
          asm volatile("" : "+v"(nl2e));
        }
        if constexpr (v == 20) {
          ka = opaque(((w >> 2) ? LDS_KIMG1 : LDS_KIMG0) + (l & 3) * KCOPY + 2 * (2028 + 8 * G - 4 * q) - 512 * 7 - 128 * (w & 3) - 64);
          ka8 = opaque(ka + 8);
        }
        if constexpr (v == 22) ub = opaque(LDS_UIMG + (w >> 2) * UCH + 2 * (240 + 16 * (l & 15) + 8 * G) - 128 * (w & 3) - 64);
        if constexpr (v == 24) yb = opaque(LDS_Y + ((w & 3) * 2 + (w >> 2)) * 8192 + (16 * (l & 15) + 4 * (G ^ ((l >> 1) & 3))) * 4);
        if constexpr (v == 26) { yo = opaque(LDS_Y + (tid >> 2) * 64 + (((tid & 3) ^ ((tid >> 3) & 3)) * 16)); uo = opaque(LDS_UIMG + 512 + tid * 8); }
      }
    };
    auto mm = [&](int buf, auto S, auto KS) {
      accg[0] = __builtin_amdgcn_mfma_f32_32x32x16_bf16(fg[buf], fx0[buf], accg[0], 0, 0, 0);
      SB0(); side(S, KS, ic<0>{}); SB0();
      accu[0] = __builtin_amdgcn_mfma_f32_32x32x16_bf16(fu[buf], fx0[buf], accu[0], 0, 0, 0);
      SB0(); side(S, KS, ic<1>{}); SB0();
      accg[1] = __builtin_amdgcn_mfma_f32_32x32x16_bf16(fg[buf], fx1[buf], accg[1], 0, 0, 0);
      SB0(); side(S, KS, ic<2>{}); SB0();
      accu[1] = __builtin_amdgcn_mfma_f32_32x32x16_bf16(fu[buf], fx1[buf], accu[1], 0, 0, 0);
      SB0(); side(S, KS, ic<3>{});
      asm volatile("" :: "v"(fg[buf]), "v"(fu[buf]), "v"(fx0[buf]), "v"(fx1[buf]));
      SB0();
    };
    auto stage = [&](auto S) {
      constexpr int s = decltype(S)::value;
      PRIO(2); rd(1, s, 1); SB0(); mm(0, S, ic<0>{});
      PRIO(1); rd(0, s, 2); SB0(); mm(1, S, ic<1>{});
      PRIO(0); rd(1, s, 3); SB0(); mm(0, S, ic<2>{});
      PRIO(3);
      if constexpr (s < 6) {
        WAITL0();
        if constexpr (s == 0) WAITV(5);
        else if constexpr (s < 4) WAITV(4);
        else if constexpr (s == 4) {
          asm volatile("s_waitcnt vmcnt(4)" : "+v"(wflag) :: "memory");
          if (w == 0 && !__all(((wflag.x ^ tag_v) | (wflag.y ^ tag_v) | (wflag.z ^ tag_v) | (wflag.w ^ tag_v)) == 0)) {
#pragma unroll 1
            for (int sp = 0; sp < SPIN_CAP; ++sp) {
              asm volatile("" ::: "memory");
              const v4u f = __builtin_amdgcn_raw_buffer_load_b128(rws, wfo, 0, 16);
              if (__all(f.x == tag && f.y == tag && f.z == tag && f.w == tag)) break;
            }
          }
        } else {
          asm volatile("s_waitcnt vmcnt(%1)" : "+v"(uflag) : "i"(LW_G == 16 ? 6 : LW_G == 12 ? 4 : 3) : "memory");
          if (w == 0) wait_flags((const gu32*)(ws + ufo), tag, uflag, 2);
        }
        BAR();
        rd(0, s + 1, 0);
        if constexpr (s == 1) { if (w == 0) flag_store(rws, WS_FLAG_W + cb * 4, tag_v); }
        if constexpr (s == 3)
          asm volatile("s_cmp_lg_u32 %3, 0\n\ts_cbranch_scc1 1f\n\tbuffer_load_dwordx4 %0, %1, %2, 0 offen sc1\n1:"
                       : "=v"(wflag) : "v"(wfo), "s"(rws), "s"(w) : "memory", "scc");
        if constexpr (s == 4)
          asm volatile("s_cmp_lg_u32 %3, 0\n\ts_cbranch_scc1 1f\n\tbuffer_load_dword %0, %1, %2, 0 offen sc1\n1:"
                       : "=v"(uflag) : "v"(ufo), "s"(rws), "s"(w) : "memory", "scc");
        if constexpr (s >= 4) __builtin_amdgcn_fence(__ATOMIC_ACQUIRE, "wavefront");
        if constexpr (s == 5) {
          ut4 = __builtin_amdgcn_raw_buffer_load_b128(rws, uta, WS_UT + (cb >> 3) * 2048 + (cb & 7) * 128, 16);
          gate4 = __builtin_amdgcn_raw_buffer_load_b128(rws, (tid >> 5) * 32, WS_GATE + m2 * 512 + (nt & 3) * 4, 16);
        }
        STAMP(s < 5 ? 22 + s : 20);
        if constexpr (s == 5) WSC(0);
        SB0();
      }
      mm(1, S, ic<3>{});
    };
    {
      const v2u lo = lds_tr(wst_a), hi = lds_tr(wst_a + 256);
      st16<16>((v4u){lo.x, lo.y, hi.x, hi.y}, rws, wt_o);
    }
    WAITL0(); BAR();
    STAMP(3);
    rd(0, 0, 0);
    SB0();
    stage(ic<0>{}); stage(ic<1>{}); stage(ic<2>{}); stage(ic<3>{}); stage(ic<4>{}); stage(ic<5>{}); stage(ic<6>{});
  }
  STAMP(4);
  WSC(1);
  u32 hflag;
  v4u hf[28];
  u32 hso[4];
#pragma unroll
  for (int tt = 0; tt < 4; ++tt) hso[tt] = WS_H + (m2 * 4 + ((tt + nt) & 3)) * 56 * 1024;
  auto ldh = [&](int n) {
    const int i = n % 7;
    hf[n] = __builtin_amdgcn_raw_buffer_load_b128(rws, vl16, hso[n / 7] + koff[i], 16);
    SB0();
  };
  constexpr int AHEAD = AHD, LW_C = LW_G + LW_E * (LW_L ? 4 : 3);
  static_assert(LW_C >= 16);
  {
    v4f acc[8];
    v8bf A[8][2], B[8][2];
    auto lda_ = [&](int pp) {
#pragma unroll
      for (int qq = 0; qq < 2; ++qq)
        A[pp][qq] = as_frag(lds_ld<v2u>(ka + 64 - 64 * qq + 512 * (7 - pp)), lds_ld<v2u>(ka8 + 64 - 64 * qq + 512 * (7 - pp)));
    };
#pragma unroll
    for (int gI = 0; gI < 4; ++gI) {
      silu8(gI);
#pragma unroll
      for (int k = 0; k < LW_E; ++k) if (gI < 3 || LW_L) ldw(LW_G + LW_E * gI + k);
      if (gI == 3) img_writes();
      SB0();
    }
    WAITL0(); BAR();
    lda_(0);
#pragma unroll
    for (int e = 7; e >= 0; --e) B[e][0] = as_frag(lds_ld<v4u>(ub + 64 + 512 * e));
#pragma unroll
    for (int e = 7; e >= 0; --e) B[e][1] = as_frag(lds_ld<v4u>(ub + 512 * e));
    lda_(1);
    asm volatile("" :: "v"(hst[3]));
    asm volatile("s_waitcnt vmcnt(%0)" :: "i"(LW_L ? LW_E : 0) : "memory");
    flag_store(rws, WS_FLAG_H + ((2 * mt + wt) * 56 + 4 * ft + wf) * (HFS * 4), tag_v);
    WST(1);
    SB0();
    PRIO(2);
    asm volatile("" ::: "memory");
    STAMP(5);
    WST(0);
    WSC(2);
#pragma unroll
    for (int pp = 0; pp < 8; ++pp) {
#pragma unroll
      for (int qq = 0; qq < 2; ++qq) {
#pragma unroll
        for (int c = 7; c >= pp; --c) {
          acc[c] = __builtin_amdgcn_mfma_f32_16x16x32_bf16(A[pp][qq], B[c - pp][qq], (qq == 0 && pp == 0) ? (v4f){0.f, 0.f, 0.f, 0.f} : acc[c], 0, 0, 0);
          if (c == 5 && pp < 3 && LW_C + 4 * pp + 2 * qq < 28) { SB0(); ldw(LW_C + 4 * pp + 2 * qq); SB0(); }
        }
        SB0();
        if (qq == 0) { if (pp < 6) lda_(pp + 2); if (pp > 0) lds_st<v4f>(yb + (pp - 1) * 1024, acc[pp - 1]); }
        if (pp < 3 && LW_C + 1 + 4 * pp + 2 * qq < 28) ldw(LW_C + 1 + 4 * pp + 2 * qq);
        const int hs = 2 * pp + qq;
        if (hs == HL_P - HL_D) hflag = __builtin_amdgcn_raw_buffer_load_b32(rws, hfo, 0, 16);
        if (hs == HL_P) {
          WSC(5);
          STAMP(7);
          WST(2);
          wait_flags((const gu32*)(ws + hfo), tag, hflag, 3);
          __builtin_amdgcn_fence(__ATOMIC_ACQUIRE, "wavefront");
          STAMP(8);
          WST(3);
        }
        if (hs >= HL_P && 2 * (hs - HL_P) < 8) { ldh(2 * (hs - HL_P)); ldh(2 * (hs - HL_P) + 1); }
        if (qq == 1 && pp == 1) PRIO(1);
        if (qq == 1 && pp == 3) PRIO(0);
        SB0();
      }
    }
    lds_st<v4f>(yb + 7 * 1024, acc[7]);
  }
  WSC(6);

  {
    v4u a_out;
    v4f yv[8], ys[2];
    v2u uv[2];
    auto a_step = [&](int k) {
      if (k < 2) {
        uv[k] = lds_ld<v2u>(uo + k * UCH);
#pragma unroll
        for (int z = 0; z < 4; ++z) yv[4 * k + z] = lds_ld<v4f>(yo + k * 8192 + z * 16384);
      } else if (k < 4) {
        ys[k - 2] = add4s(add4s(yv[4 * k - 8], yv[4 * k - 7]), add4s(yv[4 * k - 6], yv[4 * k - 5]));
      } else {
        const int e = k - 4;
        const float k0 = bf_lo(dsk2), k1 = bf_hi(dsk2);
        a_out[2 * e] = opaque(pk_bf(__builtin_fmaf(bf_lo(uv[0][e]), k0, ys[0][2 * e]), __builtin_fmaf(bf_lo(uv[1][e]), k1, ys[1][2 * e])));
        a_out[2 * e + 1] = opaque(pk_bf(__builtin_fmaf(bf_hi(uv[0][e]), k0, ys[0][2 * e + 1]), __builtin_fmaf(bf_hi(uv[1][e]), k1, ys[1][2 * e + 1])));
      }
    };
    auto sta = [&]() {
      const u32 ao = vl16 + WS_A2 + cb * 16384 + w * 2048;
      st16<16>((v4u){a_out.x, tag, a_out.y, tag}, rws, ao);
      st16<16>((v4u){a_out.z, tag, a_out.w, tag}, rws, ao + 1024);
      SB0();
    };
#pragma unroll
    for (int n = 8; n < AHEAD - 1; ++n) ldh(n);
    STAMP(27);
    WST(4);
    WAITL0(); BAR();
    a_step(0); a_step(1);
    SB0();
    STAMP(12);
    WSC(7);
    v4u a4[2];
    u32 a_vo, pw, prd, asw, asr, oad;
    const u32 a_so = WS_A2 + 32 * nt * 16384 + (m2 >> 2) * 2048 + (m2 & 3) * 256;
    auto lda = [&]() {
      a4[0] = __builtin_amdgcn_raw_buffer_load_b128(rws, a_vo, a_so, 16);
      a4[1] = __builtin_amdgcn_raw_buffer_load_b128(rws, a_vo + 1024, a_so, 16);
    };
    auto check_a = [&]() {
          if (!__all(((a4[0].y ^ tag) | (a4[0].w ^ tag) | (a4[1].y ^ tag) | (a4[1].w ^ tag)) == 0)) {
#pragma unroll 1
            for (int sp = 0; sp < SPIN_CAP; ++sp) {
              asm volatile("" ::: "memory");
              lda();
              if (__all(a4[0].y == tag && a4[0].w == tag && a4[1].y == tag && a4[1].w == tag)) break;
            }
          }
          lds_st<v2u>(asw, (v2u){a4[0].x, a4[0].z}); lds_st<v2u>(asw + 8, (v2u){a4[1].x, a4[1].z});
        };
    v4f acc[2][4];
    v2f pr[8], bs[4][2];
    u32 av[4], oso[4], prd2;
    const rsrc_t ro = mk_rsrc(g.out, L * D * 2);
    auto pread = [&](int tt) {
#pragma unroll
      for (int k = 0; k < 8; ++k) pr[k] = lds_ld<v2f>((tt == 2 ? prd2 : prd + (tt == 1) * PBUF) + k * PWAVE);
    };
    auto psum = [&](int tt, int h) { bs[tt][h] = add2x2s(pr[4 * h], pr[4 * h + 1], pr[4 * h + 2], pr[4 * h + 3]); };
    auto fetch_a = [&]() {
#pragma unroll
      for (int e = 0; e < 4; ++e) av[e] = lds_ld<u32>(asr + e * 64);
    };
    auto mix = [&](int tt) {
      __builtin_amdgcn_raw_buffer_store_b32(mix2(__uint_as_float(gate4[tt]), av[tt], bs[tt][0], bs[tt][1]), ro, oad, oso[tt], 16);
    };
#pragma unroll
    for (int tt = 0; tt < 3; ++tt) {
#pragma unroll
      for (int i = 0; i < 7; ++i) {
        const int n = 7 * tt + i;
        if (i == 0) PRIO(3);
        if (i == 2) PRIO(2);
        if (i == 4) PRIO(1);
        if (i == 6) PRIO(0);
        if (n + AHEAD - 1 < 28) ldh(n + AHEAD - 1);
          if (n == 4) sta();
        if (n == LDA_N) lda();
        SB0();
#pragma unroll
        for (int c = 0; c < 4; ++c)
          acc[tt & 1][c] = __builtin_amdgcn_mfma_f32_16x16x32_bf16(as_frag(wfr[4 * i + c]), as_frag(hf[n]),
                                                                   i == 0 ? (v4f){0.f, 0.f, 0.f, 0.f} : acc[tt & 1][c], 0, 0, 0);
        SB0();
        if (n == 0) { STAMP(29); WST(5); }
        if (n == 5) pw = opaque(LDS_P + w * PWAVE + (l & 15) * PROW + G * 16);
        if (n == 6) { prd = opaque(LDS_P + (tid >> 5) * PROW + (tid & 31) * 8); prd2 = opaque(prd - LDS_P); }
        if (n == 8) a_vo = opaque((tid >> 4) * 16384 + (tid & 15) * 16);
        if (n == 9) asw = opaque(LDS_AST + (tid >> 4) * 272 + ((tid - 4 * nt) & 15) * 16);
        if (n == 10) asr = opaque(LDS_AST + (tid & 31) * 272 + (tid >> 5) * 4);
        if (n == 11) oad = opaque((tid >> 5) * (D * 2) + (tid & 31) * 4);
        if (n == 12) {
#pragma unroll
          for (int k = 0; k < 4; ++k) {
            oso[k] = (64 * m2 + 16 * ((k + nt) & 3)) * (D * 2) + 128 * nt;
            asm volatile("" : "+s"(oso[k]));
          }
        }
        if (tt > 0 && i < 4) lds_st<v4f>(pw + (tt - 1) * PBUF + i * 64, acc[(tt - 1) & 1][i]);
        if (tt > 1 && i == 0) pread(tt - 2);
        if (tt > 1 && (i == 2 || i == 3)) psum(tt - 2, i - 2);
          if (n < 4) a_step(n + 2);
        if (n == CHK_N) check_a();
        if (i == 6 && tt > 0) { WAITL0(); BAR(); }
        if (n > 0) asm volatile("" :: "v"(hf[n - 1]));
        SB0();
      }
      if (tt == 0) STAMP(9);
      if (tt == 1) STAMP(16);
      if (tt == 2) STAMP(17);
    }
    const u32 pw2 = pw - LDS_P;
#pragma unroll
    for (int c = 0; c < 4; ++c) {
      if (c == 0) PRIO(3);
      if (c == 1) PRIO(2);
      if (c == 2) PRIO(1);
      if (c == 3) PRIO(0);
#pragma unroll
      for (int i = 0; i < 7; ++i) {
        acc[1][c] = __builtin_amdgcn_mfma_f32_16x16x32_bf16(as_frag(wfr[4 * i + c]), as_frag(hf[21 + i]),
                                                            i == 0 ? (v4f){0.f, 0.f, 0.f, 0.f} : acc[1][c], 0, 0, 0);
        SB0();
        if (c == 0 && i == 0) pread(1);
        if (i == 2) lds_st<v4f>(pw2 + c * 64, acc[0][c]);
        if (i == 4 && c > 0) lds_st<v4f>(pw + (c - 1) * 64, acc[1][c - 1]);
        if (c == 1 && i == 0) psum(1, 0);
        if (c == 1 && i == 5) psum(1, 1);
        if (CHK_N < 21) {
          if (c == 0 && i == 3) fetch_a();
          if (c == 1 && i == 2) mix(0);
          if (c == 2 && i == 2) mix(1);
        } else if (c == 3 && i == 0) check_a();
        SB0();
      }
    }
    STAMP(18);
    lds_st<v4f>(pw + 3 * 64, acc[1][3]);
    WAITL0(); BAR();
    STAMP(10);
    if (CHK_N >= 21) fetch_a();
    pread(2);
    v2f pq[8];
#pragma unroll
    for (int k = 0; k < 8; ++k) pq[k] = lds_ld<v2f>(prd + k * PWAVE);
    if (CHK_N >= 21) { mix(0); mix(1); }
    psum(2, 0); psum(2, 1);
    mix(2);
    bs[3][0] = add2x2s(pq[0], pq[1], pq[2], pq[3]); bs[3][1] = add2x2s(pq[4], pq[5], pq[6], pq[7]);
    mix(3);
  }
}

__global__ void mark_k(char* ws) { __hip_atomic_store((GAS u64*)(ws + 512), __builtin_amdgcn_s_memrealtime(), RLX_AGENT); }
__global__ void spin_k(char* ws) {
  const u64 t0 = __hip_atomic_load((GAS u64*)(ws + 512), RLX_AGENT);
  for (int i = 0; i < 2000000 && __builtin_amdgcn_s_memrealtime() - t0 < 6000; ++i) __builtin_amdgcn_s_sleep(1);
}
extern "C" void kernel_launch(void* const* d_in, const int* in_sizes, int n_in, void* d_out, int out_size, void* d_ws,
                              size_t ws_size, hipStream_t stream) {
  Args a;
  for (int i = 0; i < 8; ++i) a.par[i] = (const u16*)d_in[1 + i];
  a.u = (const u16*)d_in[0];
  a.wg = (const u16*)d_in[10]; a.wu = (const u16*)d_in[11]; a.wd = (const u16*)d_in[12];
  a.gw = (const u16*)d_in[13];
  a.out = (u16*)d_out;
  a.ws = (char*)d_ws;
  (void)hipFuncSetAttribute((const void*)gated_mix_kernel, hipFuncAttributeMaxDynamicSharedMemorySize, LDS_TOTAL);
  mark_k<<<1,64,0,stream>>>((char*)d_ws);
  gated_mix_kernel<<<NMAIN + NHELP, 512, LDS_TOTAL, stream>>>(a);
  spin_k<<<1,64,0,stream>>>((char*)d_ws);
}
```

```cpp
#include <hip/hip_runtime.h>

typedef unsigned short u16;
typedef unsigned u32;
typedef unsigned long long u64;
typedef u32 v4u __attribute__((ext_vector_type(4)));
typedef u32 v2u __attribute__((ext_vector_type(2)));
typedef float v2f __attribute__((ext_vector_type(2)));
typedef float v4f __attribute__((ext_vector_type(4)));
typedef float v16f __attribute__((ext_vector_type(16)));
typedef __bf16 v8bf __attribute__((ext_vector_type(8)));
typedef __bf16 v2bf __attribute__((ext_vector_type(2)));
typedef short v4s __attribute__((ext_vector_type(4)));
#define LAS __attribute__((address_space(3)))
#define GAS __attribute__((address_space(1)))
typedef GAS u32 gu32;
typedef __amdgpu_buffer_rsrc_t rsrc_t;

#define AFFINE 0

constexpr int L = 2048, D = 448, NS = 256, F = 1792;
constexpr int NMAIN = 224, NHELP = 32;

constexpr u32 WS_EPOCH = 0;
constexpr u32 WS_FLAG_W = 1024;
constexpr u32 WS_FLAG_U = 2048;
constexpr u32 WS_GATE = 245760;
#define HF_P 0
#define LDA_N 20
#define AST_N 19
#define HL_P 8
#define CHK_N 99
#define AHD 9
#define HL_D 2
#define PAR_FIRST 1
#define HDELAY 16
#define ROTV 1
#define WROT 0
#define LW_G 16
#define LW_E 0
#define LW_L 0
#define WPF 1
#define WPD 40
#define CLU 8
#define HFS 32
constexpr u32 WS_FLAG_H = 16384;
constexpr u32 WS_UT = 262144;
constexpr u32 WS_A2 = WS_UT + D * L * 2;
constexpr u32 WS_H = WS_A2 + D * L * 4;
constexpr u32 WS_WT = WS_H + L * F * 2;
constexpr u32 WS_END = WS_WT + F * D * 2;
constexpr u32 WS_STAMP = 16u << 20;
constexpr u32 TAG_STEP = 0x9E3779B9u;

constexpr u32 LDS_X = 0;
constexpr u32 LDS_W = 32768;
constexpr u32 LDS_SCR = 131072;
constexpr u32 LDS_TOTAL = 163840;
constexpr u32 LDS_PAR = LDS_W + 2 * 32768 + 16384;
constexpr u32 LDS_WST = LDS_W + 2 * 32768;
constexpr u32 KCOPY = 4160, KCH = 4 * KCOPY;
constexpr u32 LDS_KIMG0 = LDS_W + 32768, LDS_KIMG1 = LDS_SCR + 4096;
constexpr u32 UCH = 512 + 4096;
constexpr u32 LDS_UIMG = LDS_KIMG0 + KCH;
constexpr u32 LDS_TAB = 91648;
constexpr u32 LDS_Y = 0;
constexpr u32 PROW = 272, PWAVE = 16 * PROW, PBUF = 8 * PWAVE;
constexpr u32 LDS_P = 91648;
constexpr u32 LDS_AST = 65536;

struct ThreadTab { u32 v[512][8]; };
constexpr int round_bf16(int s) {
  const int sh = s >= 1024 ? 3 : s >= 512 ? 2 : s >= 256 ? 1 : 0;
  return sh ? ((s + (1 << (sh - 1)) - 1 + ((s >> sh) & 1)) >> sh) << sh : s;
}
constexpr int tab_pos(int s) {
  if (s < 0) return 2 * 512 + 32 * 8;
  const int v = round_bf16(s);
  int x = 0, row = 0, j = 0;
  if (v < 256) { j = v >> 3; row = v & 7; }
  else if (v < 512) { j = (v - 256) >> 3; row = 8 + ((v & 7) >> 1); }
  else if (v < 1024) { j = (v & 255) >> 3; row = 12 + 2 * ((v - 512) >> 8) + ((v & 7) >> 2); }
  else { x = 1; j = (v & 255) >> 3; row = (v - 1024) >> 8; }
  return (2 * x + (j >> 4)) * 512 + ((j & 15) + 16 * (row >> 2)) * 8 + (row & 3) * 2;
}
constexpr ThreadTab make_tab() {
  ThreadTab t{};
  for (int tid = 0; tid < 512; ++tid) {
    const int l = tid & 63, w = tid >> 6, wt = w & 1, wf = w >> 1, G = l >> 4, q = (l & 15) >> 2, p = l & 3;
    u32 e[16] = {};
    for (int j = 0; j < 11; ++j) e[j] = (tid >> 8) * 2048 + tab_pos(2047 - 8 * (tid & 255) - j);
    e[11] = ((tid & 127) < 16 ? ((tid & 8) ? LDS_KIMG1 : LDS_KIMG0) + ((tid >> 1) & 3) * KCOPY + 4096 + (tid & 1) * 16
                              : LDS_UIMG + ((tid >> 5) & 1) * UCH + (tid & 31) * 16) >> 4;
    e[12] = (LDS_UIMG + ((tid >> 2) & 1) * UCH + 512 + (tid >> 3) * 64 + (tid & 3) * 16) >> 4;
    e[14] = LDS_X + (8 * wt + ((l & 15) >> 3)) * 1024 + (l & 7) * 128 + ((G ^ (l & 7)) * 16);
    e[15] = LDS_W + (8 * G + q) * 256 + (((4 * wf + (p >> 1)) ^ (2 * q + 8 * (G & 1))) * 16) + 8 * (p & 1);
    for (int k = 0; k < 8; ++k) t.v[tid][k] = e[2 * k] | (e[2 * k + 1] << 16);
  }
  return t;
}
static __device__ const ThreadTab thread_tab = make_tab();

#define SB0() __builtin_amdgcn_sched_barrier(0)
#define WAITV(n) asm volatile("s_waitcnt vmcnt(" #n ")" ::: "memory")
#define WAITL0() asm volatile("s_waitcnt lgkmcnt(0)" ::: "memory")
#define BAR() __builtin_amdgcn_s_barrier()
#define PRIO(n) __builtin_amdgcn_s_setprio(n)
#define RLX_AGENT __ATOMIC_RELAXED, __HIP_MEMORY_SCOPE_AGENT
constexpr int CODE_KB = 16;
constexpr int SPIN_CAP = 1 << 18;

#define STAMP(i) do { } while (0)
#define WST(i) do { } while (0)
#define WSC(i) do { } while (0)

extern __shared__ __attribute__((aligned(16))) char smem[];

__device__ __forceinline__ float bf_lo(u32 x) { return __uint_as_float(x << 16); }
__device__ __forceinline__ float bf_hi(u32 x) { return __uint_as_float(x & 0xffff0000u); }
__device__ __forceinline__ u32 pk_bf(float lo, float hi) {
  v2f v = {lo, hi};
  return __builtin_bit_cast(u32, __builtin_convertvector(v, v2bf));
}
__device__ __forceinline__ float dot2bf(u32 a, u32 b, float c) {
  return __builtin_amdgcn_fdot2_f32_bf16(__builtin_bit_cast(v2bf, a), __builtin_bit_cast(v2bf, b), c, false);
}
__device__ __forceinline__ float rbf(float x) { return bf_lo(pk_bf(x, x)); }
template <class T> __device__ __forceinline__ T lds_ld(u32 off) { return *(const LAS T*)((LAS char*)smem + off); }
template <class T> __device__ __forceinline__ void lds_st(u32 off, T v) { *(LAS T*)((LAS char*)smem + off) = v; }
__device__ __forceinline__ v2u lds_tr(u32 off) {
  return __builtin_bit_cast(v2u, __builtin_amdgcn_ds_read_tr16_b64_v4i16((LAS v4s*)((LAS char*)smem + off)));
}
__device__ __forceinline__ v8bf as_frag(v4u x) { return __builtin_bit_cast(v8bf, x); }
__device__ __forceinline__ v8bf as_frag(v2u a, v2u b) { v4u x = {a.x, a.y, b.x, b.y}; return __builtin_bit_cast(v8bf, x); }
__device__ __forceinline__ rsrc_t mk_rsrc(const void* p, u32 bytes) {
  return __builtin_amdgcn_make_buffer_rsrc((void*)p, (short)0, (int)bytes, 0x00020000);
}
template <int AUX> __device__ __forceinline__ void st16(v4u v, rsrc_t r, u32 voff) {
  __builtin_amdgcn_raw_buffer_store_b128(v, r, voff, 0, AUX);
}
__device__ __forceinline__ void dma16(rsrc_t r, u32 voff, u32 soff, u32 lds) {
  asm volatile("s_mov_b32 m0, %0\n\ts_nop 0\n\tbuffer_load_dwordx4 %1, %2, %3 offen lds"
               :: "s"(lds), "v"(voff), "s"(r), "s"(soff) : "memory");
}
template <int LO, int SO> __device__ __forceinline__ void dma16i(rsrc_t r, u32 voff, u32 soff, u32 lds) {
  u32 t;
  asm volatile("s_add_u32 m0, %1, %5\n\ts_add_u32 %0, %4, %6\n\tbuffer_load_dwordx4 %2, %3, %0 offen lds"
               : "=&s"(t) : "s"(lds), "v"(voff), "s"(r), "s"(soff), "i"(LO), "i"(SO) : "memory", "scc");
}
__device__ __forceinline__ void dma16_56(rsrc_t r, u32 voff, u32 soff, u32 lds) {
  asm volatile("s_mov_b32 m0, %0\n\ts_mov_b32 exec_hi, 0xffffff\n\tbuffer_load_dwordx4 %1, %2, %3 offen lds\n\ts_mov_b32 exec_hi, -1"
               :: "s"(lds), "v"(voff), "s"(r), "s"(soff) : "memory");
}
__device__ __forceinline__ void dma4_sc1(rsrc_t r, u32 voff, u32 soff, u32 lds) {
  asm volatile("s_mov_b32 m0, %0\n\ts_nop 0\n\tbuffer_load_dword %1, %2, %3 offen sc1 lds"
               :: "s"(lds), "v"(voff), "s"(r), "s"(soff) : "memory");
}
typedef v2f cpx;
#define CM_A(x, y, ax, bx, by) "v_mul_f32 " x ", " ax ", " bx "\n\tv_mul_f32 " y ", " ax ", " by "\n\t"
#define CM_B(x, y, ay, bx, nb) "v_fmac_f32 " x ", " ay ", " nb "\n\tv_fmac_f32 " y ", " ay ", " bx "\n\t"
__device__ __forceinline__ void cmul4(cpx& r0, cpx& r1, cpx& r2, cpx& r3, cpx a0, cpx a1, cpx a2, cpx a3, cpx b, float nb) {
  float x0, y0, x1, y1, x2, y2, x3, y3;
  asm volatile(CM_A("%0", "%1", "%8", "%16", "%17") CM_A("%2", "%3", "%10", "%16", "%17") CM_A("%4", "%5", "%12", "%16", "%17")
               CM_A("%6", "%7", "%14", "%16", "%17") CM_B("%0", "%1", "%9", "%16", "%18") CM_B("%2", "%3", "%11", "%16", "%18")
               CM_B("%4", "%5", "%13", "%16", "%18") CM_B("%6", "%7", "%15", "%16", "%18")
               : "=&v"(x0), "=&v"(y0), "=&v"(x1), "=&v"(y1), "=&v"(x2), "=&v"(y2), "=&v"(x3), "=&v"(y3)
               : "v"(a0.x), "v"(a0.y), "v"(a1.x), "v"(a1.y), "v"(a2.x), "v"(a2.y), "v"(a3.x), "v"(a3.y), "v"(b.x), "v"(b.y), "v"(nb));
  r0 = (cpx){x0, y0}; r1 = (cpx){x1, y1}; r2 = (cpx){x2, y2}; r3 = (cpx){x3, y3};
}
__device__ __forceinline__ void cmul2(cpx& r0, cpx& r1, cpx a0, cpx b0, float n0, cpx a1, cpx b1, float n1) {
  float x0, y0, x1, y1;
  asm volatile(CM_A("%0", "%1", "%4", "%6", "%7") CM_A("%2", "%3", "%9", "%11", "%12")
               CM_B("%0", "%1", "%5", "%6", "%8") CM_B("%2", "%3", "%10", "%11", "%13")
               : "=&v"(x0), "=&v"(y0), "=&v"(x1), "=&v"(y1)
               : "v"(a0.x), "v"(a0.y), "v"(b0.x), "v"(b0.y), "v"(n0), "v"(a1.x), "v"(a1.y), "v"(b1.x), "v"(b1.y), "v"(n1));
  r0 = (cpx){x0, y0}; r1 = (cpx){x1, y1};
}
__device__ __forceinline__ v2f add2x2s(v2f a, v2f b, v2f c, v2f d) {
  float x, y, z, t;
  asm("v_add_f32 %0, %4, %6\n\tv_add_f32 %1, %5, %7\n\tv_add_f32 %2, %8, %10\n\tv_add_f32 %3, %9, %11\n\t"
      "v_add_f32 %0, %0, %2\n\tv_add_f32 %1, %1, %3"
      : "=&v"(x), "=&v"(y), "=&v"(z), "=&v"(t)
      : "v"(a.x), "v"(a.y), "v"(b.x), "v"(b.y), "v"(c.x), "v"(c.y), "v"(d.x), "v"(d.y));
  return (v2f){x, y};
}
__device__ __forceinline__ u32 mix2(float g, u32 a, v2f b0, v2f b1) {
  float x, y, lo, hi;
  u32 o;
  asm("v_add_f32 %1, %7, %9\n\tv_add_f32 %2, %8, %10\n\tv_lshlrev_b32 %3, 16, %6\n\tv_and_b32 %4, 0xffff0000, %6\n\t"
      "v_sub_f32 %3, %3, %1\n\tv_sub_f32 %4, %4, %2\n\tv_fmac_f32 %1, %5, %3\n\tv_fmac_f32 %2, %5, %4\n\t"
      "s_nop 0\n\tv_cvt_pk_bf16_f32 %0, %1, %2"
      : "=&v"(o), "=&v"(x), "=&v"(y), "=&v"(lo), "=&v"(hi)
      : "v"(g), "v"(a), "v"(b0.x), "v"(b0.y), "v"(b1.x), "v"(b1.y));
  return o;
}
__device__ __forceinline__ v4f add4s(v4f a, v4f b) {
  float x, y, z, t;
  asm("v_add_f32 %0, %4, %8\n\tv_add_f32 %1, %5, %9\n\tv_add_f32 %2, %6, %10\n\tv_add_f32 %3, %7, %11"
      : "=&v"(x), "=&v"(y), "=&v"(z), "=&v"(t)
      : "v"(a.x), "v"(a.y), "v"(a.z), "v"(a.w), "v"(b.x), "v"(b.y), "v"(b.z), "v"(b.w));
  return (v4f){x, y, z, t};
}
constexpr int frow(int r) {
  return r < 7 ? LDS_W + 32768 + r * 288 : r < 13 ? LDS_W + 49152 + 224 + (r - 7) * 288 : LDS_W + 65536 + 1024 + 160 + (r - 13) * 288;
}
constexpr int FM0 = 49152;
constexpr float RND_UP = 1.0027f;
template <int R> __device__ __forceinline__ void rows4(v4u& keep, u32 m0v, cpx a, cpx b, cpx c, cpx d) {
  u32 t0, t1, t2, t3;
  asm volatile("s_mov_b32 m0, %4\n\tv_perm_b32 %0, %6, %5, %21\n\tv_perm_b32 %1, %8, %7, %21\n\tv_perm_b32 %2, %10, %9, %21\n\t"
               "v_perm_b32 %3, %12, %11, %21\n\tds_write_addtid_b32 %0 offset:%17\n\tds_write_addtid_b32 %1 offset:%18\n\t"
               "ds_write_addtid_b32 %2 offset:%19\n\tds_write_addtid_b32 %3 offset:%20"
               : "=&v"(t0), "=&v"(t1), "=&v"(t2), "=&v"(t3)
               : "s"(m0v), "v"(a.x), "v"(a.y), "v"(b.x), "v"(b.y), "v"(c.x), "v"(c.y), "v"(d.x), "v"(d.y),
                 "v"(keep.x), "v"(keep.y), "v"(keep.z), "v"(keep.w),
                 "i"(frow(R) - FM0), "i"(frow(R + 1) - FM0), "i"(frow(R + 2) - FM0), "i"(frow(R + 3) - FM0), "s"(0x07060302u)
               : "memory");
  keep = (v4u){t0, t1, t2, t3};
}
template <int H> __device__ __forceinline__ u32 add_half(u32 base, u32 x) {
  u32 r;
  if constexpr (H == 0) asm volatile("v_add_u32_sdwa %0, %1, %2 dst_sel:DWORD dst_unused:UNUSED_PAD src0_sel:DWORD src1_sel:WORD_0" : "=v"(r) : "s"(base), "v"(x));
  else asm volatile("v_add_u32_sdwa %0, %1, %2 dst_sel:DWORD dst_unused:UNUSED_PAD src0_sel:DWORD src1_sel:WORD_1" : "=v"(r) : "s"(base), "v"(x));
  return r;
}
__device__ __forceinline__ void flag_store(rsrc_t r, u32 soff, u32 tag_v) {
  asm volatile("s_mov_b64 exec, 1\n\tbuffer_store_dword %0, off, %1, %2 sc1\n\ts_mov_b64 exec, -1" :: "v"(tag_v), "s"(r), "s"(soff) : "memory");
}
__device__ __forceinline__ u32 sopaque(u32 x) { asm volatile("" : "+s"(x)); return x; }
__device__ __forceinline__ u32 opaque(u32 x) { asm volatile("" : "+v"(x)); return x; }
__device__ __forceinline__ cpx cpow_l(float a2, float bf, float l) {
  float m = __builtin_amdgcn_exp2f(a2 * l), ph = __builtin_amdgcn_fractf(bf * l);
  return (cpx){m * __builtin_amdgcn_cosf(ph), m * __builtin_amdgcn_sinf(ph)};
}
__device__ __forceinline__ void wait_flags(const gu32* p, u32 tag, u32 first, int code = 0, int nap = 1) {
  if (__all(first == tag)) return;
  u32 v;
#pragma unroll 1
  for (int s = 0; s < SPIN_CAP; ++s) {
    v = __hip_atomic_load(p, RLX_AGENT);
    if (__all(v == tag)) break;
    if (nap == 1) __builtin_amdgcn_s_sleep(1); else __builtin_amdgcn_s_sleep(8);
  }
}

template <int V> struct ic { static constexpr int value = V; };
struct Args {
  const u16* par[8];
  const u16 *u, *wg, *wu, *wd, *gw;
  u16* out;
  char* ws;
};

__global__ __launch_bounds__(512, 2) void gated_mix_kernel(Args) {
  const int b = blockIdx.x, xcd = b & 7, bi = b >> 3;
  const int tid = threadIdx.x, l = tid & 63;
  const int w = __builtin_amdgcn_readfirstlane(tid >> 6);
  typedef u64 v4q __attribute__((ext_vector_type(4)));
  typedef u64 v2q __attribute__((ext_vector_type(2)));
  v4q ka0;
  v2q ka1;
  u64 ka2, par_w;
  asm volatile("s_load_dwordx8 %0, %4, 0x40\n\ts_load_dwordx4 %1, %4, 0x60\n\ts_load_dwordx2 %2, %4, 0x70\n\ts_load_dwordx2 %3, %4, %5"
               : "=&s"(ka0), "=&s"(ka1), "=&s"(ka2), "=&s"(par_w) : "s"(__builtin_amdgcn_kernarg_segment_ptr()), "s"(w * 8) : "memory");
  asm volatile("s_cmp_ge_u32 %0, %3\n\ts_cbranch_scc1 1f\n\ts_getpc_b64 s[4:5]\n\ts_mov_b32 m0, %2\n\ts_and_b32 s4, s4, 0xffffff00\n\t"
               "global_load_lds_dwordx4 %1, s[4:5]\n1:"
               :: "s"(bi * 8 + w), "v"((bi * 8 + w) * 1024 + l * 16), "s"(LDS_SCR + w * 4096), "i"(CODE_KB) : "s4", "s5", "scc", "memory");
  const int cb = xcd * 28 + bi;
  const int mt = (xcd & 3) * 4 + (bi & 3), ft = (xcd >> 2) * 7 + (bi >> 2);
  const int rot = ROTV == 0 ? ((bi >> 2) + 2 * (bi & 3) + (xcd & 3)) % 7
                : ROTV == 1 ? ((xcd & 3) + (xcd >> 2) + 3 * (((bi >> 2) + bi) & 1)) % 7
                : ROTV == 2 ? ((xcd & 3) + (xcd >> 2) + 2 * (((bi >> 2) + (bi & 3)) % 3)) % 7
                            : (2 * (xcd & 3) + (xcd >> 2)) % 7;
  const int m2 = 4 * xcd + (bi & 3), nt = bi >> 2;
  const int wt = w & 1, wf = w >> 1;

  const u32 vl16 = opaque(l * 16);
  const u32 vx0 = opaque((l >> 3) * 896 + (((l & 7) ^ (l >> 3)) * 16)), vx1 = opaque(vx0 + 8 * 896);
  const u32 vw = opaque((l >> 4) * (F * 2) + (((l & 15) ^ (2 * (l >> 4) + 8 * (w & 1))) * 16));
  const u32 sx = sopaque((mt * 128 + 16 * w) * 896 + rot * 128), sw = sopaque(8 * w * (F * 2) + ft * 256 + rot * (64 * F * 2)), lw = sopaque(w * 2048);
  const u32 par_o = sopaque(w < 6 ? cb * 1024 : (cb * 4) & ~15), par_n = sopaque(w < 6 ? D * NS * 2 : D * 2), par_l = sopaque(LDS_PAR + w * 1024);
  asm volatile("s_waitcnt lgkmcnt(0)" : "+s"(ka0), "+s"(ka1), "+s"(ka2), "+s"(par_w) :: "memory");
  struct { const u16 *u, *wg, *wu, *wd, *gw; u16* out; char* ws; } g = {
      (const u16*)ka0.x, (const u16*)ka0.y, (const u16*)ka0.z, (const u16*)ka0.w, (const u16*)ka1.x, (u16*)ka1.y, (char*)ka2};
  char* ws = g.ws;
  const rsrc_t rws = mk_rsrc(ws, WS_END);
  const rsrc_t rx = mk_rsrc(g.u, L * D * 2);
  STAMP(0);

  if (bi >= 28) {
    const int hb = b - NMAIN, t0 = 64 * hb;
    constexpr u32 HX = 0, HG = 64 * 896;
    const rsrc_t rgw = mk_rsrc(g.gw, D * 2);
    if (HDELAY) __builtin_amdgcn_s_sleep(HDELAY);
#pragma unroll
    for (int j = 0; j < 8; ++j) dma16_56(rx, vl16 ^ (((j & 2) + 4 * (w & 1)) * 16), (t0 + 8 * w + j) * 896, HX + (8 * w + j) * 896);
    if (w == 0) { dma4_sc1(rws, 0, WS_EPOCH, HG + 1024); dma16(rgw, vl16, 0, HG); }
    const int G = l >> 4, q = (l & 15) >> 2, p = l & 3;
    const u32 a_u = opaque(HX + (32 * (w & 1) + 8 * G + q) * 896 + (((w & 6) + (p >> 1)) ^ ((q & 2) + 4 * (G & 1))) * 16 + 8 * (p & 1));
    WAITV(0); BAR();
    STAMP(1);
    const u32 tag = __builtin_amdgcn_readfirstlane(lds_ld<u32>(HG + 1024)) + TAG_STEP;
    {
      const u32 o = WS_UT + (hb * 56 + w) * 1024 + (l & 15) * 64 + G * 16;
#pragma unroll
      for (int j = 0; j < 7; ++j) {
        v2u lo = lds_tr(a_u + 128 * j), hi = lds_tr(a_u + 128 * j + 4 * 896);
        st16<16>((v4u){lo.x, lo.y, hi.x, hi.y}, rws, o + j * 8192);
      }
    }
    {
      const int r = 8 * w + (l >> 3);
      const u32 xa = opaque(HX + r * 896 + ((l & 7) ^ (((l >> 3) & 2) + 4 * (w & 1))) * 16), ga = opaque(HG + (l & 7) * 16);
      float s0 = 0.f, s1 = 0.f;
#pragma unroll
      for (int j = 0; j < 7; ++j) {
        const v4u xv = lds_ld<v4u>(xa + 128 * j), gv = lds_ld<v4u>(ga + 128 * j);
#pragma unroll
        for (int e = 0; e < 4; e += 2) {
          s0 = dot2bf(xv[e], gv[e], s0);
          s1 = dot2bf(xv[e + 1], gv[e + 1], s1);
        }
      }
      float s = s0 + s1;
      s += __shfl_xor(s, 1); s += __shfl_xor(s, 2); s += __shfl_xor(s, 4);
      s = rbf(s);
      float gt = rbf(__builtin_amdgcn_rcpf(1.f + __builtin_amdgcn_exp2f(-1.44269504f * s)));
      if ((l & 7) < 2) __hip_atomic_store((gu32*)(ws + WS_GATE) + hb * 128 + (r & 15) * 8 + (r >> 4) + 4 * (l & 1), __float_as_uint(gt), RLX_AGENT);
    }
    WAITV(0); BAR();
    if (tid == 0) __hip_atomic_store((gu32*)(ws + WS_FLAG_U) + hb, tag, RLX_AGENT);
    STAMP(2);
    if (WPD) __builtin_amdgcn_s_sleep(WPD);
#pragma unroll 1
    for (int i = 0; i < 21 * WPF; ++i) {
      const int id = (hb >> 3) * 8 + w + 32 * (i % 21), unit = id >> 2, n7 = unit / 24, r = unit - 24 * n7, k8 = r / 3;
      int k = r - 3 * k8 + (n7 < 4 ? 4 : 1) - (i >= 21 ? 3 : 0);
      k = k < 0 ? k + 7 : k;
      dma16(rws, vl16, __builtin_amdgcn_readfirstlane(WS_WT + ((n7 * 56 + k8 * 7 + k) * 4 + (id & 3)) * 1024), w * 1024);
    }
    WAITV(0);
    if (hb == NHELP - 1) {
      if (w < 4) wait_flags((const gu32*)(ws + (tid < NMAIN ? WS_FLAG_W : WS_FLAG_U - NMAIN * 4)) + tid, tag, ~tag, 1, 8);
      BAR();
      if (tid == 0) __hip_atomic_store((gu32*)(ws + WS_EPOCH), tag, RLX_AGENT);
    }
    return;
  }

  {
    const rsrc_t rp = mk_rsrc((const void*)par_w, par_n);
    dma16(rp, vl16, par_o, par_l);
    if (w == 0) dma4_sc1(rws, 0, WS_EPOCH, LDS_PAR + 8192);
  }
  const rsrc_t rwg = mk_rsrc(g.wg, D * F * 2), rwu = mk_rsrc(g.wu, D * F * 2);
  auto piece = [&](auto S, auto I) {
    constexpr int s = decltype(S)::value, i = decltype(I)::value;
    const bool wrap = s + rot >= 7;
    if constexpr (i < 2) dma16i<LDS_X + (s & 1) * 16384 + i * 1024, s * 128>(rx, i ? vx1 : vx0, wrap ? sx - 7 * 128 : sx, lw);
    else dma16i<LDS_W + (s % 3) * 32768 + (i >= 4) * 16384 + (i & 1) * 1024, (64 * s + 4 * (i & 1)) * F * 2>(i < 4 ? rwg : rwu, vw, wrap ? sw - 7 * 64 * F * 2 : sw, lw);
  };
#define PIECE(s, i) piece(ic<(s)>{}, ic<(i)>{})
  const int ct = 7 * (cb & 3) + (w < 7 ? w : 6);
  const int G = l >> 4, q = (l & 15) >> 2, p = l & 3;
  const u32 srd = opaque(lw + ((l & 15) < 7 ? frow(0) : (l & 15) < 13 ? frow(7) - 7 * 288 : frow(13) - 13 * 288) + (l & 15) * 288 + (l >> 4) * 16);
  const u32 po = opaque(LDS_PAR + (w >> 2) * 512 + (w & 3) * 128 + l * 2);
  const u32 pld = opaque(LDS_PAR + 6144 + ((cb * 4) & 15) + 2 * (w >> 2)), pdk = opaque(LDS_PAR + 7168 + ((cb * 4) & 15));
  const u32 wst_v = opaque((l >> 1) * 896 + (l & 1) * 16), tab_v = opaque(tid * 32);
  const u32 wst_a = opaque(LDS_WST + lw + (4 * G + q) * 32 + p * 8);
  const u32 wt_o = opaque(WS_WT + (((ct >> 2) * 56 + (cb >> 2)) * 4 + (ct & 3)) * 1024 + vl16);
  const u32 kpw = opaque(LDS_SCR + w * 4096 + vl16), kpr = opaque(LDS_SCR + (w & 4) * 4096 + (w & 3) * 1024 + vl16);
  const u32 wfo = opaque(WS_FLAG_W + 16 * (l < 56 ? l : 0));
  const u32 ufo = opaque(WS_FLAG_U + (l & 31) * 4);
  PIECE(0, 0); PIECE(0, 1); PIECE(0, 2); PIECE(0, 3);
  WAITV(4); BAR();
  PRIO(3);
  STAMP(1);

  auto frags = [&](v8bf (&f)[4]) {
#pragma unroll
    for (int k = 0; k < 4; ++k) f[k] = as_frag(lds_ld<v4u>(srd + k * 64));
  };
  u32 tag, dsk2;
  v4u tb0, tb1;
  {
    cpx c0, w1, w2, w256, w512;
    const u32 fm0 = lw + FM0;
    v8bf J[2][4], Hf[4], Hg[4];
    v4u keep = {0, 0, 0, 0};
    const float ld = bf_lo(lds_ld<u16>(pld));
    const u32 q0 = lds_ld<u16>(po), q1 = lds_ld<u16>(po + 1024), q2 = lds_ld<u16>(po + 2048), q3 = lds_ld<u16>(po + 3072);
    const u32 q4 = lds_ld<u16>(po + 4096), q5 = lds_ld<u16>(po + 5120);
    const u32 ep = lds_ld<u32>(pdk + 1024), dk = lds_ld<u32>(pdk);
    PIECE(0, 4); PIECE(0, 5);
    const float dt = rbf(__builtin_amdgcn_exp2f(ld * 1.44269504f));
    const float a2 = (dt * bf_lo(q0)) * 1.44269504f, bf = (dt * bf_lo(q1)) * 0.15915494f;
    const cpx w8 = cpow_l(a2, bf, 8.f);
    w1 = cpow_l(a2, -bf, 1.f); w256 = cpow_l(a2, -bf, 256.f);
    const cpx cc = {bf_lo(q4), -bf_lo(q5)}, bb = {(dt * RND_UP) * bf_lo(q2), -(dt * RND_UP) * bf_lo(q3)};
    tag = __builtin_amdgcn_readfirstlane(ep) + TAG_STEP; dsk2 = __builtin_amdgcn_readfirstlane(dk);
    cpx w16, w24, w32, j1, j2, j3;
    cmul2(w16, c0, w8, w8, -w8.y, cc, bb, -bb.y);
    cmul2(w24, w32, w16, w8, -w8.y, w16, w16, -w16.y);
    cpx j0 = {RND_UP, 0.f};
    j1 = w8 * RND_UP; j2 = w16 * RND_UP; j3 = w24 * RND_UP;
    const float n32 = -w32.y, n1 = -w1.y, n256 = -w256.y;
#define JSTEP(k) rows4<((k) & 3) * 4>(keep, fm0, j0, j1, j2, j3)
#define JNEXT() cmul4(j0, j1, j2, j3, j0, j1, j2, j3, w32, n32)
    JSTEP(0); JNEXT();
    JSTEP(1); JNEXT();
    PRIO(2);
    dma16(mk_rsrc(g.wd, F * D * 2), wst_v, 32 * (cb >> 2) * 896 + ct * 32, LDS_WST + lw);
    JSTEP(2); JNEXT();
    JSTEP(3); frags(J[0]); JNEXT();
    JSTEP(4); JNEXT();
    JSTEP(5); JNEXT();
    JSTEP(6); JNEXT();
    cmul2(w2, w512, w1, w1, n1, w256, w256, n256);
    PRIO(1);
    asm volatile("global_load_dwordx4 %0, %2, %3\n\tglobal_load_dwordx4 %1, %2, %3 offset:16"
                 : "=&v"(tb0), "=&v"(tb1) : "v"(tab_v), "s"(&thread_tab.v[0][0]) : "memory");
    JSTEP(7); frags(J[1]);
    cpx c1, c2, c3, c4, c5, c6, c7, w4, d0, d2, d4, d6, e0, e4, f0, f4, g0, g1, g2, g3, g4;
    const float n2 = -w2.y, n512 = -w512.y;
    cmul2(c1, c2, c0, w1, n1, c0, w2, n2);
    cmul2(c3, w4, c2, w1, n1, w2, w2, n2);
    cmul4(c4, c5, c6, c7, c0, c1, c2, c3, w4, -w4.y);
    rows4<0>(keep, fm0, c0, c1, c2, c3);
    cmul4(d0, d2, d4, d6, c0, c2, c4, c6, w256, n256);
    rows4<4>(keep, fm0, c4, c5, c6, c7);
    cmul4(e0, e4, f0, f4, c0, c4, d0, d4, w512, n512);
    rows4<8>(keep, fm0, d0, d2, d4, d6);
    cmul2(g0, g1, e0, w512, n512, f0, w512, n512);
    PRIO(0);
    rows4<12>(keep, fm0, e0, e4, f0, f4);
    frags(Hf);
    cmul2(g2, g3, g0, w512, n512, g1, w512, n512);
    g4 = (cpx){g2.x * w512.x + g2.y * n512, g2.x * w512.y + g2.y * w512.x};
    rows4<0>(keep, fm0, g0, g1, g2, g3);
    asm volatile("ds_write_addtid_b32 %0 offset:%1" :: "v"(__builtin_amdgcn_perm(__float_as_uint(g4.y), __float_as_uint(g4.x), 0x07060302u)),
                 "i"(frow(4) - FM0) : "memory");
    frags(Hg);
    SB0();
    v4f kc[4];
    auto kmm = [&](int k) {
#pragma unroll
      for (int t = 0; t < 4; ++t)
        kc[t] = __builtin_amdgcn_mfma_f32_16x16x32_bf16((t >> 1) ? Hg[k] : Hf[k], J[t & 1][k], k == 0 ? (v4f){0.f, 0.f, 0.f, 0.f} : kc[t], 0, 0, 0);
      SB0();
    };
    WAITL0();
    kmm(0); PIECE(1, 0); kmm(1); PIECE(1, 1); kmm(2); PIECE(1, 2); kmm(3); PIECE(1, 3);
#pragma unroll
    for (int t = 0; t < 4; ++t) lds_st<v4f>(kpw + t * 1024, kc[t]);
    SB0();
    PIECE(1, 4); PIECE(1, 5);
  }
  STAMP(13);
  asm volatile("s_waitcnt vmcnt(6)" : "+v"(tb0), "+v"(tb1) :: "memory");
  u32 xa[2], wa[3][2];
  xa[0] = opaque(tb1.w & 0xffff); xa[1] = opaque(xa[0] ^ 64);
  wa[0][0] = opaque(tb1.w >> 16); wa[0][1] = opaque(wa[0][0] ^ 32);
#pragma unroll
  for (int k = 1; k < 3; ++k) { wa[k][0] = opaque(wa[0][0] + 32768 * k); wa[k][1] = opaque(wa[0][1] + 32768 * k); }
  const u32 tag_v = opaque(tag);
  STAMP(2);

  v4f accg[4][2] = {}, accu[4][2] = {};
  v4u ut4, gate4;
  v4u wfr[28];
  const int kr = w, krot = WROT == 1 ? (4 * (nt >> 2) + 2 * (bi & 3)) % 7 : WROT == 2 ? (4 * (nt >> 2) + 3 * (bi & 1)) % 7 : nt & 4;
  u32 koff[7];
#pragma unroll
  for (int i = 0; i < 7; ++i) koff[i] = (kr * 7 + (i + krot >= 7 ? i + krot - 7 : i + krot)) * 1024;
  const u32 wd_so = WS_WT + nt * 56 * 4096;
  auto ldw = [&](int i) { wfr[i] = __builtin_amdgcn_raw_buffer_load_b128(rws, vl16 + (i & 3) * 1024, wd_so + 4 * koff[i >> 2], 16); };
  u32 hfo, hadr[4], ka, ka8, ub, yb, yo, uo, xe[2][5], xkb, xz, uia;
  auto img_writes = [&]() {
#pragma unroll
    for (int o = 0; o < 2; ++o) {
      lds_st<v4u>(xkb + (3 - o) * KCOPY, (v4u){xe[o][0], xe[o][1], xe[o][2], xe[o][3]});
      lds_st<v4u>(xkb + (1 - o) * KCOPY, (v4u){xe[o][1], xe[o][2], xe[o][3], xe[o][4]});
    }
    lds_st<v4u>(xz, (v4u){0, 0, 0, 0});
    lds_st<v4u>(uia, ut4);
  };
  v2f nl2e = {-1.44269504f, -1.44269504f};
  v4u hst[4];
  auto silu8 = [&](int gI) {
    v2f gv[4], pv[4], ev[4];
#pragma unroll
    for (int e = 0; e < 4; ++e) { gv[e] = (v2f){accg[gI][e >> 1][2 * (e & 1)], accg[gI][e >> 1][2 * (e & 1) + 1]}; ev[e] = gv[e] * nl2e; }
    SB0();
#pragma unroll
    for (int e = 0; e < 4; ++e) ev[e] = (v2f){__builtin_amdgcn_exp2f(ev[e].x), __builtin_amdgcn_exp2f(ev[e].y)};
    SB0();
#pragma unroll
    for (int e = 0; e < 4; ++e) { ev[e] = ev[e] + 1.f; pv[e] = gv[e] * (v2f){accu[gI][e >> 1][2 * (e & 1)], accu[gI][e >> 1][2 * (e & 1) + 1]}; }
    SB0();
#pragma unroll
    for (int e = 0; e < 4; ++e) ev[e] = (v2f){__builtin_amdgcn_rcpf(ev[e].x), __builtin_amdgcn_rcpf(ev[e].y)};
    SB0();
#pragma unroll
    for (int e = 0; e < 4; ++e) pv[e] = pv[e] * ev[e];
    SB0();
#pragma unroll
    for (int e = 0; e < 4; ++e) hst[gI][e] = pk_bf(pv[e].x, pv[e].y);
    st16<16>(hst[gI], rws, hadr[gI]);
    if (gI > 0) asm volatile("" :: "v"(hst[gI - 1]));
    SB0();
  };
  {
    v8bf fw[2][4], fx[2][2];
    auto rdw = [&](int st, int kh, int m) {
#pragma unroll
      for (int j = 0; j < 2; ++j) {
        const u32 a = wa[st % 3][j] + kh * 8192 + m * 16384;
        fw[kh][2 * m + j] = as_frag(lds_tr(a), lds_tr(a + 1024));
      }
    };
    auto rdx = [&](int st, int ks) {
      const u32 a = xa[ks >> 1] + (st & 1) * 16384 + (ks & 1) * 4096;
      fx[ks & 1][0] = as_frag(lds_ld<v4u>(a)); fx[ks & 1][1] = as_frag(lds_ld<v4u>(a + 2048));
    };
    auto rd = [&](int st, int ks) {
      if (ks == 0) { rdw(st, 0, 0); rdx(st, 0); rdw(st, 0, 1); }
      if (ks == 1) { rdx(st, 1); rdw(st, 1, 0); }
      if (ks == 2) { rdx(st, 2); rdw(st, 1, 1); }
      if (ks == 3) rdx(st, 3);
    };
    v4f ks4[4];
    u32 kp01, kp23;
    auto kgen = [&](int n) {
      if (n == 22) {
#pragma unroll
        for (int k = 0; k < 4; ++k) ks4[k] = lds_ld<v4f>(kpr + k * 4096);
      }
      if (n == 24) {
        const v4f s = add4s(add4s(ks4[0], ks4[1]), add4s(ks4[2], ks4[3]));
        const bool keep = !(w & 2) || l < 32;
        kp01 = opaque(keep ? pk_bf(s[0], s[1]) : 0); kp23 = opaque(keep ? pk_bf(s[2], s[3]) : 0);
      }
      if (n == 25) lds_st<v2u>(LDS_TAB + w * 512 + (vl16 >> 1), (v2u){kp01, kp23});
    };
    u32 xv[11], uta;
    auto expand = [&](int step) {
      if (step == 0) {
        xv[0] = lds_ld<u16>(add_half<0>(LDS_TAB, tb0.x)); xv[1] = lds_ld<u16>(add_half<1>(LDS_TAB, tb0.x));
        xv[2] = lds_ld<u16>(add_half<0>(LDS_TAB, tb0.y)); xv[3] = lds_ld<u16>(add_half<1>(LDS_TAB, tb0.y));
      } else if (step == 1) {
        xv[4] = lds_ld<u16>(add_half<0>(LDS_TAB, tb0.z)); xv[5] = lds_ld<u16>(add_half<1>(LDS_TAB, tb0.z));
        xv[6] = lds_ld<u16>(add_half<0>(LDS_TAB, tb0.w)); xv[7] = lds_ld<u16>(add_half<1>(LDS_TAB, tb0.w));
      } else if (step == 2) {
        xv[8] = lds_ld<u16>(add_half<0>(LDS_TAB, tb1.x)); xv[9] = lds_ld<u16>(add_half<1>(LDS_TAB, tb1.x));
        xv[10] = lds_ld<u16>(add_half<0>(LDS_TAB, tb1.y));
      } else if (step < 16) {
        const int o = step - 14;
#pragma unroll
        for (int j = 0; j < 5; ++j) xe[o][j] = opaque(xv[2 * j + o] | (xv[2 * j + o + 1] << 16));
      }
    };
    v4u wflag;
    u32 uflag;
    auto side = [&](auto S, auto KS, auto H) {
      constexpr int s = decltype(S)::value, ks = decltype(KS)::value, h = decltype(H)::value;
      constexpr int st = 4 * s + ks;
      if constexpr (h == 0 || h == 2) {
        constexpr int i = h >> 1;
        if constexpr (ks == 3) { if constexpr (s < 5) PIECE(s + 2, i); }
        else if constexpr (s == 0) { if constexpr (ks < 2) PIECE(2, 2 + 2 * ks + i); }
        else if constexpr (ks < 2 && s + 2 < 7) PIECE(s + 2, 2 + 2 * ks + i);
        if constexpr (st >= 20) { if constexpr (LW_G == 16) ldw(2 * (st - 20) + i); else if constexpr (LW_G == 12) { if constexpr (i == 0 || (st & 1)) ldw(3 * ((st - 20) >> 1) + (st & 1) + i); } else if constexpr (i == 0) ldw(st - 20); }
      } else {
        constexpr int v = 2 * st + (h >> 1);
        if constexpr (v == 1) kgen(22);
        if constexpr (v == 4) kgen(24);
        if constexpr (v == 28) hfo = opaque(WS_FLAG_H + (m2 * 56 + 7 * kr + (l < 7 ? l : 0)) * (HFS * 4));
        if constexpr (v == 43) kgen(25);
        if constexpr (v >= 46 && v < 49) expand(v - 46);
        if constexpr (v == 52 || v == 53) expand(v - 38);
        if constexpr (v == 14) {
          xkb = opaque(((w >> 2) ? LDS_KIMG1 : LDS_KIMG0) + (tid & 255) * 16);
          xz = opaque((tb1.y >> 16) << 4);
          uia = opaque((tb1.z & 0xffff) << 4);
        }
        if constexpr (v == 16) uta = opaque((tid >> 4) * (56 * 1024) + ((tid >> 3) & 1) * 1024 + (tid & 7) * 16);
        if constexpr (v == 18) {
#pragma unroll
          for (int k = 0; k < 4; ++k) hadr[k] = opaque(WS_H + ((mt * 8 + 4 * wt + k) * 56 + ft * 4 + wf) * 1024 + vl16);
          asm volatile("" : "+v"(nl2e));
        }
        if constexpr (v == 20) {
          ka = opaque(((w >> 2) ? LDS_KIMG1 : LDS_KIMG0) + (l & 3) * KCOPY + 2 * (2028 + 8 * G - 4 * q) - 512 * 7 - 128 * (w & 3) - 64);
          ka8 = opaque(ka + 8);
        }
        if constexpr (v == 22) ub = opaque(LDS_UIMG + (w >> 2) * UCH + 2 * (240 + 16 * (l & 15) + 8 * G) - 128 * (w & 3) - 64);
        if constexpr (v == 24) yb = opaque(LDS_Y + ((w & 3) * 2 + (w >> 2)) * 8192 + (16 * (l & 15) + 4 * (G ^ ((l >> 1) & 3))) * 4);
        if constexpr (v == 26) { yo = opaque(LDS_Y + (tid >> 2) * 64 + (((tid & 3) ^ ((tid >> 3) & 3)) * 16)); uo = opaque(LDS_UIMG + 512 + tid * 8); }
      }
    };
    auto mm = [&](auto S, auto KS) {
      constexpr int ks = decltype(KS)::value, kh = ks >> 1, t0 = 2 * (ks & 1);
#define MM2(acc, m, t) \
      acc[t0 + t][0] = __builtin_amdgcn_mfma_f32_16x16x32_bf16(fw[kh][2 * m], fx[ks & 1][t], acc[t0 + t][0], 0, 0, 0); \
      acc[t0 + t][1] = __builtin_amdgcn_mfma_f32_16x16x32_bf16(fw[kh][2 * m + 1], fx[ks & 1][t], acc[t0 + t][1], 0, 0, 0);
      MM2(accg, 0, 0) MM2(accg, 0, 1) MM2(accu, 1, 0) MM2(accu, 1, 1) SB0();
      side(S, KS, ic<0>{}); side(S, KS, ic<1>{}); side(S, KS, ic<2>{}); side(S, KS, ic<3>{});
      asm volatile("" :: "v"(fw[kh][0]), "v"(fw[kh][1]), "v"(fw[kh][2]), "v"(fw[kh][3]), "v"(fx[ks & 1][0]), "v"(fx[ks & 1][1]));
      SB0();
    };
    auto stage = [&](auto S) {
      constexpr int s = decltype(S)::value;
      PRIO(2); rd(s, 1); SB0(); mm(S, ic<0>{});
      PRIO(1); rd(s, 2); SB0(); mm(S, ic<1>{});
      PRIO(0); rd(s, 3); SB0(); mm(S, ic<2>{});
      PRIO(3);
      if constexpr (s < 6) {
        WAITL0();
        if constexpr (s == 0) WAITV(5);
        else if constexpr (s < 4) WAITV(4);
        else if constexpr (s == 4) {
          asm volatile("s_waitcnt vmcnt(4)" : "+v"(wflag) :: "memory");
          if (w == 0 && !__all(((wflag.x ^ tag_v) | (wflag.y ^ tag_v) | (wflag.z ^ tag_v) | (wflag.w ^ tag_v)) == 0)) {
#pragma unroll 1
            for (int sp = 0; sp < SPIN_CAP; ++sp) {
              asm volatile("" ::: "memory");
              const v4u f = __builtin_amdgcn_raw_buffer_load_b128(rws, wfo, 0, 16);
              if (__all(f.x == tag && f.y == tag && f.z == tag && f.w == tag)) break;
            }
          }
        } else {
          asm volatile("s_waitcnt vmcnt(%1)" : "+v"(uflag) : "i"(LW_G == 16 ? 6 : LW_G == 12 ? 4 : 3) : "memory");
          if (w == 0) wait_flags((const gu32*)(ws + ufo), tag, uflag, 2);
        }
        BAR();
        rd(s + 1, 0);
        if constexpr (s == 1) { if (w == 0) flag_store(rws, WS_FLAG_W + cb * 4, tag_v); }
        if constexpr (s == 3)
          asm volatile("s_cmp_lg_u32 %3, 0\n\ts_cbranch_scc1 1f\n\tbuffer_load_dwordx4 %0, %1, %2, 0 offen sc1\n1:"
                       : "=v"(wflag) : "v"(wfo), "s"(rws), "s"(w) : "memory", "scc");
        if constexpr (s == 4)
          asm volatile("s_cmp_lg_u32 %3, 0\n\ts_cbranch_scc1 1f\n\tbuffer_load_dword %0, %1, %2, 0 offen sc1\n1:"
                       : "=v"(uflag) : "v"(ufo), "s"(rws), "s"(w) : "memory", "scc");
        if constexpr (s >= 4) __builtin_amdgcn_fence(__ATOMIC_ACQUIRE, "wavefront");
        if constexpr (s == 5) {
          ut4 = __builtin_amdgcn_raw_buffer_load_b128(rws, uta, WS_UT + (cb >> 3) * 2048 + (cb & 7) * 128, 16);
          gate4 = __builtin_amdgcn_raw_buffer_load_b128(rws, (tid >> 5) * 32, WS_GATE + m2 * 512 + (nt & 3) * 4, 16);
        }
        STAMP(s < 5 ? 22 + s : 20);
        if constexpr (s == 5) WSC(0);
        SB0();
      }
      mm(S, ic<3>{});
    };
    {
      const v2u lo = lds_tr(wst_a), hi = lds_tr(wst_a + 512);
      st16<16>((v4u){lo.x, lo.y, hi.x, hi.y}, rws, wt_o);
    }
    WAITL0(); BAR();
    STAMP(3);
    rd(0, 0);
    SB0();
    stage(ic<0>{}); stage(ic<1>{}); stage(ic<2>{}); stage(ic<3>{}); stage(ic<4>{}); stage(ic<5>{}); stage(ic<6>{});
  }
  STAMP(4);
  WSC(1);
  u32 hflag;
  v4u hf[28];
  u32 hso[4];
#pragma unroll
  for (int tt = 0; tt < 4; ++tt) hso[tt] = WS_H + (m2 * 4 + ((tt + nt) & 3)) * 56 * 1024;
  auto ldh = [&](int n) {
    const int i = n % 7;
    hf[n] = __builtin_amdgcn_raw_buffer_load_b128(rws, vl16, hso[n / 7] + koff[i], 16);
    SB0();
  };
  constexpr int AHEAD = AHD, LW_C = LW_G + LW_E * (LW_L ? 4 : 3);
  static_assert(LW_C >= 16);
  {
    v4f acc[8];
    v8bf A[8][2], B[8][2];
    auto lda_ = [&](int pp) {
#pragma unroll
      for (int qq = 0; qq < 2; ++qq)
        A[pp][qq] = as_frag(lds_ld<v2u>(ka + 64 - 64 * qq + 512 * (7 - pp)), lds_ld<v2u>(ka8 + 64 - 64 * qq + 512 * (7 - pp)));
    };
#pragma unroll
    for (int gI = 0; gI < 4; ++gI) {
      silu8(gI);
#pragma unroll
      for (int k = 0; k < LW_E; ++k) if (gI < 3 || LW_L) ldw(LW_G + LW_E * gI + k);
      if (gI == 3) img_writes();
      SB0();
    }
    WAITL0(); BAR();
    lda_(0);
#pragma unroll
    for (int e = 7; e >= 0; --e) B[e][0] = as_frag(lds_ld<v4u>(ub + 64 + 512 * e));
#pragma unroll
    for (int e = 7; e >= 0; --e) B[e][1] = as_frag(lds_ld<v4u>(ub + 512 * e));
    lda_(1);
    asm volatile("" :: "v"(hst[3]));
    asm volatile("s_waitcnt vmcnt(%0)" :: "i"(LW_L ? LW_E : 0) : "memory");
    flag_store(rws, WS_FLAG_H + ((2 * mt + wt) * 56 + 4 * ft + wf) * (HFS * 4), tag_v);
    WST(1);
    SB0();
    PRIO(2);
    asm volatile("" ::: "memory");
    STAMP(5);
    WST(0);
    WSC(2);
#pragma unroll
    for (int pp = 0; pp < 8; ++pp) {
#pragma unroll
      for (int qq = 0; qq < 2; ++qq) {
#pragma unroll
        for (int c = 7; c >= pp; --c) {
          acc[c] = __builtin_amdgcn_mfma_f32_16x16x32_bf16(A[pp][qq], B[c - pp][qq], (qq == 0 && pp == 0) ? (v4f){0.f, 0.f, 0.f, 0.f} : acc[c], 0, 0, 0);
          if (c == 5 && pp < 3 && LW_C + 4 * pp + 2 * qq < 28) { SB0(); ldw(LW_C + 4 * pp + 2 * qq); SB0(); }
        }
        SB0();
        if (qq == 0) { if (pp < 6) lda_(pp + 2); if (pp > 0) lds_st<v4f>(yb + (pp - 1) * 1024, acc[pp - 1]); }
        if (pp < 3 && LW_C + 1 + 4 * pp + 2 * qq < 28) ldw(LW_C + 1 + 4 * pp + 2 * qq);
        const int hs = 2 * pp + qq;
        if (hs == HL_P - HL_D) hflag = __builtin_amdgcn_raw_buffer_load_b32(rws, hfo, 0, 16);
        if (hs == HL_P) {
          WSC(5);
          STAMP(7);
          WST(2);
          wait_flags((const gu32*)(ws + hfo), tag, hflag, 3);
          __builtin_amdgcn_fence(__ATOMIC_ACQUIRE, "wavefront");
          STAMP(8);
          WST(3);
        }
        if (hs >= HL_P && 2 * (hs - HL_P) < 8) { ldh(2 * (hs - HL_P)); ldh(2 * (hs - HL_P) + 1); }
        if (qq == 1 && pp == 1) PRIO(1);
        if (qq == 1 && pp == 3) PRIO(0);
        SB0();
      }
    }
    lds_st<v4f>(yb + 7 * 1024, acc[7]);
  }
  WSC(6);

  {
    v4u a_out;
    v4f yv[8], ys[2];
    v2u uv[2];
    auto a_step = [&](int k) {
      if (k < 2) {
        uv[k] = lds_ld<v2u>(uo + k * UCH);
#pragma unroll
        for (int z = 0; z < 4; ++z) yv[4 * k + z] = lds_ld<v4f>(yo + k * 8192 + z * 16384);
      } else if (k < 4) {
        ys[k - 2] = add4s(add4s(yv[4 * k - 8], yv[4 * k - 7]), add4s(yv[4 * k - 6], yv[4 * k - 5]));
      } else {
        const int e = k - 4;
        const float k0 = bf_lo(dsk2), k1 = bf_hi(dsk2);
        a_out[2 * e] = opaque(pk_bf(__builtin_fmaf(bf_lo(uv[0][e]), k0, ys[0][2 * e]), __builtin_fmaf(bf_lo(uv[1][e]), k1, ys[1][2 * e])));
        a_out[2 * e + 1] = opaque(pk_bf(__builtin_fmaf(bf_hi(uv[0][e]), k0, ys[0][2 * e + 1]), __builtin_fmaf(bf_hi(uv[1][e]), k1, ys[1][2 * e + 1])));
      }
    };
    auto sta = [&]() {
      const u32 ao = vl16 + WS_A2 + cb * 16384 + w * 2048;
      st16<16>((v4u){a_out.x, tag, a_out.y, tag}, rws, ao);
      st16<16>((v4u){a_out.z, tag, a_out.w, tag}, rws, ao + 1024);
      SB0();
    };
#pragma unroll
    for (int n = 8; n < AHEAD - 1; ++n) ldh(n);
    STAMP(27);
    WST(4);
    WAITL0(); BAR();
    a_step(0); a_step(1);
    SB0();
    STAMP(12);
    WSC(7);
    v4u a4[2];
    u32 a_vo, pw, prd, asw, asr, oad;
    const u32 a_so = WS_A2 + 32 * nt * 16384 + (m2 >> 2) * 2048 + (m2 & 3) * 256;
    auto lda = [&]() {
      a4[0] = __builtin_amdgcn_raw_buffer_load_b128(rws, a_vo, a_so, 16);
      a4[1] = __builtin_amdgcn_raw_buffer_load_b128(rws, a_vo + 1024, a_so, 16);
    };
    auto check_a = [&]() {
          if (!__all(((a4[0].y ^ tag) | (a4[0].w ^ tag) | (a4[1].y ^ tag) | (a4[1].w ^ tag)) == 0)) {
#pragma unroll 1
            for (int sp = 0; sp < SPIN_CAP; ++sp) {
              asm volatile("" ::: "memory");
              lda();
              if (__all(a4[0].y == tag && a4[0].w == tag && a4[1].y == tag && a4[1].w == tag)) break;
            }
          }
          lds_st<v2u>(asw, (v2u){a4[0].x, a4[0].z}); lds_st<v2u>(asw + 8, (v2u){a4[1].x, a4[1].z});
        };
    v4f acc[2][4];
    v2f pr[8], bs[4][2];
    u32 av[4], oso[4], prd2;
    const rsrc_t ro = mk_rsrc(g.out, L * D * 2);
    auto pread = [&](int tt) {
#pragma unroll
      for (int k = 0; k < 8; ++k) pr[k] = lds_ld<v2f>((tt == 2 ? prd2 : prd + (tt == 1) * PBUF) + k * PWAVE);
    };
    auto psum = [&](int tt, int h) { bs[tt][h] = add2x2s(pr[4 * h], pr[4 * h + 1], pr[4 * h + 2], pr[4 * h + 3]); };
    auto fetch_a = [&]() {
#pragma unroll
      for (int e = 0; e < 4; ++e) av[e] = lds_ld<u32>(asr + e * 64);
    };
    auto mix = [&](int tt) {
      __builtin_amdgcn_raw_buffer_store_b32(mix2(__uint_as_float(gate4[tt]), av[tt], bs[tt][0], bs[tt][1]), ro, oad, oso[tt], 16);
    };
#pragma unroll
    for (int tt = 0; tt < 3; ++tt) {
#pragma unroll
      for (int i = 0; i < 7; ++i) {
        const int n = 7 * tt + i;
        if (i == 0) PRIO(3);
        if (i == 2) PRIO(2);
        if (i == 4) PRIO(1);
        if (i == 6) PRIO(0);
        if (n + AHEAD - 1 < 28) ldh(n + AHEAD - 1);
          if (n == 4) sta();
        if (n == LDA_N) lda();
        SB0();
#pragma unroll
        for (int c = 0; c < 4; ++c)
          acc[tt & 1][c] = __builtin_amdgcn_mfma_f32_16x16x32_bf16(as_frag(wfr[4 * i + c]), as_frag(hf[n]),
                                                                   i == 0 ? (v4f){0.f, 0.f, 0.f, 0.f} : acc[tt & 1][c], 0, 0, 0);
        SB0();
        if (n == 0) { STAMP(29); WST(5); }
        if (n == 5) pw = opaque(LDS_P + w * PWAVE + (l & 15) * PROW + G * 16);
        if (n == 6) { prd = opaque(LDS_P + (tid >> 5) * PROW + (tid & 31) * 8); prd2 = opaque(prd - LDS_P); }
        if (n == 8) a_vo = opaque((tid >> 4) * 16384 + (tid & 15) * 16);
        if (n == 9) asw = opaque(LDS_AST + (tid >> 4) * 272 + ((tid - 4 * nt) & 15) * 16);
        if (n == 10) asr = opaque(LDS_AST + (tid & 31) * 272 + (tid >> 5) * 4);
        if (n == 11) oad = opaque((tid >> 5) * (D * 2) + (tid & 31) * 4);
        if (n == 12) {
#pragma unroll
          for (int k = 0; k < 4; ++k) {
            oso[k] = (64 * m2 + 16 * ((k + nt) & 3)) * (D * 2) + 128 * nt;
            asm volatile("" : "+s"(oso[k]));
          }
        }
        if (tt > 0 && i < 4) lds_st<v4f>(pw + (tt - 1) * PBUF + i * 64, acc[(tt - 1) & 1][i]);
        if (tt > 1 && i == 0) pread(tt - 2);
        if (tt > 1 && (i == 2 || i == 3)) psum(tt - 2, i - 2);
          if (n < 4) a_step(n + 2);
        if (n == CHK_N) check_a();
        if (i == 6 && tt > 0) { WAITL0(); BAR(); }
        if (n > 0) asm volatile("" :: "v"(hf[n - 1]));
        SB0();
      }
      if (tt == 0) STAMP(9);
      if (tt == 1) STAMP(16);
      if (tt == 2) STAMP(17);
    }
    const u32 pw2 = pw - LDS_P;
#pragma unroll
    for (int c = 0; c < 4; ++c) {
      if (c == 0) PRIO(3);
      if (c == 1) PRIO(2);
      if (c == 2) PRIO(1);
      if (c == 3) PRIO(0);
#pragma unroll
      for (int i = 0; i < 7; ++i) {
        acc[1][c] = __builtin_amdgcn_mfma_f32_16x16x32_bf16(as_frag(wfr[4 * i + c]), as_frag(hf[21 + i]),
                                                            i == 0 ? (v4f){0.f, 0.f, 0.f, 0.f} : acc[1][c], 0, 0, 0);
        SB0();
        if (c == 0 && i == 0) pread(1);
        if (i == 2) lds_st<v4f>(pw2 + c * 64, acc[0][c]);
        if (i == 4 && c > 0) lds_st<v4f>(pw + (c - 1) * 64, acc[1][c - 1]);
        if (c == 1 && i == 0) psum(1, 0);
        if (c == 1 && i == 5) psum(1, 1);
        if (CHK_N < 21) {
          if (c == 0 && i == 3) fetch_a();
          if (c == 1 && i == 2) mix(0);
          if (c == 2 && i == 2) mix(1);
        } else if (c == 3 && i == 0) check_a();
        SB0();
      }
    }
    STAMP(18);
    lds_st<v4f>(pw + 3 * 64, acc[1][3]);
    WAITL0(); BAR();
    STAMP(10);
    if (CHK_N >= 21) fetch_a();
    pread(2);
    v2f pq[8];
#pragma unroll
    for (int k = 0; k < 8; ++k) pq[k] = lds_ld<v2f>(prd + k * PWAVE);
    if (CHK_N >= 21) { mix(0); mix(1); }
    psum(2, 0); psum(2, 1);
    mix(2);
    bs[3][0] = add2x2s(pq[0], pq[1], pq[2], pq[3]); bs[3][1] = add2x2s(pq[4], pq[5], pq[6], pq[7]);
    mix(3);
  }
}

extern "C" void kernel_launch(void* const* d_in, const int* in_sizes, int n_in, void* d_out, int out_size, void* d_ws,
                              size_t ws_size, hipStream_t stream) {
  Args a;
  for (int i = 0; i < 8; ++i) a.par[i] = (const u16*)d_in[1 + i];
  a.u = (const u16*)d_in[0];
  a.wg = (const u16*)d_in[10]; a.wu = (const u16*)d_in[11]; a.wd = (const u16*)d_in[12];
  a.gw = (const u16*)d_in[13];
  a.out = (u16*)d_out;
  a.ws = (char*)d_ws;
  (void)hipFuncSetAttribute((const void*)gated_mix_kernel, hipFuncAttributeMaxDynamicSharedMemorySize, LDS_TOTAL);
  gated_mix_kernel<<<NMAIN + NHELP, 512, LDS_TOTAL, stream>>>(a);
}
```
